```python
import torch

def _softmax(x, axis=-1):
    return torch.softmax(x, dim=axis)
def _gelu(x):
    return torch.nn.functional.gelu(x, approximate="tanh")
def _silu(x):
    return torch.nn.functional.silu(x)
def _sigmoid(x):
    return torch.sigmoid(x)
def _layernorm(x, axis=-1, eps=1e-5):
    m = x.mean(dim=axis, keepdim=True)
    v = x.var(dim=axis, keepdim=True, unbiased=False)
    return (x - m) / torch.sqrt(v + eps)
def _scatter_add(tgt, idx, src, axis=0):
    return tgt.clone().index_add_(axis, idx, src)
def _segment_sum(src, seg, n):
    out = torch.zeros((n,) + src.shape[1:], dtype=src.dtype)
    return out.index_add_(0, seg, src)
def _pad(x, pw):
    flat = tuple(v for pair in reversed(pw) for v in pair)
    return torch.nn.functional.pad(x, flat)

kernel_name = 'recurrent_scan__sliding_block_cached__glu_gated_swiglu__decode'

def setup_inputs():

    _w = (lambda a: (lambda t: t.to(torch.bfloat16) if t.is_floating_point() else t)(torch.as_tensor(a)))
    def _A_setup():
        B, Sq, D = 1, 1, 640
        Hq, Hkv, Sk, Dh = 32, 32, 32768, 192
        assert Hq % Hkv == 0, (Hq, Hkv)
        return {
            "x": _w((lambda *s: torch.randn(*s, dtype=torch.bfloat16) * 0.05)(B, Sq, D)),
            "k_cache": _w((lambda *s: torch.randn(*s, dtype=torch.bfloat16) * 3.2)(B, Hkv, Sk, Dh)),
            "v_cache": _w((lambda *s: torch.randn(*s, dtype=torch.bfloat16) * 3.2)(B, Hkv, Sk, Dh)),
            "wq": _w((lambda *s: torch.randn(*s, dtype=torch.bfloat16) * 3.2)(D, Hq * Dh) * (D ** -0.5)),
            "wo": _w((lambda *s: torch.randn(*s, dtype=torch.bfloat16) * 3.2)(Hq * Dh, D) * ((Hq * Dh) ** -0.5)),
        }
    _A_in = _A_setup()

    def _B_setup():
        B, S, D, F = 1, 1, 640, 1536
        return {
            "x": _w((lambda *s: torch.randn(*s, dtype=torch.bfloat16) * 0.05)(B, S, D)),
            "w1": _w((lambda *s: torch.randn(*s, dtype=torch.bfloat16) * 0.4)(D, F) * (2.0 / D) ** 0.5),
            "w3": _w((lambda *s: torch.randn(*s, dtype=torch.bfloat16) * 0.4)(D, F) * (2.0 / D) ** 0.5),
            "w2": _w((lambda *s: torch.randn(*s, dtype=torch.bfloat16) * 0.4)(F, D) * (2.0 / F) ** 0.5),
        }
    _B_in = _B_setup()

    return {
        "A_x": _A_in["x"],
        "A_k_cache": _A_in["k_cache"],
        "A_v_cache": _A_in["v_cache"],
        "A_wq": _A_in["wq"],
        "A_wo": _A_in["wo"],
        "B_x": _B_in["x"],
        "B_w1": _B_in["w1"],
        "B_w3": _B_in["w3"],
        "B_w2": _B_in["w2"],
    }

def reference(A_x, A_k_cache, A_v_cache, A_wq, A_wo, B_x, B_w1, B_w3, B_w2):
    def _A_ref(x, k_cache, v_cache, wq, wo):
        B, Sq, D = x.shape
        Hq, Dh = 32, 192
        Hkv, Sk = k_cache.shape[1], k_cache.shape[2]
        W = min(4096, max(256, Sk // 4))
        rep = Hq // Hkv
        q = (lambda x, *d: x.permute(*d))((x @ wq).reshape(B, Sq, Hq, Dh), 0, 2, 1, 3)
        k = (lambda x, n, axis: torch.repeat_interleave(x, n, dim=axis))(k_cache[:, :, Sk - W:, :], rep, axis=1)
        v = (lambda x, n, axis: torch.repeat_interleave(x, n, dim=axis))(v_cache[:, :, Sk - W:, :], rep, axis=1)
        scores = torch.einsum("bhqd,bhkd->bhqk", q, k) * (Dh ** -0.5)
        p = _softmax(scores, axis=-1)
        o = torch.einsum("bhqk,bhkd->bhqd", p, v)
        return (lambda x, *d: x.permute(*d))(o, 0, 2, 1, 3).reshape(B, Sq, Hq * Dh) @ wo

    def _B_ref(x, w1, w3, w2):
        gate = torch.einsum("bsd,df->bsf", x, w1)
        up = torch.einsum("bsd,df->bsf", x, w3)
        h = _silu(gate) * up
        out = torch.einsum("bsf,fd->bsd", h, w2)
        return out

    x = A_x
    xn = _layernorm(x, axis=-1)
    s = _A_ref(xn, A_k_cache, A_v_cache, A_wq, A_wo)
    g = _silu(_B_ref(xn, B_w1, B_w3, B_w2))
    return x + s * g
```

```cpp
#include <hip/hip_runtime.h>
#include <hip/hip_bf16.h>
#include <stdint.h>

typedef __hip_bfloat16 bf16;
typedef unsigned long long u64;
typedef unsigned u32x4 __attribute__((ext_vector_type(4)));
typedef unsigned u32x8 __attribute__((ext_vector_type(8)));
typedef float f32x4 __attribute__((ext_vector_type(4)));
typedef float f32x2 __attribute__((ext_vector_type(2)));
typedef __bf16 bf16x2_t __attribute__((ext_vector_type(2)));

namespace {

constexpr int D = 640, HQ = 32, DH = 192, SK = 32768, WIN = 4096, FF = 1536;
constexpr int NSP = 8;
constexpr int NWG = HQ * NSP;
constexpr int NT = 512;
constexpr float SCALE = 0.07216878364870322f;
constexpr float LOG2E = 1.4426950408889634f;
constexpr unsigned SPIN_MAX = 1u << 16;
constexpr int SC1 = 16;
constexpr int NOWHERE = 1 << 30;

#define KNOB(n, v) constexpr int n = v
#define PS 8
#define PL 2
#define PV_ 81
#define PK_ 82
#define PK2 82
#define TS_ 358
#define SB_ 246
#define PT_ 215
#define FT_ 0
#define WT_ PV_
#define NFL 7
#define CDE 0
#define CDL 2
#define AR 3
#define GR 5
#define GRW 2
#define RSLEEP 1
#define MW 4
#define MRD 2
#define MAP_ 1
#define UTHR 16
#define UMISS 60
#define USH 4
#define UNOM 90
#define MNB 1
#define ULIM 20
#define PVL 2
#define PA 1
#define MAO -2
#define MRDA 2
#define ATHR 26
#define ASH 4
#define AGX 7
#define AG0 6
#define AG1 8
#define LOC 30
#define FA 1
#define ALIM 20
#define UDF 0
#define UGX 8
#define HROT 16
constexpr int VR = 8 + PL, MR = PS + PL - 1;
constexpr bool is_v(int r) { return r >= VR || (r >= PS && r < PS + PL); }
constexpr int tile_of(int r) { return r < PS ? r : r < PS + PL ? r - PS : r < VR ? r - PL : r - 8; }
constexpr int t_round(int r) { return r <= 2 ? TS_ : t_round(r - 1) + (is_v(r - 1) ? PV_ : r - 1 < PS ? PK_ : PK2); }
constexpr int T_LAST = t_round(15);
constexpr int t_late(int i) { return t_round(i); }
constexpr int t_early(int i) { return t_round(i); }

constexpr int QS = 144;
constexpr int YB = 2 * (16 * 32 + 16);
constexpr int RS = 144;
constexpr size_t GQ = 0;
constexpr size_t G2 = GQ + (size_t)NWG * QS;
constexpr size_t GY = G2 + (size_t)NWG * RS;
constexpr size_t GZ = GY + 16;
constexpr size_t GEP = GY + (size_t)NSP * 5 * YB;
constexpr size_t GL = GEP + 16;

__device__ __forceinline__ unsigned fbits(float x) { return __builtin_bit_cast(unsigned, x); }
__device__ __forceinline__ u64 gld(const u64* p) {
  return __hip_atomic_load(p, __ATOMIC_RELAXED, __HIP_MEMORY_SCOPE_AGENT);
}
__device__ __forceinline__ float bf_lo(unsigned u) { return __builtin_bit_cast(float, u << 16); }
__device__ __forceinline__ float bf_hi(unsigned u) { return __builtin_bit_cast(float, u & 0xffff0000u); }
__device__ __forceinline__ float bfr(float x) { return __bfloat162float(__float2bfloat16(x)); }
__device__ __forceinline__ unsigned short bfbits(float x) {
  return __builtin_bit_cast(unsigned short, __float2bfloat16(x));
}
__device__ __forceinline__ float dot2(unsigned a, unsigned b, float c) {
  return __builtin_amdgcn_fdot2_f32_bf16(__builtin_bit_cast(bf16x2_t, a), __builtin_bit_cast(bf16x2_t, b), c, false);
}
template <int CTRL>
__device__ __forceinline__ float dpp(float x) {
  return __builtin_bit_cast(float, __builtin_amdgcn_mov_dpp(__builtin_bit_cast(int, x), CTRL, 0xf, 0xf, true));
}
__device__ __forceinline__ float grp8_sum(float x) {
  x += dpp<0xB1>(x);
  x += dpp<0x4E>(x);
  x += dpp<0x141>(x);
  return x;
}
__device__ __forceinline__ float sw16(float a, float b) {
  auto s = __builtin_amdgcn_permlane16_swap(fbits(a), fbits(b), false, false);
  return __builtin_bit_cast(float, (unsigned)s[0]) + __builtin_bit_cast(float, (unsigned)s[1]);
}
__device__ __forceinline__ float sw32(float a, float b) {
  auto s = __builtin_amdgcn_permlane32_swap(fbits(a), fbits(b), false, false);
  return __builtin_bit_cast(float, (unsigned)s[0]) + __builtin_bit_cast(float, (unsigned)s[1]);
}
__device__ __forceinline__ float xg_sum(float x) {
  x += dpp<0x128>(x);
  x = sw16(x, x);
  return sw32(x, x);
}
__device__ __forceinline__ float xg_max(float x) {
  x = fmaxf(x, dpp<0x128>(x));
  auto s = __builtin_amdgcn_permlane16_swap(fbits(x), fbits(x), false, false);
  x = fmaxf(__builtin_bit_cast(float, (unsigned)s[0]), __builtin_bit_cast(float, (unsigned)s[1]));
  auto t = __builtin_amdgcn_permlane32_swap(fbits(x), fbits(x), false, false);
  return fmaxf(__builtin_bit_cast(float, (unsigned)t[0]), __builtin_bit_cast(float, (unsigned)t[1]));
}
__device__ __forceinline__ float wave_sum(float x) { return xg_sum(grp8_sum(x)); }
__device__ __forceinline__ float ex2(float x) { return __builtin_amdgcn_exp2f(x); }
__device__ __forceinline__ float silu(float x) { return x / (1.0f + __expf(-x)); }
__device__ __forceinline__ float xg_reduce8(const float (&a)[8], bool g0) {
  float r[4];
#pragma unroll
  for (int i = 0; i < 4; ++i) r[i] = sw32(a[i], a[i + 4]);
  const float s0 = sw16(r[0], r[2]), s1 = sw16(r[1], r[3]);
  const float keep = g0 ? s1 : s0, send = g0 ? s0 : s1;
  return keep + dpp<0x128>(send);
}
__device__ __forceinline__ __amdgpu_buffer_rsrc_t rsrc(const void* p, unsigned bytes) {
  return __builtin_amdgcn_make_buffer_rsrc(const_cast<void*>(p), 0, bytes, 0x00020000);
}
__device__ __forceinline__ void gst16(u64* p, unsigned v0, unsigned v1, unsigned tag) {
  const u32x4 o = {v0, tag, v1, tag};
  asm volatile("global_store_dwordx4 %0, %1, off sc1\n\ts_nop 1" ::"v"(p), "v"(o) : "memory");
}
__device__ __forceinline__ unsigned bfpack(float a, float b) {
  return __builtin_bit_cast(unsigned, __builtin_convertvector(f32x2{a, b}, bf16x2_t));
}
__device__ __forceinline__ float2 rows_reduce(const float (&v)[8]) {
  float r[4];
#pragma unroll
  for (int i = 0; i < 4; ++i) r[i] = sw32(v[i], v[i + 4]);
  return float2{sw16(r[0], r[2]), sw16(r[1], r[3])};
}
template <int OFF>
__device__ __forceinline__ void xg_store(const float (&v)[8], float f, unsigned a) {
  float p[4], q[4], t, u;
#pragma unroll
  for (int i = 0; i < 4; ++i) {
    auto s = __builtin_amdgcn_permlane32_swap(fbits(v[i]), fbits(v[i + 4]), false, false);
    p[i] = __builtin_bit_cast(float, (unsigned)s[0]);
    q[i] = __builtin_bit_cast(float, (unsigned)s[1]);
  }
  const f32x2 r01 = f32x2{p[0], p[1]} + f32x2{q[0], q[1]}, r23 = f32x2{p[2], p[3]} + f32x2{q[2], q[3]};
  auto s0 = __builtin_amdgcn_permlane16_swap(fbits(r01[0]), fbits(r23[0]), false, false);
  auto s1 = __builtin_amdgcn_permlane16_swap(fbits(r01[1]), fbits(r23[1]), false, false);
  asm volatile("v_add_f32 %0, %2, %3\n\tv_add_f32 %1, %4, %5\n\ts_nop 0\n\t"
               "v_add_f32_dpp %0, %0, %0 row_ror:8 row_mask:0xf bank_mask:0x3\n\t"
               "v_add_f32_dpp %0, %1, %1 row_ror:8 row_mask:0xf bank_mask:0xc\n\t"
               "v_mul_f32 %0, %6, %0\n\tds_write_b32 %7, %0 offset:%8"
               : "=&v"(t), "=&v"(u)
               : "v"((unsigned)s0[0]), "v"((unsigned)s0[1]), "v"((unsigned)s1[0]), "v"((unsigned)s1[1]), "v"(f), "v"(a), "n"(OFF)
               : "memory");
}
#define SUM8(a)                                                                                                          \
  "ds_read_b128 v[116:119], " a "\n\tds_read_b128 v[120:123], " a " offset:768\n\t"                                      \
  "ds_read_b128 v[124:127], " a " offset:1536\n\tds_read_b128 v[128:131], " a " offset:2304\n\t"                         \
  "ds_read_b128 v[132:135], " a " offset:3072\n\tds_read_b128 v[136:139], " a " offset:3840\n\t"                         \
  "ds_read_b128 v[140:143], " a " offset:4608\n\tds_read_b128 v[144:147], " a " offset:5376\n\ts_waitcnt lgkmcnt(6)\n\t"
#define SUM8B                                                                                                            \
  "v_pk_add_f32 v[116:117], v[116:117], v[120:121]\n\tv_pk_add_f32 v[118:119], v[118:119], v[122:123]\n\t"               \
  "s_waitcnt lgkmcnt(4)\n\t"                                                                                             \
  "v_pk_add_f32 v[124:125], v[124:125], v[128:129]\n\tv_pk_add_f32 v[126:127], v[126:127], v[130:131]\n\t"               \
  "v_pk_add_f32 v[116:117], v[116:117], v[124:125]\n\tv_pk_add_f32 v[118:119], v[118:119], v[126:127]\n\t"
#define SUM8C                                                                                                            \
  "s_waitcnt lgkmcnt(2)\n\t"                                                                                             \
  "v_pk_add_f32 v[132:133], v[132:133], v[136:137]\n\tv_pk_add_f32 v[134:135], v[134:135], v[138:139]\n\t"               \
  "s_waitcnt lgkmcnt(0)\n\t"                                                                                             \
  "v_pk_add_f32 v[140:141], v[140:141], v[144:145]\n\tv_pk_add_f32 v[142:143], v[142:143], v[146:147]\n\t"               \
  "v_pk_add_f32 v[132:133], v[132:133], v[140:141]\n\tv_pk_add_f32 v[134:135], v[134:135], v[142:143]\n\t"               \
  "v_pk_add_f32 v[116:117], v[116:117], v[132:133]\n\tv_pk_add_f32 v[118:119], v[118:119], v[134:135]\n\t"
#define BC12 " row_newbcast:12 row_mask:0xf bank_mask:0xf\n\t"
#define DOTB(y, a, n) "v_dot2c_f32_bf16_dpp v" #y ", v124, v" #a " row_newbcast:" #n " row_mask:0xf bank_mask:0xf\n\t"
#define DOT8(a, b, c, d, e, f, g, h, n) \
  DOTB(160, a, n) DOTB(161, b, n) DOTB(162, c, n) DOTB(163, d, n) DOTB(164, e, n) DOTB(165, f, n) DOTB(166, g, n) DOTB(167, h, n)
#define DOTF(y, a, n) "v_dot2c_f32_bf16 v" #y ", v" #n ", v" #a "\n\t"
#define DOT8F(a, b, c, d, e, f, g, h, n) \
  DOTF(160, a, n) DOTF(161, b, n) DOTF(162, c, n) DOTF(163, d, n) DOTF(164, e, n) DOTF(165, f, n) DOTF(166, g, n) DOTF(167, h, n)
#define YRED                                                                                                             \
  "v_permlane32_swap_b32 v160, v164\n\tv_permlane32_swap_b32 v161, v165\n\tv_permlane32_swap_b32 v162, v166\n\t"         \
  "v_permlane32_swap_b32 v163, v167\n\tv_pk_add_f32 v[160:161], v[160:161], v[164:165]\n\t"                              \
  "v_pk_add_f32 v[162:163], v[162:163], v[166:167]\n\ts_nop 1\n\t"                                                       \
  "v_permlane16_swap_b32 v160, v162\n\tv_permlane16_swap_b32 v161, v163\n\t"
#define CLOB_T                                                                                                     \
  "v112", "v113", "v114", "v115", "v116", "v117", "v118", "v119", "v120", "v121", "v122", "v123", "v124", "v125",        \
      "v126", "v127", "v128", "v129", "v130", "v131", "v132", "v133", "v134", "v135", "v136", "v137", "v138", "v139",    \
      "v140", "v141", "v142", "v143", "v144", "v145", "v146", "v147"
#define SCHED_FENCE() __builtin_amdgcn_sched_barrier(0)
#define STR2(x) #x
#define STR(x) STR2(x)
#define THRN(n) do { SCHED_FENCE(); asm volatile("s_waitcnt vmcnt(%0)" ::"n"(n) : "memory"); SCHED_FENCE(); } while (0)
#define NOWT() ((unsigned)__builtin_amdgcn_s_memrealtime())
template <int N>
struct IC {
  static constexpr int v = N;
};
template <int I, int E, class F>
__device__ __forceinline__ void sfor(F&& f) {
  if constexpr (I < E) {
    f(IC<I>{});
    sfor<I + 1, E>(f);
  }
}
#define LDS_DRAIN() asm volatile("s_waitcnt lgkmcnt(0)" ::: "memory")
__device__ __forceinline__ unsigned lds_add(float* lds, int idx) {
  return __hip_atomic_fetch_add(reinterpret_cast<unsigned*>(lds + idx), 1u, __ATOMIC_RELAXED, __HIP_MEMORY_SCOPE_WORKGROUP);
}
__device__ __forceinline__ void lds_wait(float* lds, int idx, unsigned n) {
  SCHED_FENCE();
#pragma unroll 1
  for (unsigned spin = 0; spin < (SPIN_MAX << 4); ++spin) {
    if (__hip_atomic_load(reinterpret_cast<unsigned*>(lds + idx), __ATOMIC_RELAXED, __HIP_MEMORY_SCOPE_WORKGROUP) >= n) break;
    __builtin_amdgcn_s_sleep(1);
  }
  asm volatile("" ::: "memory");
  SCHED_FENCE();
}
template <int T, int R>
__device__ __forceinline__ void pace(unsigned clk, int ph) {
  unsigned o;
  SCHED_FENCE();
  asm volatile(
      "s_branch .Lpb%=\n"
      ".Lpa%=:\n\ts_sleep 1\n"
      ".Lpb%=:\n\tds_read_b32 %0, %1 offset:%4\n\ts_memrealtime s[98:99]\n\ts_waitcnt lgkmcnt(0)\n\tv_readfirstlane_b32 s99, %0\n\t"
      "s_sub_u32 s98, s98, %2\n\ts_sub_u32 s98, s98, s99\n\ts_cmp_lt_i32 s98, %3\n\ts_cbranch_scc1 .Lpa%="
      : "=&v"(o) : "v"(clk), "s"(ph), "n"(T), "n"(4 * R) : "s98", "s99", "scc", "memory");
  SCHED_FENCE();
}

#define STAMP(i)
#define STAMP_FLUSH()
#define TT(i)
constexpr int L_QP = 0;
constexpr int L_QS = L_QP + 4 * 128;
constexpr int L_PART = L_QS + 96;
constexpr int L_ML = L_PART + 8 * DH;
constexpr int L_Z = L_ML + 16;
constexpr int L_Y = L_Z + 320;
constexpr int L_H = L_Y + 512;
constexpr int L_SY = L_H + 64;
constexpr int L_CK = L_SY + 8;
constexpr int L_END = L_CK + 16;

#define PIN(x) asm volatile("" : "+v"(x))

__device__ __forceinline__ void wg_ml(const float* lds, float& M, float& L) {
  float mm[8], ll[8];
#pragma unroll
  for (int i = 0; i < 4; ++i) {
    const f32x4 t = *reinterpret_cast<const f32x4*>(lds + L_ML + 4 * i);
    mm[2 * i] = t[0];
    ll[2 * i] = t[1];
    mm[2 * i + 1] = t[2];
    ll[2 * i + 1] = t[3];
  }
  M = mm[0];
#pragma unroll
  for (int ww = 1; ww < 8; ++ww) M = fmaxf(M, mm[ww]);
  L = 0.f;
#pragma unroll
  for (int ww = 0; ww < 8; ++ww) L = fmaf(ex2((mm[ww] - M) * LOG2E), ll[ww], L);
}
__device__ __forceinline__ unsigned lds_addr(const float* p) {
  return (unsigned)(size_t)(const __attribute__((address_space(3))) float*)p;
}

template <bool early>
__device__ __forceinline__ void body(float* lds, const int h, const int sp, const int w, const bf16* __restrict__ x,
                                     const bf16* __restrict__ kc, const bf16* __restrict__ vc,
                                     const bf16* __restrict__ wq, const bf16* __restrict__ wo,
                                     const bf16* __restrict__ w1, const bf16* __restrict__ w3,
                                     const bf16* __restrict__ w2, u64* __restrict__ gw, bf16* __restrict__ out,
                                     unsigned cpf) {
  const int b = blockIdx.x;
  const int tid = threadIdx.x, lane = tid & 63;
  const int g = lane >> 3, c = lane & 7;
  const int rec = h * NSP + sp;

  STAMP(0);
  const bool isF = sp < 6;
  u32x4 PR[8], xa, xb, xq;
  unsigned epoch = 0, tk = 0;
  int ra = 0, RA0 = 0;
  bool va = false;
  if constexpr (early) {
    int s0_ = 240, s1_ = 80, s2_ = 240 * (sp & 1), s3_ = 80 * sp, s4_ = 64 * (sp >> 1), s5_ = 64 * w, s6_ = 24 * w;
    asm("" : "+s"(s0_), "+s"(s1_), "+s"(s2_), "+s"(s3_), "+s"(s4_), "+s"(s5_), "+s"(s6_));
    const int rw0 = isF ? s5_ : s6_;
    int lim = (isF ? s0_ : s1_) - rw0, lim2 = isF ? 64 : 24;
    asm("" : "+s"(lim), "+s"(lim2));
    lim = lim < lim2 ? lim : lim2;
    ra = rw0 + 8 * g;
    va = 8 * g < lim;
    RA0 = isF ? s2_ : s3_;
    int aoff = (int)__umul24(g, 8 * HQ * DH * 2) + 16 * c;
    PIN(aoff);
    aoff = va ? aoff : NOWHERE;
    int sa = ((RA0 + rw0) * (HQ * DH) + h * DH + (isF ? s4_ : 64)) * 2;
    PIN(aoff);
    asm volatile("" : "+s"(sa));
    SCHED_FENCE();
    const __amdgpu_buffer_rsrc_t rw = rsrc(wq, NOWHERE);
    PR[0] = __builtin_amdgcn_raw_buffer_load_b128(rw, aoff, sa, 2);
    SCHED_FENCE();
    tk = NOWT();
    SCHED_FENCE();
    asm volatile("s_load_dword %0, %1, %2" : "=&s"(epoch) : "s"(gw), "n"(GEP * 8) : "memory");
    SCHED_FENCE();
#pragma unroll
    for (int i = 1; i < 8; ++i) PR[i] = __builtin_amdgcn_raw_buffer_load_b128(rw, aoff, sa + i * (HQ * DH * 2), 2);
    SCHED_FENCE();
  }
  const unsigned short* xs = reinterpret_cast<const unsigned short*>(x);
  const size_t wgb = ((size_t)h * SK + (SK - WIN)) * DH;
  const __amdgpu_buffer_rsrc_t rk = rsrc(kc + wgb, WIN * DH * 2);
  const __amdgpu_buffer_rsrc_t rv = rsrc(vc + wgb, WIN * DH * 2);
  const int voff = (g * DH + 8 * c) * 2;
  auto tile = [&](int t) { return (t * 64 + h * HROT + w * 8 + sp) & 511; };
  u32x4 K[8][3], V[8][3];
  auto kld = [&](int t, int j) { K[t][j] = __builtin_amdgcn_raw_buffer_load_b128(rk, voff + 128 * j, tile(t) * 3072, 2); };
  auto vld = [&](int t, int j) { V[t][j] = __builtin_amdgcn_raw_buffer_load_b128(rv, voff + 128 * j, tile(t) * 3072, 2); };
  unsigned tag = 0;
  const unsigned clk = lds_addr(lds + L_CK);
  const int ph = (w * PV_) >> 3;
  const int r4 = lane >> 4, c16 = lane & 15;
  const int ch = c16 < 10 ? c16 : 9;
  const int fi = (h & 3) * 6 + (isF ? sp : 5), gl = fi >> 3, gI = (h >> 2) * 3 + gl, fr = fi & 7;
  u32x4 WO[6], X[6], W2[4];
  unsigned tch = 0;
  const int wooff = ((48 * w + r4 + (isF ? 24 * (g & 1) : 24)) * D + 64 * sp + 8 * c + (isF ? 0 : 128 * (g & 1))) * 2;
  typedef unsigned u32x2 __attribute__((ext_vector_type(2)));
  const __amdgpu_buffer_rsrc_t rl = rsrc(gw + GL, 3 * 2048);
  const bool pub = !((sp + (b >> 6)) & 3);
  const int lw_ = lane == 0 && pub ? (sp * 8 + (b & 7)) * 32 : NOWHERE;
  u32x2 lat = {0u, 0u};
  u64 own = 0, pre = 0;
  unsigned pro = 0, st1 = 0, old = 0;
  float ma = 0.f;
  int sa_ = 0;
  if constexpr (early) {
    u32x4 PF[5];
    const int F0 = (isF ? 160 * (fr >> 1) : 480) + 40 * w;
    const int fs = isF ? FF : HQ * DH;
    int s6_ = 64 * gI, s7_ = h * DH + 128 * (sp & 1);
    asm("" : "+s"(s6_), "+s"(s7_));
    const int boff = ((int)__umul24(g, fs) + 8 * c) * 2;
    const int sb = (F0 * fs + (isF ? s6_ : s7_)) * 2;
    const bf16* pf_ = (fr & 1) ? w3 : w1;
    asm("" : "+s"(pf_));
    const __amdgpu_buffer_rsrc_t rf = rsrc(isF ? pf_ : wq, NOWHERE);
    unsigned short xr[5];
#pragma unroll
    for (int i = 0; i < 5; ++i) PF[i] = __builtin_amdgcn_raw_buffer_load_b128(rf, boff, sb + i * 16 * fs, 2);
    SCHED_FENCE();
    if (!isF) {
#pragma unroll
      for (int i = 0; i < 6; ++i)
        X[i] = __builtin_amdgcn_raw_buffer_load_b128(rsrc(wo + (size_t)h * DH * D, DH * D * 2), wooff - 24 * D * 2 + i * (4 * D * 2), 0, 2);
    } else {
#pragma unroll
      for (int i = 0; i < 6; ++i) X[i] = u32x4{0u, 0u, 0u, 0u};
    }
    SCHED_FENCE();
    xa = *reinterpret_cast<const u32x4*>(x + 8 * lane);
    xb = *reinterpret_cast<const u32x4*>(x + 512 + 8 * (lane & 15));
    xq = *reinterpret_cast<const u32x4*>(x + RA0 + (va ? ra : 0));
#pragma unroll
    for (int i = 0; i < 5; ++i) xr[i] = xs[F0 + 8 * i + g];
    SCHED_FENCE();
    const unsigned epv = __hip_atomic_load(reinterpret_cast<unsigned*>(gw + GEP), __ATOMIC_RELAXED, __HIP_MEMORY_SCOPE_AGENT);
    SCHED_FENCE();
    if (tid < 16) reinterpret_cast<unsigned*>(lds)[L_CK + tid] = tk;
    asm volatile("s_waitcnt lgkmcnt(0)\n\ts_barrier" : "+s"(epoch)::"memory");
    STAMP(6);
    SCHED_FENCE();
#pragma unroll
    for (int t = 0; t < 3; ++t)
#pragma unroll
      for (int j = 0; j < 3; ++j) kld(t, j);
    SCHED_FENCE();
    float mb, den;
    {
      float xf[8], xg[8];
#pragma unroll
      for (int i = 0; i < 4; ++i) {
        xf[2 * i] = bf_lo(xa[i]);
        xf[2 * i + 1] = bf_hi(xa[i]);
        xg[2 * i] = bf_lo(xb[i]);
        xg[2 * i + 1] = bf_hi(xb[i]);
      }
      const bool lo16 = lane < 16;
      float sum = 0.f;
#pragma unroll
      for (int i = 0; i < 8; ++i) sum += xf[i] + (lo16 ? xg[i] : 0.f);
      sum = wave_sum(sum);
      const float mean = sum * (1.0f / D);
      float sq = 0.f;
#pragma unroll
      for (int i = 0; i < 8; ++i) {
        const float d1 = xf[i] - mean, d2 = xg[i] - mean;
        sq += d1 * d1 + (lo16 ? d2 * d2 : 0.f);
      }
      sq = wave_sum(sq);
      mb = bfr(mean);
      const float vb = bfr(sq * (1.0f / D));
      den = bfr(sqrtf(bfr(vb + 1e-5f)));
    }
    {
      unsigned xn[13];
#pragma unroll
      for (int i = 0; i < 4; ++i) {
        xn[2 * i] = fbits(bfr(bfr(bf_lo(xq[i]) - mb) / den));
        xn[2 * i + 1] = fbits(bfr(bfr(bf_hi(xq[i]) - mb) / den));
      }
#pragma unroll
      for (int i = 0; i < 5; ++i) xn[8 + i] = fbits(bfr(bfr(bf_lo(xr[i]) - mb) / den));
#pragma unroll
      for (int i = 0; i < 13; ++i) PIN(xn[i]);
      SCHED_FENCE();
      pace<t_round(2), 2>(clk, ph);
#pragma unroll
      for (int j = 0; j < 3; ++j) kld(3, j);
      TT(2);
      SCHED_FENCE();
      float fa[8], fg[8];
#pragma unroll
      for (int k = 0; k < 8; ++k) fa[k] = fg[k] = 0.f;
#pragma unroll
      for (int i = 0; i < 8; ++i)
#pragma unroll
        for (int k = 0; k < 4; ++k) {
          fa[2 * k] = dot2(PR[i][k], xn[i] >> 16, fa[2 * k]);
          fa[2 * k + 1] = dot2(PR[i][k], xn[i], fa[2 * k + 1]);
        }
      lds[L_QP + w * 128 + 8 * c + g] = xg_reduce8(fa, g & 1);
#pragma unroll
      for (int i = 0; i < 5; ++i)
#pragma unroll
        for (int k = 0; k < 4; ++k) {
          fg[2 * k] = dot2(PF[i][k], xn[8 + i] >> 16, fg[2 * k]);
          fg[2 * k + 1] = dot2(PF[i][k], xn[8 + i], fg[2 * k + 1]);
        }
      lds[L_QP + w * 128 + 64 + 8 * c + g] = xg_reduce8(fg, g & 1);
    }
    SCHED_FENCE();
    tag = __builtin_amdgcn_readfirstlane(epv) + 1u;
    if (tid == 0) reinterpret_cast<unsigned*>(lds)[L_SY + 3] = tag;
    LDS_DRAIN();
    if (lane == 0) old = lds_add(lds, L_SY);
    old = __builtin_amdgcn_readfirstlane(old);
    if (old == 3) {
      const float2* qp = reinterpret_cast<const float2*>(lds + L_QP) + lane;
      const float2 p0_ = qp[0], p1_ = qp[64], p2_ = qp[128], p3_ = qp[192];
      gst16(gw + GQ + (size_t)rec * QS + 2 * lane, fbits((p0_.x + p1_.x) + (p2_.x + p3_.x)),
            fbits((p0_.y + p1_.y) + (p2_.y + p3_.y)), tag);
    }
    STAMP(11);
  } else {
    if (tid >= NT - 8) reinterpret_cast<unsigned*>(lds)[L_SY + tid - (NT - 8)] = 0u;
    unsigned st0 = 0;
    {
      const bf16* tp = w == 4 ? kc + wgb : w == 5 ? (!isF ? wo + (size_t)h * DH * D : (fr & 1) ? w3 : w1) : w == 6 ? ((sp & 1) ? x : wq) : vc + wgb;
      asm volatile("s_waitcnt lgkmcnt(0)\n\ts_load_dword %0, %1, 0x0" : "=&s"(st0) : "s"(tp) : "memory");
    }
    SCHED_FENCE();
#pragma unroll
    for (int j = 0; j < 3; ++j) kld(0, j);
    SCHED_FENCE();
    asm volatile("s_barrier" ::: "memory");
    STAMP(6);
    SCHED_FENCE();
    if (w < 6) {
      const int l = lane < 42 ? lane : 41, k = l & 15;
      const int yg = 4 * (k < 8 || isF ? sp : sp + 2) + ((k >> 1) & 3);
      const size_t yo = l < 32 || !isF ? GY + (size_t)yg * YB + 32 * h + 16 * (h >> 4) + 8 * (k & 1)
                                       : GZ + (size_t)(fr * 5 + (k >> 1)) * YB + 32 * gI + 16 * (gI >> 4) + 8 * (k & 1);
      unsigned o4 = (unsigned)(l < 16 ? G2 + (size_t)rec * RS + 8 * l : yo);
      unsigned o5 = (unsigned)(GQ + (size_t)rec * QS + 8 * (lane < 16 ? lane : 15));
      PIN(o4);
      PIN(o5);
      pre = gld(gw + (w == 4 ? o4 : o5));
    }
    if (w == 7 && h < 5)
      pro = __hip_atomic_load(reinterpret_cast<const unsigned*>(out + 80 * sp + 16 * h) + (lane & 7), __ATOMIC_RELAXED,
                              __HIP_MEMORY_SCOPE_AGENT);
    SCHED_FENCE();
    asm volatile("s_waitcnt lgkmcnt(0)" ::"s"(st0) : "memory");
#pragma unroll
    for (int j = 0; j < 3; ++j) {
      kld(1, j);
      THRN(5);
    }
    THRN(3);
    TT(1);
    own = __builtin_amdgcn_s_memrealtime() - SB_;
    __builtin_amdgcn_raw_buffer_store_b64(u32x2{(unsigned)own, (unsigned)(own >> 32)}, rl, w == 6 ? lw_ : NOWHERE, 0, SC1);
    SCHED_FENCE();
    if (LOC && w == 6 && lane < 16) {
      const unsigned o = (unsigned)own - LOC;
      if ((int)(o - reinterpret_cast<unsigned*>(lds)[L_CK + lane]) > 0) reinterpret_cast<unsigned*>(lds)[L_CK + lane] = o;
    }
    SCHED_FENCE();
    {
      const bf16* tp = (w & 1) ? wo + (size_t)h * DH * D : w2 + (size_t)gI * 64 * D;
      asm volatile("s_load_dword %0, %1, 0x0" : "=&s"(st1) : "s"(tp) : "memory");
    }
    SCHED_FENCE();
  }
  u32x4 qf[3];
  float sc[8], mw, lw, acc[24];
  unsigned pb[8];
  auto score = [&](int t) {
    float d = 0.f;
#pragma unroll
    for (int j = 0; j < 3; ++j)
#pragma unroll
      for (int i = 0; i < 4; ++i) d = dot2(K[t][j][i], qf[j][i], d);
    d = grp8_sum(d);
    sc[t] = bfr(bfr(d) * SCALE);
  };
  auto pv = [&](int t) {
    const unsigned plo = pb[t], phi = plo << 16;
#pragma unroll
    for (int j = 0; j < 3; ++j)
#pragma unroll
      for (int i = 0; i < 4; ++i) {
        acc[8 * j + 2 * i] = dot2(V[t][j][i], plo, t ? acc[8 * j + 2 * i] : 0.f);
        acc[8 * j + 2 * i + 1] = dot2(V[t][j][i], phi, t ? acc[8 * j + 2 * i + 1] : 0.f);
        PIN(acc[8 * j + 2 * i]);
        PIN(acc[8 * j + 2 * i + 1]);
      }
  };
  const bool hf = lane >> 5;
  const __amdgpu_buffer_rsrc_t rq_ = rsrc(gw + GQ, NWG * QS * 8);
  int go[9];
  {
    const int l16 = (lane & 31) * 16;
    const int qa = l16 + (8 * h + (hf ? 4 : 0)) * (QS * 8);
    go[0] = qa;
    go[1] = qa + QS * 8;
    go[2] = l16 + (8 * h + (hf ? 7 : 6)) * (QS * 8) + 512;
    go[3] = qa + 2 * QS * 8;
    go[4] = qa + 3 * QS * 8;
#pragma unroll
    for (int k = 0; k < 4; ++k) {
      const int m = 8 * gl + 2 * k + (hf ? 1 : 0);
      go[5 + k] = isF ? l16 + (32 * (h >> 2) + m + (((m * 43) >> 7) & 6)) * (QS * 8) + 512 : NOWHERE;
    }
  }
  const bool gath = old == GRW;
  u32x4 pa[9];
  constexpr int C0 = VR - 2 + (early ? CDE : CDL);
  static_assert(C0 + 3 < 15 && C0 > GR + 1, "schedule");
  sfor<early ? 3 : 2, 16>([&](auto R) {
    constexpr int r = R.v, t = tile_of(r) + (early && !is_v(r));
    pace<t_round(r), r>(clk, ph);
    if constexpr (!early) {
      sfor<PA ? 0 : 1, 2>([&](auto Q) {
        constexpr int q = Q.v, mr = q ? MR : PS - 1 + MAO, rd = q ? MRD : MRDA;
        if (FA && r == mr + rd) lat = __builtin_amdgcn_raw_buffer_load_b64(rl, w == MW ? lane * 32 : NOWHERE, 2048 * (q + 1), SC1);
      });
      SCHED_FENCE();
    }
    if constexpr (!is_v(r) && t == 8) {
      const __amdgpu_buffer_rsrc_t rx = rsrc(w2 + ((size_t)gI * 64 + 16 * w) * D + 80 * fr, isF ? 16 * D * 2 : 0);
#pragma unroll
      for (int i = 0; i < 4; ++i)
        W2[i] = __builtin_amdgcn_raw_buffer_load_b128(rx, (r4 * D + 8 * ch) * 2 + i * (4 * D * 2), 0, 2);
      tch = __builtin_amdgcn_raw_buffer_load_b32(rsrc(wo + (size_t)h * DH * D, DH * D * 2), tid ? NOWHERE : wooff, 0, 2);
    } else {
#pragma unroll
      for (int j = 0; j < 3; ++j) {
        if (is_v(r)) vld(t, j); else kld(t, j);
        if (r == 15 && j == 2) break;
        if (early && r == GR + 1) continue;
        THRN(early ? (r == (PS == 8 ? 8 : VR) ? NFL + 2 : r == 3 ? NFL + 1 : NFL)
                   : NFL);
      }
    }
    TT(r);
    SCHED_FENCE();
    if constexpr (early) {
      if (r == GR && gath) {
#pragma unroll
        for (int s = 0; s < 9; ++s) pa[s] = __builtin_amdgcn_raw_buffer_load_b128(rq_, go[s], 0, SC1);
        SCHED_FENCE();
      }
      if (r == GR + 1) {
        if (gath) {
          const unsigned tf = isF ? tag : 0u;
          bool ok = true;
#pragma unroll
          for (int s = 0; s < 9; ++s) ok = ok & (pa[s][1] == (s < 5 ? tag : tf)) & (pa[s][3] == (s < 5 ? tag : tf));
          if (!__all(ok)) {
            for (unsigned spin = 0;; ++spin) {
              ok = true;
              asm volatile("" ::: "memory");
#pragma unroll
              for (int s = 0; s < 9; ++s) {
                pa[s] = __builtin_amdgcn_raw_buffer_load_b128(rq_, go[s], 0, SC1);
                ok = ok & (pa[s][1] == (s < 5 ? tag : tf)) & (pa[s][3] == (s < 5 ? tag : tf));
              }
              if (__all(ok) || spin >= SPIN_MAX) break;
              __builtin_amdgcn_s_sleep(1);
            }
          }
          float qo[2], qm[2], hu[2];
#pragma unroll
          for (int k = 0; k < 2; ++k) {
            auto val = [&](int s) { return __builtin_bit_cast(float, (unsigned)pa[s][2 * k]); };
            qo[k] = (val(0) + val(1)) + val(2);
            const float t_ = val(3) + val(4);
            qm[k] = sw32(t_, t_);
            const float u = bfr((val(5) + val(6)) + (val(7) + val(8)));
            auto sw = __builtin_amdgcn_permlane32_swap(fbits(u), fbits(u), false, false);
            hu[k] = bfr(bfr(silu(__builtin_bit_cast(float, (unsigned)sw[0]))) * __builtin_bit_cast(float, (unsigned)sw[1]));
          }
          reinterpret_cast<unsigned*>(lds + L_QS)[lane + (hf ? 32 : 0)] = bfpack(qo[0], qo[1]);
          if (hf) {
            reinterpret_cast<unsigned*>(lds + L_QS)[lane] = bfpack(qm[0], qm[1]);
          } else {
            *reinterpret_cast<float2*>(lds + L_H + 2 * lane) = float2{hu[0], hu[1]};
          }
          LDS_DRAIN();
          if (lane == 0) lds_add(lds, L_SY + 2);
        }
        STAMP(2);
      }
    } else {
      unsigned org = 0;
      if (r == MR + 1 || r == PS + MAO) org = __builtin_amdgcn_readfirstlane(reinterpret_cast<const unsigned*>(lds)[L_CK + r]);
      if (r == 2) asm volatile("" ::"s"(st1), "v"(cpf), "v"(pre), "v"(pro));
      if (r == AR) lat = __builtin_amdgcn_raw_buffer_load_b64(rl, w == 6 ? lane * 32 : NOWHERE, 0, SC1);
      if (r == AR + 1) {
        const u64 d = (((u64)lat[1] << 32) | lat[0]) - own + 200u;
        const bool ok = d <= 400u;
        const float s_ = wave_sum(ok ? (float)(unsigned)d : 0.f), n = (float)__builtin_popcountll(__ballot(ok));
        const unsigned o = (unsigned)own + (n > 0.f ? (unsigned)(s_ * __builtin_amdgcn_rcpf(n)) - 200u : 0u);
        if (w == 6 && lane < 16) reinterpret_cast<unsigned*>(lds)[L_CK + lane] = o;
      }
      sfor<PA ? 0 : 1, 2>([&](auto Q) {
        constexpr int q = Q.v, mr = q ? MR : PS - 1 + MAO, rd = q ? MRD : MRDA;
        if (!FA && r == mr + rd + 1) {
          const float m = wave_sum((float)(lat[1] == tag && lat[0] < UMISS ? lat[0] : UMISS)) * (1.f / 64);
          int d = 0;
          if (q == 0) {
            ma = m;
            sa_ = m >= ATHR ? (int)(m * (ASH / 4.f)) : 0;
            d = m >= ATHR ? sa_ + AGX * (lane <= AG0 ? 0 : lane < AG1 ? lane - AG0 : AG1 - AG0) : 0;
          } else {
            const int sb_ = (int)(m * (USH / 4.f));
            d = (m >= UTHR && sb_ > 0 ? sb_ : 0) + (m - ma * (UDF / 4.f) >= UTHR && lane > VR ? UGX * (lane - VR) : 0);
          }
          if (w == MW && lane < 16) reinterpret_cast<unsigned*>(lds)[L_CK + lane] += d;
        }
        if (r == mr + 1) {
          tag = __builtin_amdgcn_readfirstlane(reinterpret_cast<const unsigned*>(lds)[L_SY + 3]);
          THRN(3);
          const unsigned l = NOWT() - org - (unsigned)(t_round(r) + ph + 9);
          __builtin_amdgcn_raw_buffer_store_b64(u32x2{l > (1u << 30) ? 0u : l, tag}, rl, w == MW && (int)l < (q ? ULIM : ALIM) ? lw_ : NOWHERE,
                                                2048 * (q + 1), SC1);
        }
        if (!FA && r == mr + rd) lat = __builtin_amdgcn_raw_buffer_load_b64(rl, w == MW ? lane * 32 : NOWHERE, 2048 * (q + 1), SC1);
      });
      SCHED_FENCE();
    }
    if (r == C0) {
      lds_wait(lds, L_SY + 2, 1u);
      STAMP(3);
#pragma unroll
      for (int j = 0; j < 3; ++j) qf[j] = *reinterpret_cast<const u32x4*>(lds + L_QS + (c + 8 * j) * 4);
#pragma unroll
      for (int k = 0; k < 4; ++k) score(k);
    } else if (r == C0 + 1) {
#pragma unroll
      for (int k = 4; k < 7; ++k) score(k);
    } else if (r == C0 + 2) {
      score(7);
      mw = sc[0];
#pragma unroll
      for (int k = 1; k < 8; ++k) mw = fmaxf(mw, sc[k]);
      mw = xg_max(mw);
      lw = 0.f;
#pragma unroll
      for (int k = 0; k < 8; ++k) {
        const float p = ex2((sc[k] - mw) * LOG2E);
        lw += p;
        pb[k] = bfbits(p);
      }
      lw = xg_sum(lw);
      if (lane == 0) {
        lds[L_ML + 2 * w] = mw;
        lds[L_ML + 2 * w + 1] = lw;
      }
      if (early) {
        float z[8];
#pragma unroll
        for (int n = 0; n < 8; ++n) z[n] = 0.f;
#pragma unroll
        for (int r = 0; r < 4; ++r) {
          const unsigned chi = fbits(lds[L_H + 16 * w + 4 * r + r4]), clo = chi >> 16;
#pragma unroll
          for (int n = 0; n < 4; ++n) {
            z[2 * n] = dot2(W2[r][n], clo, z[2 * n]);
            z[2 * n + 1] = dot2(W2[r][n], chi, z[2 * n + 1]);
          }
        }
        const float2 zr = rows_reduce(z);
        if (c16 < 10) *reinterpret_cast<float2*>(lds + L_Z + 80 * w + 8 * c16 + 2 * r4) = zr;
      }
      LDS_DRAIN();
      if (lane == 0) lds_add(lds, L_SY + 4);
      STAMP(4);
    } else if (r == C0 + 3) {
#pragma unroll
      for (int k = 0; k < t + 1 - PVL; ++k) pv(k);
    } else if (r > C0 + 3 && r < 15) {
      pv(t - PVL);
    }
    if constexpr (!early) {
      SCHED_FENCE();
      sfor<PA ? 0 : 1, 2>([&](auto Q) {
        constexpr int q = Q.v, mr = q ? MR : PS - 1 + MAO, rd = q ? MRD : MRDA;
        if (FA && r == mr + rd) {
          const float m = wave_sum((float)(lat[1] == tag && lat[0] < UMISS ? lat[0] : UMISS)) * (1.f / 64);
          int d = 0;
          if (q == 0) {
            ma = m;
            sa_ = m >= ATHR ? (int)(m * (ASH / 4.f)) : 0;
            d = m >= ATHR ? sa_ + AGX * (lane <= AG0 ? 0 : lane < AG1 ? lane - AG0 : AG1 - AG0) : 0;
          } else {
            const int sb_ = (int)(m * (USH / 4.f));
            d = (m >= UTHR && sb_ > 0 ? sb_ : 0) + (m - ma * (UDF / 4.f) >= UTHR && lane > VR ? UGX * (lane - VR) : 0);
          }
          if (w == MW && lane < 16) reinterpret_cast<unsigned*>(lds)[L_CK + lane] += d;
        }
      });
    }
  });
  asm volatile("" ::"v"(tch));
  SCHED_FENCE();
  const __amdgpu_buffer_rsrc_t rwo = rsrc(wo + (size_t)h * DH * D, DH * D * 2);
  lds_wait(lds, L_SY + 4, 8u);
  float f, M, L;
  wg_ml(lds, M, L);
  f = ex2((mw - M) * LOG2E) / L;
  const unsigned cw = fbits(fmaf(M, LOG2E, __builtin_amdgcn_logf(L)));
  if (!early) tag = __builtin_amdgcn_readfirstlane(reinterpret_cast<const unsigned*>(lds)[L_SY + 3]);
  PIN(f);
  SCHED_FENCE();
  const int l40 = lane < 40 ? lane : 39;
  u64* rdst = gw + G2 + (size_t)rec * RS + 32 * r4 + 2 * c16;
  u64* zdst = gw + GZ + (size_t)(fr * 5 + (l40 >> 3)) * YB + 32 * gI + 16 * (gI >> 4) + 2 * (l40 & 7);
  unsigned pdst = lds_addr(lds + L_PART + w * DH + 8 * c + g), psrc = lds_addr(lds + L_PART + 48 * r4 + 4 * c16);
  unsigned cnt6 = lds_addr(lds + L_SY + 6), one = 1u;
  u32x4 rq = {cw, tag, cw, tag};
  u64 m48 = 0x0fff0fff0fff0fffull, m52 = 0x1fff1fff1fff1fffull;
  asm volatile("" : "+s"(m48), "+s"(m52), "+{v[156:159]}"(rq));
  PIN(rdst);
  PIN(zdst);
  PIN(pdst);
  PIN(psrc);
  PIN(cnt6);
  PIN(one);
#pragma unroll
  for (int k = 7 - PVL; k < 7; ++k) pv(k);
  {
    unsigned plo = pb[7], phi = plo << 16;
    PIN(plo);
    PIN(phi);
    sfor<0, 3>([&](auto J) {
      constexpr int j = J.v;
      SCHED_FENCE();
      float a[8];
#pragma unroll
      for (int i = 0; i < 4; ++i) {
        a[2 * i] = dot2(V[7][j][i], plo, acc[8 * j + 2 * i]);
        a[2 * i + 1] = dot2(V[7][j][i], phi, acc[8 * j + 2 * i + 1]);
      }
      xg_store<256 * j>(a, f, pdst);
    });
  }
  STAMP(5);
  unsigned arr;
  asm volatile(
      "s_mov_b64 exec, 1\n\tds_add_rtn_u32 v112, %[cnt], %[one]\n\ts_mov_b64 exec, %[m48]\n\t" SUM8("%[src]")
      "v_readfirstlane_b32 %[arr], v112\n\t" SUM8B
      "s_cmp_eq_u32 %[arr], 7\n\t" SUM8C
      "v_cvt_pk_bf16_f32 v156, v116, v117\n\tv_cvt_pk_bf16_f32 v158, v118, v119\n\t"
      "s_cselect_b64 exec, %[m52], 0\n\tglobal_store_dwordx4 %[dst], v[156:159], off sc1\n\ts_mov_b64 exec, -1\n\ts_nop 0"
      : [arr] "=&s"(arr), "+{v[156:159]}"(rq)
      : [cnt] "v"(cnt6), [one] "v"(one), [m48] "s"(m48), [m52] "s"(m52), [src] "v"(psrc), [dst] "v"(rdst)
      : CLOB_T, "scc", "memory");
  if (arr == 0 && isF) {
    const float2* zp = reinterpret_cast<const float2*>(lds + L_Z) + l40;
    const float2 z0 = zp[0], z1 = zp[40], z2 = zp[80], z3 = zp[120];
    gst16(zdst, fbits((z0.x + z1.x) + (z2.x + z3.x)), fbits((z0.y + z1.y) + (z2.y + z3.y)), tag);
  }
  if constexpr (!early) {
    if (w == 7 && h < 5) {
      const int col = 80 * sp + 16 * h + 2 * c + (lane >> 5);
      const float xv = bf_lo(xs[col]);
      const u64* py = gw + GZ + (size_t)(sp * 5 + h) * YB + 32 * g + 2 * c + 392;
      bf16* po = out + col;
      float gt = 0.f;
      u64 m3 = 0ull, ms = 0ull;
      PIN(po);
#pragma unroll 1
      for (int pass = 0; pass < 2; ++pass) {
        lds_wait(lds, L_SY + (pass ? 1 : 6), pass ? 4u : 8u);
        if (FT_ && pass) pace<T_LAST + FT_, 15>(clk, 0);
        STAMP(8);
        asm volatile("" : "+s"(m3), "+s"(ms));
        float t;
        unsigned n;
        asm volatile(
            "v_mov_b32 v124, 0\n\tv_mov_b32 v125, %[tag]\n\tv_mov_b32 v126, 0\n\tv_mov_b32 v127, %[tag]\n\t"
            "s_mov_b64 vcc, -1\n\ts_mov_b32 %[n], 0\n"
            ".Lfp%=:\n\t"
            "s_add_u32 %[n], %[n], 1\n\ts_cmp_gt_u32 %[n], %[max]\n\ts_cbranch_scc1 .Lfd%=\n\t"
            "s_mov_b64 exec, vcc\n\t"
            "global_load_dwordx4 v[112:115], %[p], off offset:-3136 sc1\n\t"
            "global_load_dwordx4 v[116:119], %[p], off offset:-1088 sc1\n\t"
            "global_load_dwordx4 v[120:123], %[p], off offset:1088 sc1\n\t"
            "s_and_b64 exec, vcc, %[m3]\n\t"
            "global_load_dwordx4 v[124:127], %[p], off offset:3136 sc1\n\t"
            "s_mov_b64 exec, -1\n\ts_waitcnt vmcnt(0)\n\t"
            "v_bitop3_b32 v128, v113, v115, %[tag] bitop3:0x7e\n\tv_bitop3_b32 v129, v117, v119, %[tag] bitop3:0x7e\n\t"
            "v_bitop3_b32 v130, v121, v123, %[tag] bitop3:0x7e\n\tv_bitop3_b32 v131, v125, v127, %[tag] bitop3:0x7e\n\t"
            "v_or3_b32 v128, v128, v129, v130\n\tv_add_f32 v132, v112, v116\n\tv_or_b32 v128, v128, v131\n\t"
            "v_add_f32 v134, v120, v124\n\tv_cmp_ne_u32 vcc, 0, v128\n\tv_add_f32 v133, v114, v118\n\t"
            "s_cbranch_vccnz .Lfp%=\n"
            ".Lfd%=:\n\t"
            "v_add_f32 v135, v122, v126\n\tv_add_f32 v132, v132, v134\n\tv_add_f32 v133, v133, v135\n\ts_nop 1\n\t"
            "v_permlane32_swap_b32 v132, v133\n\tv_add_f32 v134, v132, v133\n\tv_add_f32 v135, v132, v133\n\ts_nop 1\n\t"
            "v_permlane16_swap_b32 v134, v135\n\tv_add_f32 v134, v134, v135\n\ts_nop 1\n\t"
            "v_add_f32_dpp v134, v134, v134 row_ror:8 row_mask:0xf bank_mask:0xf\n\t"
            "v_cvt_pk_bf16_f32 v134, v134, v134\n\tv_lshlrev_b32 %[t], 16, v134\n\t"
            "v_mul_f32 v134, %[t], %[gt]\n\tv_cvt_pk_bf16_f32 v134, v134, v134\n\tv_lshlrev_b32 v134, 16, v134\n\t"
            "v_add_f32 v134, %[xv], v134\n\tv_cvt_pk_bf16_f32 v134, v134, v134\n\t"
            "s_mov_b64 exec, %[ms]\n\tglobal_store_short %[po], v134, off sc1\n\ts_mov_b64 exec, -1"
            : [t] "=&v"(t), [n] "=&s"(n)
            : [tag] "s"(tag), [max] "n"(SPIN_MAX), [p] "v"(py), [m3] "s"(m3), [ms] "s"(ms), [gt] "v"(gt), [xv] "v"(xv), [po] "v"(po)
            : CLOB_T, "vcc", "scc", "memory");
        if (pass) break;
        gt = bfr(silu(t));
        py -= GZ - GY;
        m3 = ~0ull;
        ms = 0x000000ff000000ffull;
      }
      if (b == 0 && lane == 0)
        __hip_atomic_store(reinterpret_cast<unsigned*>(gw + GEP), tag, __ATOMIC_RELAXED, __HIP_MEMORY_SCOPE_AGENT);
      STAMP(10);
    }
    STAMP_FLUSH();
    asm volatile(".Lcode_lastb:");
    return;
  }
  pace<T_LAST + WT_, 15>(clk, 2 * ph);
#pragma unroll
  for (int i = 0; i < 6; ++i) {
    if (i == 3) pace<T_LAST + WT_ + PV_ / 8, 15>(clk, 2 * ph);
    asm volatile("buffer_load_dwordx4 %0, %1, %2, %3 offen nt" : "=&v"(WO[i]) : "v"(wooff), "s"(rwo), "s"(i * (4 * D * 2)) : "memory");
  }
  SCHED_FENCE();
  const int ly = isF ? lane & 31 : lane;
  u64* ydst = gw + GY + (size_t)(4 * sp + (ly >> 3) + 4 * (ly >> 5)) * YB + 32 * h + 16 * (h >> 4) + 2 * (ly & 7);
  static_assert(RS * 8 * 4 == 4608, "record r4 + 4 is 4608 bytes behind record r4");
  const u64* pr = gw + G2 + (size_t)(h * NSP + r4 + 2) * RS + 32 * w + 2 * c16;
  const u64 ownm = 0xffffull << (16 * (sp & 3));
  u64 own0 = sp < 4 ? ownm : 0ull, own1 = sp < 4 ? 0ull : ownm, my = isF ? 0xffffffffull : ~0ull;
  u64 for0 = m52 & ~own0, for1 = m52 & ~own1;
  own0 &= m48;
  own1 &= m48;
  unsigned osrc = lds_addr(lds + L_PART + 48 * w + 4 * c16), ysrc = lds_addr(lds + L_Y + 2 * ly), cnt1 = lds_addr(lds + L_SY + 1);
  unsigned ydl = lds_addr(lds + L_Y + 128 * w + (isF ? 8 * c + g : 8 * c16 + 2 * r4));
  asm volatile("" : "+s"(own0), "+s"(own1), "+s"(for0), "+s"(for1), "+s"(my));
  PIN(ydst);
  PIN(pr);
  PIN(osrc);
  PIN(ysrc);
  PIN(cnt1);
  PIN(ydl);
  u32x8 WP[3], XP[3];
#pragma unroll
  for (int p = 0; p < 3; ++p)
#pragma unroll
    for (int k = 0; k < 4; ++k) {
      XP[p][2 * k] = __builtin_amdgcn_perm(X[2 * p + 1][k], X[2 * p][k], 0x05040100u);
      XP[p][2 * k + 1] = __builtin_amdgcn_perm(X[2 * p + 1][k], X[2 * p][k], 0x07060302u);
    }
  asm volatile("" : "+{v[64:71]}"(XP[0]), "+{v[72:79]}"(XP[1]), "+{v[80:87]}"(XP[2]));
  SCHED_FENCE();
  auto perm_wo = [&]() {
#pragma unroll
    for (int p = 0; p < 3; ++p)
#pragma unroll
      for (int k = 0; k < 4; ++k) {
        WP[p][2 * k] = __builtin_amdgcn_perm(WO[2 * p + 1][k], WO[2 * p][k], 0x05040100u);
        WP[p][2 * k + 1] = __builtin_amdgcn_perm(WO[2 * p + 1][k], WO[2 * p][k], 0x07060302u);
      }
    asm volatile("" : "+{v[88:95]}"(WP[0]), "+{v[96:103]}"(WP[1]), "+{v[104:111]}"(WP[2]));
    SCHED_FENCE();
    STAMP(1);
  };
  lds_wait(lds, L_SY + 6, 8u);
  if (PT_) pace<T_LAST + PT_, 15>(clk, 0);
  STAMP(4);
  u32x4 ra0, ra1;
  asm volatile(
      "v_mov_b32 v148, %[cw]\n\tv_mov_b32 v149, %[tag]\n\tv_mov_b32 v151, %[tag]\n\t"
      "v_mov_b32 v152, %[cw]\n\tv_mov_b32 v153, %[tag]\n\tv_mov_b32 v155, %[tag]\n\t"
      "s_mov_b64 exec, %[for0]\n\tglobal_load_dwordx4 v[148:151], %[p], off offset:-2304 sc1\n\t"
      "s_mov_b64 exec, %[for1]\n\tglobal_load_dwordx4 v[152:155], %[p], off offset:2304 sc1\n\t"
      "s_mov_b64 exec, %[m48]\n\t" SUM8("%[src]") SUM8B SUM8C
      "s_mov_b64 exec, %[own0]\n\tv_cvt_pk_bf16_f32 v148, v116, v117\n\tv_cvt_pk_bf16_f32 v150, v118, v119\n\t"
      "s_mov_b64 exec, %[own1]\n\tv_cvt_pk_bf16_f32 v152, v116, v117\n\tv_cvt_pk_bf16_f32 v154, v118, v119\n\t"
      "s_mov_b64 exec, -1\n\ts_waitcnt vmcnt(2)"
      : "=&{v[148:151]}"(ra0), "=&{v[152:155]}"(ra1), "+v"(WO[0]), "+v"(WO[1]), "+v"(WO[2]), "+v"(WO[3]), "+v"(WO[4]), "+v"(WO[5])
      : [cw] "v"(cw), [tag] "s"(tag), [for0] "s"(for0), [for1] "s"(for1), [p] "v"(pr), [own0] "s"(own0), [own1] "s"(own1),
        [m48] "s"(m48), [src] "v"(osrc)
      : CLOB_T, "memory");
  SCHED_FENCE();
  perm_wo();
  unsigned np;
  asm volatile(
      "s_mov_b32 %[n], 0\n\t"
      "s_mov_b64 exec, -1\n\tv_mov_b64 v[160:161], 0\n\tv_mov_b64 v[162:163], 0\n\tv_mov_b64 v[164:165], 0\n\t"
      "v_mov_b64 v[166:167], 0\n\ts_branch .Lrc%=\n"
      ".Lrp%=:\n\t"
      "s_add_u32 %[n], %[n], 1\n\ts_cmp_gt_u32 %[n], %[max]\n\ts_cbranch_scc1 .Lrd%=\n\t"
      "v_cmp_ne_u32 vcc, 0, v112\n\tv_cmp_ne_u32 s[98:99], 0, v113\n\ts_sleep " STR(RSLEEP) "\n\t"
      "s_mov_b64 exec, vcc\n\tglobal_load_dwordx4 v[148:151], %[p], off offset:-2304 sc1\n\t"
      "s_mov_b64 exec, s[98:99]\n\tglobal_load_dwordx4 v[152:155], %[p], off offset:2304 sc1\n\t"
      "s_mov_b64 exec, -1\n"
      ".Lrc%=:\n\t"
      "s_waitcnt vmcnt(0)\n\t"
      "v_bitop3_b32 v112, v149, v151, %[tag] bitop3:0x7e\n\tv_bitop3_b32 v113, v153, v155, %[tag] bitop3:0x7e\n\t"
      "v_max_f32 v115, v148, v152\n\tv_or_b32 v114, v112, v113\n\tv_max_f32 v116, v148, v152\n\t"
      "v_cmp_ne_u32 vcc, 0, v114\n\tv_lshlrev_b32 v122, 16, v148\n\ts_cbranch_vccnz .Lrp%=\n"
      ".Lrd%=:\n\t"
      "v_permlane16_swap_b32 v115, v116\n\tv_max_f32 v117, v115, v116\n\tv_max_f32 v118, v115, v116\n\t"
      "v_and_b32 v123, 0xffff0000, v148\n\tv_lshlrev_b32 v124, 16, v150\n\t"
      "v_permlane32_swap_b32 v117, v118\n\tv_max_f32 v117, v117, v118\n\t"
      "v_sub_f32 v115, v148, v117\n\tv_sub_f32 v116, v152, v117\n\tv_exp_f32 v115, v115\n\tv_exp_f32 v116, v116\n\t"
      "v_and_b32 v125, 0xffff0000, v150\n\tv_lshlrev_b32 v126, 16, v152\n\t"
      "v_add_f32 v117, v115, v116\n\tv_add_f32 v118, v115, v116\n\t"
      "v_mul_f32_dpp v122, v115, v122" BC12 "v_mul_f32_dpp v123, v115, v123" BC12
      "v_permlane16_swap_b32 v117, v118\n\tv_add_f32 v119, v117, v118\n\tv_add_f32 v120, v117, v118\n\t"
      "v_mul_f32_dpp v124, v115, v124" BC12 "v_mul_f32_dpp v125, v115, v125" BC12
      "v_permlane32_swap_b32 v119, v120\n\tv_add_f32 v119, v119, v120\n\tv_lshlrev_b32 v128, 16, v154\n\t"
      "v_rcp_f32 v119, v119\n\tv_and_b32 v127, 0xffff0000, v152\n\tv_and_b32 v129, 0xffff0000, v154\n\t"
      "v_fmac_f32_dpp v122, v116, v126" BC12 "v_fmac_f32_dpp v124, v116, v128" BC12
      "v_fmac_f32_dpp v123, v116, v127" BC12 "v_fmac_f32_dpp v125, v116, v129" BC12
      "v_permlane32_swap_b32 v122, v124\n\tv_add_f32 v122, v122, v124\n\t"
      "v_permlane32_swap_b32 v123, v125\n\tv_add_f32 v123, v123, v125\n\t"
      "v_mov_b32_dpp v120, v119" BC12 "s_nop 0\n\t"
      "v_permlane16_swap_b32 v122, v123\n\tv_add_f32 v122, v122, v123\n\t"
      "v_mul_f32 v123, v122, v120\n\ts_cmp_lt_u32 %[sp], 6\n\t"
      "v_mul_f32_dpp v124, v122, v120 quad_perm:[1,0,3,2] row_mask:0xf bank_mask:0xf\n\t"
      "v_cvt_pk_bf16_f32 v124, v123, v124\n\t"
      "s_cbranch_scc1 .Lyf%=\n\ts_nop 0\n\t"
      DOT8(64, 65, 66, 67, 68, 69, 70, 71, 0) DOT8(72, 73, 74, 75, 76, 77, 78, 79, 2) DOT8(80, 81, 82, 83, 84, 85, 86, 87, 4)
      DOT8(88, 89, 90, 91, 92, 93, 94, 95, 6) DOT8(96, 97, 98, 99, 100, 101, 102, 103, 8)
      DOT8(104, 105, 106, 107, 108, 109, 110, 111, 10) YRED
      "v_pk_add_f32 v[160:161], v[160:161], v[162:163]\n\tds_write_b64 %[ydl], v[160:161]\n"
      ".Lys%=:\n\t"
      "s_mov_b64 exec, 1\n\tds_add_rtn_u32 v112, %[cnt], %[one]\n\ts_mov_b64 exec, -1\n\t"
      "ds_read2st64_b64 v[116:119], %[ysrc] offset0:0 offset1:1\n\tds_read2st64_b64 v[120:123], %[ysrc] offset0:2 offset1:3\n\t"
      "s_waitcnt lgkmcnt(2)\n\tv_readfirstlane_b32 s98, v112\n\ts_waitcnt lgkmcnt(1)\n\t"
      "v_pk_add_f32 v[116:117], v[116:117], v[118:119]\n\ts_cmp_eq_u32 s98, 3\n\ts_cselect_b64 exec, %[my], 0\n\t"
      "s_waitcnt lgkmcnt(0)\n\tv_pk_add_f32 v[120:121], v[120:121], v[122:123]\n\ts_nop 0\n\t"
      "v_add_f32 v156, v116, v120\n\tv_add_f32 v158, v117, v121\n\t"
      "global_store_dwordx4 %[dst], v[156:159], off sc1\n\ts_mov_b64 exec, -1\n\ts_branch .Lye%=\n"
      ".Lyf%=:\n\ts_nop 0\n\t"
      "v_mov_b32_dpp v125, v124 row_newbcast:0 row_mask:0xf bank_mask:0x3\n\t"
      "v_mov_b32_dpp v126, v124 row_newbcast:2 row_mask:0xf bank_mask:0x3\n\t"
      "v_mov_b32_dpp v127, v124 row_newbcast:4 row_mask:0xf bank_mask:0x3\n\t"
      "v_mov_b32_dpp v125, v124 row_newbcast:6 row_mask:0xf bank_mask:0xc\n\t"
      "v_mov_b32_dpp v126, v124 row_newbcast:8 row_mask:0xf bank_mask:0xc\n\t"
      "v_mov_b32_dpp v127, v124 row_newbcast:10 row_mask:0xf bank_mask:0xc\n\t"
      DOT8F(88, 89, 90, 91, 92, 93, 94, 95, 125) DOT8F(96, 97, 98, 99, 100, 101, 102, 103, 126)
      DOT8F(104, 105, 106, 107, 108, 109, 110, 111, 127) YRED
      "v_add_f32 v160, v160, v162\n\tv_add_f32 v161, v161, v163\n\ts_nop 0\n\t"
      "v_add_f32_dpp v160, v160, v160 row_ror:8 row_mask:0xf bank_mask:0x3\n\t"
      "v_add_f32_dpp v160, v161, v161 row_ror:8 row_mask:0xf bank_mask:0xc\n\t"
      "ds_write_b32 %[ydl], v160\n\ts_branch .Lys%=\n"
      ".Lye%=:"
      : [n] "=&s"(np), "+{v[156:159]}"(rq), "+{v[148:151]}"(ra0), "+{v[152:155]}"(ra1)
      : [tag] "s"(tag), [p] "v"(pr), [max] "n"(SPIN_MAX), [sp] "s"(sp), [ydl] "v"(ydl), [cnt] "v"(cnt1), [one] "v"(one),
        [ysrc] "v"(ysrc), [my] "s"(my), [dst] "v"(ydst), "{v[64:71]}"(XP[0]), "{v[72:79]}"(XP[1]), "{v[80:87]}"(XP[2]),
        "{v[88:95]}"(WP[0]), "{v[96:103]}"(WP[1]), "{v[104:111]}"(WP[2])
      : CLOB_T, "v160", "v161", "v162", "v163", "v164", "v165", "v166", "v167", "s98", "s99", "vcc", "scc", "memory");
  STAMP(9);
  STAMP_FLUSH();
  asm volatile(".Lcode_last:");
}

__global__ void __launch_bounds__(NT, 2)
fused_decode(const bf16* __restrict__ wq, const bf16* __restrict__ w1, const bf16* __restrict__ w3, u64* __restrict__ gw,
             const bf16* __restrict__ x, const bf16* __restrict__ kc, const bf16* __restrict__ vc,
             const bf16* __restrict__ wo, const bf16* __restrict__ w2, bf16* __restrict__ out) {
  __shared__ __attribute__((aligned(16))) float lds[L_END];
  const int b = blockIdx.x;
  const int xcd = b & 7, jj = b >> 3;
  const int h = xcd * 4 + (jj >> 3), sp = jj & 7;
  const int w = __builtin_amdgcn_readfirstlane(threadIdx.x >> 6);
  unsigned cpf = threadIdx.x;
  {
    u64 pc;
    unsigned len;
    asm volatile(
        "s_cmp_lt_u32 %[w], 4\n\ts_cbranch_scc1 .Lnocpf\n\t"
        "s_getpc_b64 %[pc]\n.Lcode_here:\n\tv_xor_b32 %[c], %[hi], %[c]\n\tv_lshlrev_b32 %[c], 6, %[c]\n\t"
        "s_mov_b32 %[len], .Lcode_last-.Lcode_here-4\n\ts_max_i32 %[len], %[len], .Lcode_lastb-.Lcode_here-4\n\t"
        "v_min_u32 %[c], %[len], %[c]\n\tglobal_load_dword %[c], %[c], %[pc]\n.Lnocpf:"
        : [c] "+v"(cpf), [pc] "=&s"(pc), [len] "=&s"(len) : [w] "s"(w), [hi] "s"((b & 24) << 5) : "scc", "memory");
  }
  SCHED_FENCE();
  if (w >= 4)
    body<false>(lds, h, sp, w, x, kc, vc, wq, wo, w1, w3, w2, gw, out, cpf);
  else
    body<true>(lds, h, sp, w, x, kc, vc, wq, wo, w1, w3, w2, gw, out, cpf);
}

}

extern "C" void kernel_launch(void* const* d_in, const int* in_sizes, int n_in, void* d_out, int out_size, void* d_ws,
                              size_t ws_size, hipStream_t stream) {
  const bf16* x = (const bf16*)d_in[0];
  const bf16* kc = (const bf16*)d_in[1];
  const bf16* vc = (const bf16*)d_in[2];
  const bf16* wq = (const bf16*)d_in[3];
  const bf16* wo = (const bf16*)d_in[4];
  const bf16* w1 = (const bf16*)d_in[6];
  const bf16* w3 = (const bf16*)d_in[7];
  const bf16* w2 = (const bf16*)d_in[8];
  u64* gw = (u64*)d_ws;
  fused_decode<<<NWG, NT, 0, stream>>>(wq, w1, w3, gw, x, kc, vc, wo, w2, (bf16*)d_out);
}
```

```cpp
#include <hip/hip_runtime.h>
#include <hip/hip_bf16.h>
#include <stdint.h>

typedef __hip_bfloat16 bf16;
typedef unsigned long long u64;
typedef unsigned u32x4 __attribute__((ext_vector_type(4)));
typedef unsigned u32x8 __attribute__((ext_vector_type(8)));
typedef float f32x4 __attribute__((ext_vector_type(4)));
typedef float f32x2 __attribute__((ext_vector_type(2)));
typedef __bf16 bf16x2_t __attribute__((ext_vector_type(2)));

namespace {

constexpr int D = 640, HQ = 32, DH = 192, SK = 32768, WIN = 4096, FF = 1536;
constexpr int NSP = 8;
constexpr int NWG = HQ * NSP;
constexpr int NT = 512;
constexpr float SCALE = 0.07216878364870322f;
constexpr float LOG2E = 1.4426950408889634f;
constexpr unsigned SPIN_MAX = 1u << 16;
constexpr int SC1 = 16;
constexpr int NOWHERE = 1 << 30;

#define PV_ 81
#define PK_ 82
#define PX_ PK_
#define TS_ 358
#define SB_ 246
#define LOC 30
#define WT_ 98
#define W2T 132
#define W1N 3
#define PT_ 215
#define NFL 7
#define CE 7
#define CL 8
#define AR 3
#define GR 5
#define GRW 2
#define RSLEEP 1
#define MW 4
#define PM 0x1440
#define MEAS 1
#define ATHR 20
#define ASH 4
#define AGX 7
#define UTHR 8
#define UTH2 40
#define RTHR 99
#define USH 4
#define UGX 9
#define ULIM 20
#define UMISS 60
#define PVL 1
#define PB0 0
#define LSP 16
#define GLP 1
#define E1P 1
#define RSH 4
#define USA 0
#define HROT 16
constexpr int t_round(int r) { return r <= 2 ? TS_ : t_round(r - 1) + (r > 8 ? PV_ : r == 8 ? PX_ : PK_); }
constexpr int T_LAST = t_round(15);
constexpr int t_late(int i) { return t_round(i); }
constexpr int t_early(int i) { return t_round(i); }

constexpr int QS = 144;
constexpr int YB = 2 * (16 * 32 + 16);
constexpr int RS = 144;
constexpr size_t GQ = 0;
constexpr size_t G2 = GQ + (size_t)NWG * QS;
constexpr size_t GY = G2 + (size_t)NWG * RS;
constexpr size_t GZ = GY + 16;
constexpr size_t GEP = GY + (size_t)NSP * 5 * YB;
constexpr size_t GL = GEP + 16;

__device__ __forceinline__ unsigned fbits(float x) { return __builtin_bit_cast(unsigned, x); }
__device__ __forceinline__ u64 gld(const u64* p) {
  return __hip_atomic_load(p, __ATOMIC_RELAXED, __HIP_MEMORY_SCOPE_AGENT);
}
__device__ __forceinline__ float bf_lo(unsigned u) { return __builtin_bit_cast(float, u << 16); }
__device__ __forceinline__ float bf_hi(unsigned u) { return __builtin_bit_cast(float, u & 0xffff0000u); }
__device__ __forceinline__ float bfr(float x) { return __bfloat162float(__float2bfloat16(x)); }
__device__ __forceinline__ unsigned short bfbits(float x) {
  return __builtin_bit_cast(unsigned short, __float2bfloat16(x));
}
__device__ __forceinline__ float dot2(unsigned a, unsigned b, float c) {
  return __builtin_amdgcn_fdot2_f32_bf16(__builtin_bit_cast(bf16x2_t, a), __builtin_bit_cast(bf16x2_t, b), c, false);
}
template <int CTRL>
__device__ __forceinline__ float dpp(float x) {
  return __builtin_bit_cast(float, __builtin_amdgcn_mov_dpp(__builtin_bit_cast(int, x), CTRL, 0xf, 0xf, true));
}
__device__ __forceinline__ float grp8_sum(float x) {
  x += dpp<0xB1>(x);
  x += dpp<0x4E>(x);
  x += dpp<0x141>(x);
  return x;
}
__device__ __forceinline__ float sw16(float a, float b) {
  auto s = __builtin_amdgcn_permlane16_swap(fbits(a), fbits(b), false, false);
  return __builtin_bit_cast(float, (unsigned)s[0]) + __builtin_bit_cast(float, (unsigned)s[1]);
}
__device__ __forceinline__ float sw32(float a, float b) {
  auto s = __builtin_amdgcn_permlane32_swap(fbits(a), fbits(b), false, false);
  return __builtin_bit_cast(float, (unsigned)s[0]) + __builtin_bit_cast(float, (unsigned)s[1]);
}
__device__ __forceinline__ float xg_sum(float x) {
  x += dpp<0x128>(x);
  x = sw16(x, x);
  return sw32(x, x);
}
__device__ __forceinline__ float xg_max(float x) {
  x = fmaxf(x, dpp<0x128>(x));
  auto s = __builtin_amdgcn_permlane16_swap(fbits(x), fbits(x), false, false);
  x = fmaxf(__builtin_bit_cast(float, (unsigned)s[0]), __builtin_bit_cast(float, (unsigned)s[1]));
  auto t = __builtin_amdgcn_permlane32_swap(fbits(x), fbits(x), false, false);
  return fmaxf(__builtin_bit_cast(float, (unsigned)t[0]), __builtin_bit_cast(float, (unsigned)t[1]));
}
__device__ __forceinline__ float wave_sum(float x) { return xg_sum(grp8_sum(x)); }
__device__ __forceinline__ float ex2(float x) { return __builtin_amdgcn_exp2f(x); }
__device__ __forceinline__ float silu(float x) { return x / (1.0f + __expf(-x)); }
__device__ __forceinline__ float xg_reduce8(const float (&a)[8], bool g0) {
  float r[4];
#pragma unroll
  for (int i = 0; i < 4; ++i) r[i] = sw32(a[i], a[i + 4]);
  const float s0 = sw16(r[0], r[2]), s1 = sw16(r[1], r[3]);
  const float keep = g0 ? s1 : s0, send = g0 ? s0 : s1;
  return keep + dpp<0x128>(send);
}
__device__ __forceinline__ __amdgpu_buffer_rsrc_t rsrc(const void* p, unsigned bytes) {
  return __builtin_amdgcn_make_buffer_rsrc(const_cast<void*>(p), 0, bytes, 0x00020000);
}
__device__ __forceinline__ void gst16(u64* p, unsigned v0, unsigned v1, unsigned tag) {
  const u32x4 o = {v0, tag, v1, tag};
  asm volatile("global_store_dwordx4 %0, %1, off sc1\n\ts_nop 1" ::"v"(p), "v"(o) : "memory");
}
__device__ __forceinline__ unsigned bfpack(float a, float b) {
  return __builtin_bit_cast(unsigned, __builtin_convertvector(f32x2{a, b}, bf16x2_t));
}
__device__ __forceinline__ float2 rows_reduce(const float (&v)[8]) {
  float r[4];
#pragma unroll
  for (int i = 0; i < 4; ++i) r[i] = sw32(v[i], v[i + 4]);
  return float2{sw16(r[0], r[2]), sw16(r[1], r[3])};
}
template <int OFF>
__device__ __forceinline__ void xg_store(const float (&v)[8], float f, unsigned a) {
  float p[4], q[4], t, u;
#pragma unroll
  for (int i = 0; i < 4; ++i) {
    auto s = __builtin_amdgcn_permlane32_swap(fbits(v[i]), fbits(v[i + 4]), false, false);
    p[i] = __builtin_bit_cast(float, (unsigned)s[0]);
    q[i] = __builtin_bit_cast(float, (unsigned)s[1]);
  }
  const f32x2 r01 = f32x2{p[0], p[1]} + f32x2{q[0], q[1]}, r23 = f32x2{p[2], p[3]} + f32x2{q[2], q[3]};
  auto s0 = __builtin_amdgcn_permlane16_swap(fbits(r01[0]), fbits(r23[0]), false, false);
  auto s1 = __builtin_amdgcn_permlane16_swap(fbits(r01[1]), fbits(r23[1]), false, false);
  asm volatile("v_add_f32 %0, %2, %3\n\tv_add_f32 %1, %4, %5\n\ts_nop 0\n\t"
               "v_add_f32_dpp %0, %0, %0 row_ror:8 row_mask:0xf bank_mask:0x3\n\t"
               "v_add_f32_dpp %0, %1, %1 row_ror:8 row_mask:0xf bank_mask:0xc\n\t"
               "v_mul_f32 %0, %6, %0\n\tds_write_b32 %7, %0 offset:%8"
               : "=&v"(t), "=&v"(u)
               : "v"((unsigned)s0[0]), "v"((unsigned)s0[1]), "v"((unsigned)s1[0]), "v"((unsigned)s1[1]), "v"(f), "v"(a), "n"(OFF)
               : "memory");
}
#define SUM8(a)                                                                                                          \
  "ds_read_b128 v[116:119], " a "\n\tds_read_b128 v[120:123], " a " offset:768\n\t"                                      \
  "ds_read_b128 v[124:127], " a " offset:1536\n\tds_read_b128 v[128:131], " a " offset:2304\n\t"                         \
  "ds_read_b128 v[132:135], " a " offset:3072\n\tds_read_b128 v[136:139], " a " offset:3840\n\t"                         \
  "ds_read_b128 v[140:143], " a " offset:4608\n\tds_read_b128 v[144:147], " a " offset:5376\n\ts_waitcnt lgkmcnt(6)\n\t"
#define SUM8B                                                                                                            \
  "v_pk_add_f32 v[116:117], v[116:117], v[120:121]\n\tv_pk_add_f32 v[118:119], v[118:119], v[122:123]\n\t"               \
  "s_waitcnt lgkmcnt(4)\n\t"                                                                                             \
  "v_pk_add_f32 v[124:125], v[124:125], v[128:129]\n\tv_pk_add_f32 v[126:127], v[126:127], v[130:131]\n\t"               \
  "v_pk_add_f32 v[116:117], v[116:117], v[124:125]\n\tv_pk_add_f32 v[118:119], v[118:119], v[126:127]\n\t"
#define SUM8C                                                                                                            \
  "s_waitcnt lgkmcnt(2)\n\t"                                                                                             \
  "v_pk_add_f32 v[132:133], v[132:133], v[136:137]\n\tv_pk_add_f32 v[134:135], v[134:135], v[138:139]\n\t"               \
  "s_waitcnt lgkmcnt(0)\n\t"                                                                                             \
  "v_pk_add_f32 v[140:141], v[140:141], v[144:145]\n\tv_pk_add_f32 v[142:143], v[142:143], v[146:147]\n\t"               \
  "v_pk_add_f32 v[132:133], v[132:133], v[140:141]\n\tv_pk_add_f32 v[134:135], v[134:135], v[142:143]\n\t"               \
  "v_pk_add_f32 v[116:117], v[116:117], v[132:133]\n\tv_pk_add_f32 v[118:119], v[118:119], v[134:135]\n\t"
#define BC12 " row_newbcast:12 row_mask:0xf bank_mask:0xf\n\t"
#define DOTB(y, a, n) "v_dot2c_f32_bf16_dpp v" #y ", v124, v" #a " row_newbcast:" #n " row_mask:0xf bank_mask:0xf\n\t"
#define DOT8(a, b, c, d, e, f, g, h, n) \
  DOTB(160, a, n) DOTB(161, b, n) DOTB(162, c, n) DOTB(163, d, n) DOTB(164, e, n) DOTB(165, f, n) DOTB(166, g, n) DOTB(167, h, n)
#define DOTF(y, a, n) "v_dot2c_f32_bf16 v" #y ", v" #n ", v" #a "\n\t"
#define DOT8F(a, b, c, d, e, f, g, h, n) \
  DOTF(160, a, n) DOTF(161, b, n) DOTF(162, c, n) DOTF(163, d, n) DOTF(164, e, n) DOTF(165, f, n) DOTF(166, g, n) DOTF(167, h, n)
#define YRED                                                                                                             \
  "v_permlane32_swap_b32 v160, v164\n\tv_permlane32_swap_b32 v161, v165\n\tv_permlane32_swap_b32 v162, v166\n\t"         \
  "v_permlane32_swap_b32 v163, v167\n\tv_pk_add_f32 v[160:161], v[160:161], v[164:165]\n\t"                              \
  "v_pk_add_f32 v[162:163], v[162:163], v[166:167]\n\ts_nop 1\n\t"                                                       \
  "v_permlane16_swap_b32 v160, v162\n\tv_permlane16_swap_b32 v161, v163\n\t"
#define CLOB_T                                                                                                     \
  "v112", "v113", "v114", "v115", "v116", "v117", "v118", "v119", "v120", "v121", "v122", "v123", "v124", "v125",        \
      "v126", "v127", "v128", "v129", "v130", "v131", "v132", "v133", "v134", "v135", "v136", "v137", "v138", "v139",    \
      "v140", "v141", "v142", "v143", "v144", "v145", "v146", "v147"
#define SCHED_FENCE() __builtin_amdgcn_sched_barrier(0)
#define STR2(x) #x
#define STR(x) STR2(x)
#define THRN(n) do { SCHED_FENCE(); asm volatile("s_waitcnt vmcnt(%0)" ::"n"(n) : "memory"); SCHED_FENCE(); } while (0)
#define NOWT() ((unsigned)__builtin_amdgcn_s_memrealtime())
template <int N>
struct IC {
  static constexpr int v = N;
};
template <int I, int E, class F>
__device__ __forceinline__ void sfor(F&& f) {
  if constexpr (I < E) {
    f(IC<I>{});
    sfor<I + 1, E>(f);
  }
}
#define LDS_DRAIN() asm volatile("s_waitcnt lgkmcnt(0)" ::: "memory")
__device__ __forceinline__ unsigned lds_add(float* lds, int idx) {
  return __hip_atomic_fetch_add(reinterpret_cast<unsigned*>(lds + idx), 1u, __ATOMIC_RELAXED, __HIP_MEMORY_SCOPE_WORKGROUP);
}
__device__ __forceinline__ void lds_wait(float* lds, int idx, unsigned n) {
  SCHED_FENCE();
#pragma unroll 1
  for (unsigned spin = 0; spin < (SPIN_MAX << 4); ++spin) {
    if (__hip_atomic_load(reinterpret_cast<unsigned*>(lds + idx), __ATOMIC_RELAXED, __HIP_MEMORY_SCOPE_WORKGROUP) >= n) break;
    __builtin_amdgcn_s_sleep(1);
  }
  asm volatile("" ::: "memory");
  SCHED_FENCE();
}
template <int T, int R>
__device__ __forceinline__ void pace(unsigned clk, int ph) {
  unsigned o;
  SCHED_FENCE();
  asm volatile(
      "s_branch .Lpb%=\n"
      ".Lpa%=:\n\ts_sleep 1\n"
      ".Lpb%=:\n\tds_read_b32 %0, %1 offset:%4\n\ts_memrealtime s[98:99]\n\ts_waitcnt lgkmcnt(0)\n\tv_readfirstlane_b32 s99, %0\n\t"
      "s_sub_u32 s98, s98, %2\n\ts_sub_u32 s98, s98, s99\n\ts_cmp_lt_i32 s98, %3\n\ts_cbranch_scc1 .Lpa%="
      : "=&v"(o) : "v"(clk), "s"(ph), "n"(T), "n"(4 * R) : "s98", "s99", "scc", "memory");
  SCHED_FENCE();
}

template <int T, int R>
__device__ __forceinline__ unsigned pace_m(unsigned clk, int ph) {
  unsigned o, tl;
  SCHED_FENCE();
  asm volatile(
      "s_mov_b32 %1, 0\n\ts_branch .Lpb%=\n"
      ".Lpa%=:\n\ts_sleep 1\n"
      ".Lpb%=:\n\tds_read_b32 %0, %2 offset:%5\n\ts_getreg_b32 s97, hwreg(HW_REG_IB_STS, 0, 4)\n\ts_memrealtime s[98:99]\n\t"
      "s_or_b32 s97, s97, %1\n\ts_waitcnt lgkmcnt(0)\n\ts_cselect_b32 %1, %1, s98\n\tv_readfirstlane_b32 s99, %0\n\t"
      "s_sub_u32 s98, s98, %3\n\ts_sub_u32 s98, s98, s99\n\ts_cmp_lt_i32 s98, %4\n\ts_cbranch_scc1 .Lpa%="
      : "=&v"(o), "=&s"(tl) : "v"(clk), "s"(ph), "n"(T), "n"(4 * R) : "s97", "s98", "s99", "scc", "memory");
  SCHED_FENCE();
  return tl;
}

#define STAMP(i)
#define STAMP_FLUSH()
#define TT(i)
constexpr int L_QP = 0;
constexpr int L_QS = L_QP + 4 * 128;
constexpr int L_PART = L_QS + 96;
constexpr int L_ML = L_PART + 8 * DH;
constexpr int L_Z = L_ML + 16;
constexpr int L_Y = L_Z + 320;
constexpr int L_H = L_Y + 512;
constexpr int L_SY = L_H + 64;
constexpr int L_CK = L_SY + 8;
constexpr int L_END = L_CK + 16;

#define PIN(x) asm volatile("" : "+v"(x))

__device__ __forceinline__ void wg_ml(const float* lds, float& M, float& L) {
  float mm[8], ll[8];
#pragma unroll
  for (int i = 0; i < 4; ++i) {
    const f32x4 t = *reinterpret_cast<const f32x4*>(lds + L_ML + 4 * i);
    mm[2 * i] = t[0];
    ll[2 * i] = t[1];
    mm[2 * i + 1] = t[2];
    ll[2 * i + 1] = t[3];
  }
  M = mm[0];
#pragma unroll
  for (int ww = 1; ww < 8; ++ww) M = fmaxf(M, mm[ww]);
  L = 0.f;
#pragma unroll
  for (int ww = 0; ww < 8; ++ww) L = fmaf(ex2((mm[ww] - M) * LOG2E), ll[ww], L);
}
__device__ __forceinline__ unsigned lds_addr(const float* p) {
  return (unsigned)(size_t)(const __attribute__((address_space(3))) float*)p;
}

template <bool early>
__device__ __forceinline__ void body(float* lds, const int h, const int sp, const int w, const bf16* __restrict__ x,
                                     const bf16* __restrict__ kc, const bf16* __restrict__ vc,
                                     const bf16* __restrict__ wq, const bf16* __restrict__ wo,
                                     const bf16* __restrict__ w1, const bf16* __restrict__ w3,
                                     const bf16* __restrict__ w2, u64* __restrict__ gw, bf16* __restrict__ out,
                                     unsigned cpf) {
  const int b = blockIdx.x;
  const int tid = threadIdx.x, lane = tid & 63;
  const int g = lane >> 3, c = lane & 7;
  const int rec = h * NSP + sp;

  STAMP(0);
  const bool isF = sp < 6;
  u32x4 PR[8], xa, xb, xq;
  unsigned epoch = 0, tk = 0;
  int ra = 0, RA0 = 0;
  bool va = false;
  if constexpr (early) {
    int s0_ = 240, s1_ = 80, s2_ = 240 * (sp & 1), s3_ = 80 * sp, s4_ = 64 * (sp >> 1), s5_ = 64 * w, s6_ = 24 * w;
    asm("" : "+s"(s0_), "+s"(s1_), "+s"(s2_), "+s"(s3_), "+s"(s4_), "+s"(s5_), "+s"(s6_));
    const int rw0 = isF ? s5_ : s6_;
    int lim = (isF ? s0_ : s1_) - rw0, lim2 = isF ? 64 : 24;
    asm("" : "+s"(lim), "+s"(lim2));
    lim = lim < lim2 ? lim : lim2;
    ra = rw0 + 8 * g;
    va = 8 * g < lim;
    RA0 = isF ? s2_ : s3_;
    int aoff = (int)__umul24(g, 8 * HQ * DH * 2) + 16 * c;
    PIN(aoff);
    aoff = va ? aoff : NOWHERE;
    int sa = ((RA0 + rw0) * (HQ * DH) + h * DH + (isF ? s4_ : 64)) * 2;
    PIN(aoff);
    asm volatile("" : "+s"(sa));
    SCHED_FENCE();
    const __amdgpu_buffer_rsrc_t rw = rsrc(wq, NOWHERE);
    PR[0] = __builtin_amdgcn_raw_buffer_load_b128(rw, aoff, sa, 2);
    SCHED_FENCE();
    tk = NOWT();
    SCHED_FENCE();
    asm volatile("s_load_dword %0, %1, %2" : "=&s"(epoch) : "s"(gw), "s"(E1P && w == 1 ? (int)GL * 8 + (sp * 8 + (b & 7)) * LSP : (int)GEP * 8) : "memory");
    SCHED_FENCE();
#pragma unroll
    for (int i = 1; i < 8; ++i) PR[i] = __builtin_amdgcn_raw_buffer_load_b128(rw, aoff, sa + i * (HQ * DH * 2), 2);
    SCHED_FENCE();
  }
  const unsigned short* xs = reinterpret_cast<const unsigned short*>(x);
  const size_t wgb = ((size_t)h * SK + (SK - WIN)) * DH;
  const __amdgpu_buffer_rsrc_t rk = rsrc(kc + wgb, WIN * DH * 2);
  const __amdgpu_buffer_rsrc_t rv = rsrc(vc + wgb, WIN * DH * 2);
  const int voff = (g * DH + 8 * c) * 2;
  auto tile = [&](int t) { return (t * 64 + h * HROT + w * 8 + sp) & 511; };
  u32x4 K[8][3], V[8][3];
  auto kld = [&](int t, int j) { K[t][j] = __builtin_amdgcn_raw_buffer_load_b128(rk, voff + 128 * j, tile(t) * 3072, 2); };
  auto vld = [&](int t, int j) { V[t][j] = __builtin_amdgcn_raw_buffer_load_b128(rv, voff + 128 * j, tile(t) * 3072, 2); };
  unsigned tag = 0;
  const unsigned clk = lds_addr(lds + L_CK);
  const int ph = (w * PV_) >> 3;
  const int r4 = lane >> 4, c16 = lane & 15;
  const int ch = c16 < 10 ? c16 : 9;
  const int fi = (h & 3) * 6 + (isF ? sp : 5), gl = fi >> 3, gI = (h >> 2) * 3 + gl, fr = fi & 7;
  u32x4 WO[6], X[6], W2[4];
  unsigned tch = 0;
  const int wooff = ((48 * w + r4 + (isF ? 24 * (g & 1) : 24)) * D + 64 * sp + 8 * c + (isF ? 0 : 128 * (g & 1))) * 2;
  typedef unsigned u32x2 __attribute__((ext_vector_type(2)));
  const __amdgpu_buffer_rsrc_t rl = rsrc(gw + GL, 16 * 64 * LSP);
  const bool pub = !((sp + (b >> 6)) & 3);
  const int lw_ = lane == 0 && pub ? (sp * 8 + (b & 7)) * LSP : NOWHERE;
  u32x2 lat = {0u, 0u};
  u64 own = 0, pre = 0;
  unsigned pro = 0, st1 = 0, old = 0;
  bool vslow = false;
  if constexpr (early) {
    u32x4 PF[5];
    const int F0 = (isF ? 160 * (fr >> 1) : 480) + 40 * w;
    const int fs = isF ? FF : HQ * DH;
    int s6_ = 64 * gI, s7_ = h * DH + 128 * (sp & 1);
    asm("" : "+s"(s6_), "+s"(s7_));
    const int boff = ((int)__umul24(g, fs) + 8 * c) * 2;
    const int sb = (F0 * fs + (isF ? s6_ : s7_)) * 2;
    const bf16* pf_ = (fr & 1) ? w3 : w1;
    asm("" : "+s"(pf_));
    const __amdgpu_buffer_rsrc_t rf = rsrc(isF ? pf_ : wq, NOWHERE);
    unsigned short xr[5];
#pragma unroll
    for (int i = 0; i < 5; ++i) PF[i] = __builtin_amdgcn_raw_buffer_load_b128(rf, boff, sb + i * 16 * fs, 2);
    SCHED_FENCE();
    if (!isF) {
#pragma unroll
      for (int i = 0; i < 6; ++i)
        X[i] = __builtin_amdgcn_raw_buffer_load_b128(rsrc(wo + (size_t)h * DH * D, DH * D * 2), wooff - 24 * D * 2 + i * (4 * D * 2), 0, 2);
    } else {
#pragma unroll
      for (int i = 0; i < 6; ++i) X[i] = u32x4{0u, 0u, 0u, 0u};
    }
    SCHED_FENCE();
    xa = *reinterpret_cast<const u32x4*>(x + 8 * lane);
    xb = *reinterpret_cast<const u32x4*>(x + 512 + 8 * (lane & 15));
    xq = *reinterpret_cast<const u32x4*>(x + RA0 + (va ? ra : 0));
#pragma unroll
    for (int i = 0; i < 5; ++i) xr[i] = xs[F0 + 8 * i + g];
    SCHED_FENCE();
    const unsigned epv = __hip_atomic_load(reinterpret_cast<unsigned*>(gw + GEP), __ATOMIC_RELAXED, __HIP_MEMORY_SCOPE_AGENT);
    SCHED_FENCE();
    if (tid < 16) reinterpret_cast<unsigned*>(lds)[L_CK + tid] = tk;
    asm volatile("s_waitcnt lgkmcnt(0)\n\ts_barrier" : "+s"(epoch)::"memory");
    STAMP(6);
    SCHED_FENCE();
#pragma unroll
    for (int t = 0; t < 3; ++t)
#pragma unroll
      for (int j = 0; j < 3; ++j) kld(t, j);
    SCHED_FENCE();
    float mb, den;
    {
      float xf[8], xg[8];
#pragma unroll
      for (int i = 0; i < 4; ++i) {
        xf[2 * i] = bf_lo(xa[i]);
        xf[2 * i + 1] = bf_hi(xa[i]);
        xg[2 * i] = bf_lo(xb[i]);
        xg[2 * i + 1] = bf_hi(xb[i]);
      }
      const bool lo16 = lane < 16;
      float sum = 0.f;
#pragma unroll
      for (int i = 0; i < 8; ++i) sum += xf[i] + (lo16 ? xg[i] : 0.f);
      sum = wave_sum(sum);
      const float mean = sum * (1.0f / D);
      float sq = 0.f;
#pragma unroll
      for (int i = 0; i < 8; ++i) {
        const float d1 = xf[i] - mean, d2 = xg[i] - mean;
        sq += d1 * d1 + (lo16 ? d2 * d2 : 0.f);
      }
      sq = wave_sum(sq);
      mb = bfr(mean);
      const float vb = bfr(sq * (1.0f / D));
      den = bfr(sqrtf(bfr(vb + 1e-5f)));
    }
    {
      unsigned xn[13];
#pragma unroll
      for (int i = 0; i < 4; ++i) {
        xn[2 * i] = fbits(bfr(bfr(bf_lo(xq[i]) - mb) / den));
        xn[2 * i + 1] = fbits(bfr(bfr(bf_hi(xq[i]) - mb) / den));
      }
#pragma unroll
      for (int i = 0; i < 5; ++i) xn[8 + i] = fbits(bfr(bfr(bf_lo(xr[i]) - mb) / den));
#pragma unroll
      for (int i = 0; i < 13; ++i) PIN(xn[i]);
      SCHED_FENCE();
      pace<t_round(2), 2>(clk, ph);
#pragma unroll
      for (int j = 0; j < 3; ++j) kld(3, j);
      TT(2);
      SCHED_FENCE();
      float fa[8], fg[8];
#pragma unroll
      for (int k = 0; k < 8; ++k) fa[k] = fg[k] = 0.f;
#pragma unroll
      for (int i = 0; i < 8; ++i)
#pragma unroll
        for (int k = 0; k < 4; ++k) {
          fa[2 * k] = dot2(PR[i][k], xn[i] >> 16, fa[2 * k]);
          fa[2 * k + 1] = dot2(PR[i][k], xn[i], fa[2 * k + 1]);
        }
      lds[L_QP + w * 128 + 8 * c + g] = xg_reduce8(fa, g & 1);
#pragma unroll
      for (int i = 0; i < 5; ++i)
#pragma unroll
        for (int k = 0; k < 4; ++k) {
          fg[2 * k] = dot2(PF[i][k], xn[8 + i] >> 16, fg[2 * k]);
          fg[2 * k + 1] = dot2(PF[i][k], xn[8 + i], fg[2 * k + 1]);
        }
      lds[L_QP + w * 128 + 64 + 8 * c + g] = xg_reduce8(fg, g & 1);
    }
    SCHED_FENCE();
    tag = __builtin_amdgcn_readfirstlane(epv) + 1u;
    if (tid == 0) reinterpret_cast<unsigned*>(lds)[L_SY + 3] = tag;
    LDS_DRAIN();
    if (lane == 0) old = lds_add(lds, L_SY);
    old = __builtin_amdgcn_readfirstlane(old);
    if (old == 3) {
      const float2* qp = reinterpret_cast<const float2*>(lds + L_QP) + lane;
      const float2 p0_ = qp[0], p1_ = qp[64], p2_ = qp[128], p3_ = qp[192];
      gst16(gw + GQ + (size_t)rec * QS + 2 * lane, fbits((p0_.x + p1_.x) + (p2_.x + p3_.x)),
            fbits((p0_.y + p1_.y) + (p2_.y + p3_.y)), tag);
    }
    STAMP(11);
  } else {
    if (tid >= NT - 8) reinterpret_cast<unsigned*>(lds)[L_SY + tid - (NT - 8)] = 0u;
    unsigned st0 = 0;
    {
      const bf16* tp = w == 4 ? kc + wgb : w == 5 ? (!isF ? wo + (size_t)h * DH * D : (fr & 1) ? w3 : w1) : w == 6 ? ((sp & 1) ? x : wq) : vc + wgb;
      asm volatile("s_waitcnt lgkmcnt(0)\n\ts_load_dword %0, %1, 0x0" : "=&s"(st0) : "s"(tp) : "memory");
    }
    SCHED_FENCE();
#pragma unroll
    for (int j = 0; j < 3; ++j) kld(0, j);
    SCHED_FENCE();
    asm volatile("s_barrier" ::: "memory");
    STAMP(6);
    SCHED_FENCE();
    if (w < 6) {
      const int l = lane < 42 ? lane : 41, k = l & 15;
      const int yg = 4 * (k < 8 || isF ? sp : sp + 2) + ((k >> 1) & 3);
      const size_t yo = l < 32 || !isF ? GY + (size_t)yg * YB + 32 * h + 16 * (h >> 4) + 8 * (k & 1)
                                       : GZ + (size_t)(fr * 5 + (k >> 1)) * YB + 32 * gI + 16 * (gI >> 4) + 8 * (k & 1);
      unsigned o4 = (unsigned)(l < 16 ? G2 + (size_t)rec * RS + 8 * l : yo);
      unsigned o5 = (unsigned)(GQ + (size_t)rec * QS + 8 * (lane < 16 ? lane : 15));
      if (GLP && pub && lane >= 16 && lane < 32 && ((PM >> (lane - 16)) & 1))
        o5 = (unsigned)GL + (unsigned)(((lane - 16) * 64 + sp * 8 + (b & 7)) * (LSP / 8));
      PIN(o4);
      PIN(o5);
      pre = gld(gw + (w == 4 ? o4 : o5));
    }
    if (w == 7 && h < 5)
      pro = __hip_atomic_load(reinterpret_cast<const unsigned*>(out + 80 * sp + 16 * h) + (lane & 7), __ATOMIC_RELAXED,
                              __HIP_MEMORY_SCOPE_AGENT);
    SCHED_FENCE();
    asm volatile("s_waitcnt lgkmcnt(0)" ::"s"(st0) : "memory");
#pragma unroll
    for (int j = 0; j < 3; ++j) {
      kld(1, j);
      THRN(5);
    }
    THRN(3);
    TT(1);
    own = __builtin_amdgcn_s_memrealtime() - SB_;
    __builtin_amdgcn_raw_buffer_store_b64(u32x2{(unsigned)own, (unsigned)(own >> 32)}, rl, w == 6 ? lw_ : NOWHERE, 0, SC1);
    SCHED_FENCE();
    if (LOC && w == 6 && lane < 16) {
      const unsigned o = (unsigned)own - LOC;
      if ((int)(o - reinterpret_cast<unsigned*>(lds)[L_CK + lane]) > 0) reinterpret_cast<unsigned*>(lds)[L_CK + lane] = o;
    }
    SCHED_FENCE();
    {
      const bf16* tp = (w & 1) ? wo + (size_t)h * DH * D : w2 + (size_t)gI * 64 * D;
      asm volatile("s_load_dword %0, %1, 0x0" : "=&s"(st1) : "s"(tp) : "memory");
    }
    SCHED_FENCE();
  }
  u32x4 qf[3];
  float sc[8], mw, lw, acc[24];
  unsigned pb[8];
  auto score = [&](int t) {
    float d = 0.f;
#pragma unroll
    for (int j = 0; j < 3; ++j)
#pragma unroll
      for (int i = 0; i < 4; ++i) d = dot2(K[t][j][i], qf[j][i], d);
    d = grp8_sum(d);
    sc[t] = bfr(bfr(d) * SCALE);
  };
  auto pv = [&](int t) {
    const unsigned plo = pb[t], phi = plo << 16;
#pragma unroll
    for (int j = 0; j < 3; ++j)
#pragma unroll
      for (int i = 0; i < 4; ++i) {
        acc[8 * j + 2 * i] = dot2(V[t][j][i], plo, t ? acc[8 * j + 2 * i] : 0.f);
        acc[8 * j + 2 * i + 1] = dot2(V[t][j][i], phi, t ? acc[8 * j + 2 * i + 1] : 0.f);
        PIN(acc[8 * j + 2 * i]);
        PIN(acc[8 * j + 2 * i + 1]);
      }
  };
  const bool hf = lane >> 5;
  const __amdgpu_buffer_rsrc_t rq_ = rsrc(gw + GQ, NWG * QS * 8);
  int go[9];
  {
    const int l16 = (lane & 31) * 16;
    const int qa = l16 + (8 * h + (hf ? 4 : 0)) * (QS * 8);
    go[0] = qa;
    go[1] = qa + QS * 8;
    go[2] = l16 + (8 * h + (hf ? 7 : 6)) * (QS * 8) + 512;
    go[3] = qa + 2 * QS * 8;
    go[4] = qa + 3 * QS * 8;
#pragma unroll
    for (int k = 0; k < 4; ++k) {
      const int m = 8 * gl + 2 * k + (hf ? 1 : 0);
      go[5 + k] = isF ? l16 + (32 * (h >> 2) + m + (((m * 43) >> 7) & 6)) * (QS * 8) + 512 : NOWHERE;
    }
  }
  const bool gath = old == GRW;
  u32x4 pa[9];
  constexpr int C0 = early ? CE : CL;
  static_assert(C0 + 3 < 15 && C0 > GR + 1 && C0 >= 6, "schedule");
  sfor<early ? 3 : 2, 16>([&](auto R) {
    constexpr int r = R.v, t = r >= 8 ? r - 8 : r + early;
    unsigned tl = 0;
    if constexpr (!early && MEAS && ((PM >> r) & 1)) tl = pace_m<t_round(r), r>(clk, ph); else pace<t_round(r), r>(clk, ph);
    if constexpr (!early && ((PM >> (r - 1)) & 1)) {
      lat = __builtin_amdgcn_raw_buffer_load_b64(rl, w == MW ? lane * LSP : NOWHERE, 64 * LSP * (r - 1), SC1);
      SCHED_FENCE();
    }
    if constexpr (r < 8 && t == 8) {
      const __amdgpu_buffer_rsrc_t rx = rsrc(w2 + ((size_t)gI * 64 + 16 * w) * D + 80 * fr, isF ? 16 * D * 2 : 0);
#pragma unroll
      for (int i = 0; i < 4; ++i)
        W2[i] = __builtin_amdgcn_raw_buffer_load_b128(rx, (r4 * D + 8 * ch) * 2 + i * (4 * D * 2), 0, 2);
      tch = __builtin_amdgcn_raw_buffer_load_b32(rsrc(wo + (size_t)h * DH * D, DH * D * 2), tid ? NOWHERE : wooff, 0, 2);
    } else {
#pragma unroll
      for (int j = 0; j < 3; ++j) {
        if (r >= 8) vld(t, j); else kld(t, j);
        if (r == 15 && j == 2) break;
        if (early && r == GR + 1) continue;
        THRN(early && r == 8 ? NFL + 2 : NFL);
      }
    }
    TT(r);
    SCHED_FENCE();
    if constexpr (early) {
      if (r == GR && gath) {
#pragma unroll
        for (int s = 0; s < 9; ++s) pa[s] = __builtin_amdgcn_raw_buffer_load_b128(rq_, go[s], 0, SC1);
        SCHED_FENCE();
      }
      if (r == GR + 1) {
        if (gath) {
          const unsigned tf = isF ? tag : 0u;
          bool ok = true;
#pragma unroll
          for (int s = 0; s < 9; ++s) ok = ok & (pa[s][1] == (s < 5 ? tag : tf)) & (pa[s][3] == (s < 5 ? tag : tf));
          if (!__all(ok)) {
            for (unsigned spin = 0;; ++spin) {
              ok = true;
              asm volatile("" ::: "memory");
#pragma unroll
              for (int s = 0; s < 9; ++s) {
                pa[s] = __builtin_amdgcn_raw_buffer_load_b128(rq_, go[s], 0, SC1);
                ok = ok & (pa[s][1] == (s < 5 ? tag : tf)) & (pa[s][3] == (s < 5 ? tag : tf));
              }
              if (__all(ok) || spin >= SPIN_MAX) break;
              __builtin_amdgcn_s_sleep(1);
            }
          }
          float qo[2], qm[2], hu[2];
#pragma unroll
          for (int k = 0; k < 2; ++k) {
            auto val = [&](int s) { return __builtin_bit_cast(float, (unsigned)pa[s][2 * k]); };
            qo[k] = (val(0) + val(1)) + val(2);
            const float t_ = val(3) + val(4);
            qm[k] = sw32(t_, t_);
            const float u = bfr((val(5) + val(6)) + (val(7) + val(8)));
            auto sw = __builtin_amdgcn_permlane32_swap(fbits(u), fbits(u), false, false);
            hu[k] = bfr(bfr(silu(__builtin_bit_cast(float, (unsigned)sw[0]))) * __builtin_bit_cast(float, (unsigned)sw[1]));
          }
          reinterpret_cast<unsigned*>(lds + L_QS)[lane + (hf ? 32 : 0)] = bfpack(qo[0], qo[1]);
          if (hf) {
            reinterpret_cast<unsigned*>(lds + L_QS)[lane] = bfpack(qm[0], qm[1]);
          } else {
            *reinterpret_cast<float2*>(lds + L_H + 2 * lane) = float2{hu[0], hu[1]};
          }
          LDS_DRAIN();
          if (lane == 0) lds_add(lds, L_SY + 2);
        }
        STAMP(2);
      }
    } else {
      if (r == 2) asm volatile("" ::"s"(st1), "v"(cpf), "v"(pre), "v"(pro));
      if (r == AR) lat = __builtin_amdgcn_raw_buffer_load_b64(rl, w == 6 ? lane * LSP : NOWHERE, 0, SC1);
      if (r == AR + 1) {
        const u64 d = (((u64)lat[1] << 32) | lat[0]) - own + 200u;
        const bool ok = d <= 400u;
        const float s_ = wave_sum(ok ? (float)(unsigned)d : 0.f), n = (float)__builtin_popcountll(__ballot(ok));
        const unsigned o = (unsigned)own + (n > 0.f ? (unsigned)(s_ * __builtin_amdgcn_rcpf(n)) - 200u : 0u);
        if (w == 6 && lane < 16) reinterpret_cast<unsigned*>(lds)[L_CK + lane] = o;
      }
      if ((PM >> r) & 1) {
        const unsigned org = __builtin_amdgcn_readfirstlane(reinterpret_cast<const unsigned*>(lds)[L_CK + r]);
        tag = __builtin_amdgcn_readfirstlane(reinterpret_cast<const unsigned*>(lds)[L_SY + 3]);
        THRN(3);
        const unsigned now = NOWT() - 9;
        const int l = (int)((tl ? tl : now) - org) - (t_round(r) + ph);
        __builtin_amdgcn_raw_buffer_store_b64(u32x2{(unsigned)(l < -64 ? 0 : l + 64), tag}, rl, w == MW && l < ULIM ? lw_ : NOWHERE,
                                              64 * LSP * r, SC1);
      }
      SCHED_FENCE();
    }
    if (r == C0) {
      lds_wait(lds, L_SY + 2, 1u);
      STAMP(3);
#pragma unroll
      for (int j = 0; j < 3; ++j) qf[j] = *reinterpret_cast<const u32x4*>(lds + L_QS + (c + 8 * j) * 4);
#pragma unroll
      for (int k = 0; k < 4; ++k) score(k);
    } else if (r == C0 + 1) {
#pragma unroll
      for (int k = 4; k < 7; ++k) score(k);
    } else if (r == C0 + 2) {
      score(7);
      mw = sc[0];
#pragma unroll
      for (int k = 1; k < 8; ++k) mw = fmaxf(mw, sc[k]);
      mw = xg_max(mw);
      lw = 0.f;
#pragma unroll
      for (int k = 0; k < 8; ++k) {
        const float p = ex2((sc[k] - mw) * LOG2E);
        lw += p;
        pb[k] = bfbits(p);
      }
      lw = xg_sum(lw);
      if (lane == 0) {
        lds[L_ML + 2 * w] = mw;
        lds[L_ML + 2 * w + 1] = lw;
      }
      if (early) {
        float z[8];
#pragma unroll
        for (int n = 0; n < 8; ++n) z[n] = 0.f;
#pragma unroll
        for (int r = 0; r < 4; ++r) {
          const unsigned chi = fbits(lds[L_H + 16 * w + 4 * r + r4]), clo = chi >> 16;
#pragma unroll
          for (int n = 0; n < 4; ++n) {
            z[2 * n] = dot2(W2[r][n], clo, z[2 * n]);
            z[2 * n + 1] = dot2(W2[r][n], chi, z[2 * n + 1]);
          }
        }
        const float2 zr = rows_reduce(z);
        if (c16 < 10) *reinterpret_cast<float2*>(lds + L_Z + 80 * w + 8 * c16 + 2 * r4) = zr;
      }
      LDS_DRAIN();
      if (lane == 0) lds_add(lds, L_SY + 4);
      STAMP(4);
    } else if (r == C0 + 3) {
#pragma unroll
      for (int k = 0; k < t + 1 - PVL; ++k) pv(k);
    } else if (r > C0 + 3 && r < 15) {
      pv(t - PVL);
    }
    if constexpr (!early && ((PM >> (r - 1)) & 1)) {
      SCHED_FENCE();
      constexpr int p = r - 1;
      const float m = wave_sum((float)(lat[1] == tag && lat[0] < UMISS + 64 ? lat[0] : UMISS + 64)) * (1.f / 64) - 64.f;
      int d = 0;
      if (p <= 8) {
        if (m >= ATHR) d = (int)(m * (ASH / 4.f)) + AGX * (lane <= p ? 0 : lane < 8 ? lane - p : 8 - p);
      } else if (!vslow && m >= (p <= 10 ? UTHR : UTH2)) {
        vslow = true;
        d = (int)(fmaxf(m, 0.f) * (USH / 4.f)) + USA + (lane > p ? UGX * (lane - p) : 0);
      } else if (m >= RTHR) {
        d = (int)(m * (RSH / 4.f));
      }
      if (w == MW && lane < 16) reinterpret_cast<unsigned*>(lds)[L_CK + lane] += d;
    }
  });
  asm volatile("" ::"v"(tch));
  SCHED_FENCE();
  const __amdgpu_buffer_rsrc_t rwo = rsrc(wo + (size_t)h * DH * D, DH * D * 2);
  lds_wait(lds, L_SY + 4, 8u);
  float f, M, L;
  wg_ml(lds, M, L);
  f = ex2((mw - M) * LOG2E) / L;
  const unsigned cw = fbits(fmaf(M, LOG2E, __builtin_amdgcn_logf(L)));
  if (!early) tag = __builtin_amdgcn_readfirstlane(reinterpret_cast<const unsigned*>(lds)[L_SY + 3]);
  PIN(f);
  SCHED_FENCE();
  const int l40 = lane < 40 ? lane : 39;
  u64* rdst = gw + G2 + (size_t)rec * RS + 32 * r4 + 2 * c16;
  u64* zdst = gw + GZ + (size_t)(fr * 5 + (l40 >> 3)) * YB + 32 * gI + 16 * (gI >> 4) + 2 * (l40 & 7);
  unsigned pdst = lds_addr(lds + L_PART + w * DH + 8 * c + g), psrc = lds_addr(lds + L_PART + 48 * r4 + 4 * c16);
  unsigned cnt6 = lds_addr(lds + L_SY + 6), one = 1u;
  u32x4 rq = {cw, tag, cw, tag};
  u64 m48 = 0x0fff0fff0fff0fffull, m52 = 0x1fff1fff1fff1fffull;
  asm volatile("" : "+s"(m48), "+s"(m52), "+{v[156:159]}"(rq));
  PIN(rdst);
  PIN(zdst);
  PIN(pdst);
  PIN(psrc);
  PIN(cnt6);
  PIN(one);
#pragma unroll
  for (int k = 7 - PVL; k < 7; ++k) pv(k);
  auto wo1 = [&]() {
    if (WT_) pace<T_LAST + WT_, 15>(clk, ph);
    SCHED_FENCE();
#pragma unroll
    for (int i = 0; i < W1N; ++i) WO[i] = __builtin_amdgcn_raw_buffer_load_b128(rwo, wooff, i * (4 * D * 2), 2);
    SCHED_FENCE();
  };
  {
    unsigned plo = pb[7], phi = plo << 16;
    PIN(plo);
    PIN(phi);
    sfor<0, 3>([&](auto J) {
      constexpr int j = J.v;
      SCHED_FENCE();
      float a[8];
#pragma unroll
      for (int i = 0; i < 4; ++i) {
        a[2 * i] = dot2(V[7][j][i], plo, acc[8 * j + 2 * i]);
        a[2 * i + 1] = dot2(V[7][j][i], phi, acc[8 * j + 2 * i + 1]);
      }
      xg_store<256 * j>(a, f, pdst);
    });
    if (early) wo1();
  }
  STAMP(5);
  unsigned arr;
  asm volatile(
      "s_mov_b64 exec, 1\n\tds_add_rtn_u32 v112, %[cnt], %[one]\n\ts_mov_b64 exec, %[m48]\n\t" SUM8("%[src]")
      "v_readfirstlane_b32 %[arr], v112\n\t" SUM8B
      "s_cmp_eq_u32 %[arr], 7\n\t" SUM8C
      "v_cvt_pk_bf16_f32 v156, v116, v117\n\tv_cvt_pk_bf16_f32 v158, v118, v119\n\t"
      "s_cselect_b64 exec, %[m52], 0\n\tglobal_store_dwordx4 %[dst], v[156:159], off sc1\n\ts_mov_b64 exec, -1\n\ts_nop 0"
      : [arr] "=&s"(arr), "+{v[156:159]}"(rq)
      : [cnt] "v"(cnt6), [one] "v"(one), [m48] "s"(m48), [m52] "s"(m52), [src] "v"(psrc), [dst] "v"(rdst)
      : CLOB_T, "scc", "memory");
  if (arr == 0 && isF) {
    const float2* zp = reinterpret_cast<const float2*>(lds + L_Z) + l40;
    const float2 z0 = zp[0], z1 = zp[40], z2 = zp[80], z3 = zp[120];
    gst16(zdst, fbits((z0.x + z1.x) + (z2.x + z3.x)), fbits((z0.y + z1.y) + (z2.y + z3.y)), tag);
  }
  if constexpr (!early) {
    if (w == 7 && h < 5) {
      const int col = 80 * sp + 16 * h + 2 * c + (lane >> 5);
      const float xv = bf_lo(xs[col]);
      const u64* py = gw + GZ + (size_t)(sp * 5 + h) * YB + 32 * g + 2 * c + 392;
      bf16* po = out + col;
      float gt = 0.f;
      u64 m3 = 0ull, ms = 0ull;
      PIN(po);
#pragma unroll 1
      for (int pass = 0; pass < 2; ++pass) {
        lds_wait(lds, L_SY + (pass ? 1 : 6), pass ? 4u : 8u);
        STAMP(8);
        asm volatile("" : "+s"(m3), "+s"(ms));
        float t;
        unsigned n;
        asm volatile(
            "v_mov_b32 v124, 0\n\tv_mov_b32 v125, %[tag]\n\tv_mov_b32 v126, 0\n\tv_mov_b32 v127, %[tag]\n\t"
            "s_mov_b64 vcc, -1\n\ts_mov_b32 %[n], 0\n"
            ".Lfp%=:\n\t"
            "s_add_u32 %[n], %[n], 1\n\ts_cmp_gt_u32 %[n], %[max]\n\ts_cbranch_scc1 .Lfd%=\n\t"
            "s_mov_b64 exec, vcc\n\t"
            "global_load_dwordx4 v[112:115], %[p], off offset:-3136 sc1\n\t"
            "global_load_dwordx4 v[116:119], %[p], off offset:-1088 sc1\n\t"
            "global_load_dwordx4 v[120:123], %[p], off offset:1088 sc1\n\t"
            "s_and_b64 exec, vcc, %[m3]\n\t"
            "global_load_dwordx4 v[124:127], %[p], off offset:3136 sc1\n\t"
            "s_mov_b64 exec, -1\n\ts_waitcnt vmcnt(0)\n\t"
            "v_bitop3_b32 v128, v113, v115, %[tag] bitop3:0x7e\n\tv_bitop3_b32 v129, v117, v119, %[tag] bitop3:0x7e\n\t"
            "v_bitop3_b32 v130, v121, v123, %[tag] bitop3:0x7e\n\tv_bitop3_b32 v131, v125, v127, %[tag] bitop3:0x7e\n\t"
            "v_or3_b32 v128, v128, v129, v130\n\tv_add_f32 v132, v112, v116\n\tv_or_b32 v128, v128, v131\n\t"
            "v_add_f32 v134, v120, v124\n\tv_cmp_ne_u32 vcc, 0, v128\n\tv_add_f32 v133, v114, v118\n\t"
            "s_cbranch_vccnz .Lfp%=\n"
            ".Lfd%=:\n\t"
            "v_add_f32 v135, v122, v126\n\tv_add_f32 v132, v132, v134\n\tv_add_f32 v133, v133, v135\n\ts_nop 1\n\t"
            "v_permlane32_swap_b32 v132, v133\n\tv_add_f32 v134, v132, v133\n\tv_add_f32 v135, v132, v133\n\ts_nop 1\n\t"
            "v_permlane16_swap_b32 v134, v135\n\tv_add_f32 v134, v134, v135\n\ts_nop 1\n\t"
            "v_add_f32_dpp v134, v134, v134 row_ror:8 row_mask:0xf bank_mask:0xf\n\t"
            "v_cvt_pk_bf16_f32 v134, v134, v134\n\tv_lshlrev_b32 %[t], 16, v134\n\t"
            "v_mul_f32 v134, %[t], %[gt]\n\tv_cvt_pk_bf16_f32 v134, v134, v134\n\tv_lshlrev_b32 v134, 16, v134\n\t"
            "v_add_f32 v134, %[xv], v134\n\tv_cvt_pk_bf16_f32 v134, v134, v134\n\t"
            "s_mov_b64 exec, %[ms]\n\tglobal_store_short %[po], v134, off sc1\n\ts_mov_b64 exec, -1"
            : [t] "=&v"(t), [n] "=&s"(n)
            : [tag] "s"(tag), [max] "n"(SPIN_MAX), [p] "v"(py), [m3] "s"(m3), [ms] "s"(ms), [gt] "v"(gt), [xv] "v"(xv), [po] "v"(po)
            : CLOB_T, "vcc", "scc", "memory");
        if (pass) break;
        gt = bfr(silu(t));
        py -= GZ - GY;
        m3 = ~0ull;
        ms = 0x000000ff000000ffull;
      }
      if (b == 0 && lane == 0)
        __hip_atomic_store(reinterpret_cast<unsigned*>(gw + GEP), tag, __ATOMIC_RELAXED, __HIP_MEMORY_SCOPE_AGENT);
      STAMP(10);
    }
    STAMP_FLUSH();
    asm volatile(".Lcode_lastb:");
    return;
  }
  pace<T_LAST + W2T, 15>(clk, ph);
#pragma unroll
  for (int i = W1N; i < 6; ++i) WO[i] = __builtin_amdgcn_raw_buffer_load_b128(rwo, wooff, i * (4 * D * 2), 2);
  unsigned dm0 = __builtin_amdgcn_raw_buffer_load_b32(rwo, NOWHERE, 0, 2), dm1 = __builtin_amdgcn_raw_buffer_load_b32(rwo, NOWHERE, 4, 2);
  STAMP(3);
  SCHED_FENCE();
  const int ly = isF ? lane & 31 : lane;
  u64* ydst = gw + GY + (size_t)(4 * sp + (ly >> 3) + 4 * (ly >> 5)) * YB + 32 * h + 16 * (h >> 4) + 2 * (ly & 7);
  static_assert(RS * 8 * 4 == 4608, "record r4 + 4 is 4608 bytes behind record r4");
  const u64* pr = gw + G2 + (size_t)(h * NSP + r4 + 2) * RS + 32 * w + 2 * c16;
  const u64 ownm = 0xffffull << (16 * (sp & 3));
  u64 own0 = sp < 4 ? ownm : 0ull, own1 = sp < 4 ? 0ull : ownm, my = isF ? 0xffffffffull : ~0ull;
  u64 for0 = m52 & ~own0, for1 = m52 & ~own1;
  own0 &= m48;
  own1 &= m48;
  unsigned osrc = lds_addr(lds + L_PART + 48 * w + 4 * c16), ysrc = lds_addr(lds + L_Y + 2 * ly), cnt1 = lds_addr(lds + L_SY + 1);
  unsigned ydl = lds_addr(lds + L_Y + 128 * w + (isF ? 8 * c + g : 8 * c16 + 2 * r4));
  asm volatile("" : "+s"(own0), "+s"(own1), "+s"(for0), "+s"(for1), "+s"(my));
  PIN(ydst);
  PIN(pr);
  PIN(osrc);
  PIN(ysrc);
  PIN(cnt1);
  PIN(ydl);
  u32x8 WP[3], XP[3];
#pragma unroll
  for (int p = 0; p < 3; ++p)
#pragma unroll
    for (int k = 0; k < 4; ++k) {
      XP[p][2 * k] = __builtin_amdgcn_perm(X[2 * p + 1][k], X[2 * p][k], 0x05040100u);
      XP[p][2 * k + 1] = __builtin_amdgcn_perm(X[2 * p + 1][k], X[2 * p][k], 0x07060302u);
    }
  asm volatile("" : "+{v[64:71]}"(XP[0]), "+{v[72:79]}"(XP[1]), "+{v[80:87]}"(XP[2]));
  SCHED_FENCE();
  auto perm_wo = [&]() {
#pragma unroll
    for (int p = 0; p < 3; ++p)
#pragma unroll
      for (int k = 0; k < 4; ++k) {
        WP[p][2 * k] = __builtin_amdgcn_perm(WO[2 * p + 1][k], WO[2 * p][k], 0x05040100u);
        WP[p][2 * k + 1] = __builtin_amdgcn_perm(WO[2 * p + 1][k], WO[2 * p][k], 0x07060302u);
      }
    asm volatile("" : "+{v[88:95]}"(WP[0]), "+{v[96:103]}"(WP[1]), "+{v[104:111]}"(WP[2]));
    SCHED_FENCE();
    STAMP(1);
  };
  lds_wait(lds, L_SY + 6, 8u);
  if (PT_) pace<T_LAST + PT_, 15>(clk, 0);
  STAMP(4);
  u32x4 ra0, ra1;
  asm volatile(
      "v_mov_b32 v148, %[cw]\n\tv_mov_b32 v149, %[tag]\n\tv_mov_b32 v151, %[tag]\n\t"
      "v_mov_b32 v152, %[cw]\n\tv_mov_b32 v153, %[tag]\n\tv_mov_b32 v155, %[tag]\n\t"
      "s_mov_b64 exec, %[for0]\n\tglobal_load_dwordx4 v[148:151], %[p], off offset:-2304 sc1\n\t"
      "s_mov_b64 exec, %[for1]\n\tglobal_load_dwordx4 v[152:155], %[p], off offset:2304 sc1\n\t"
      "s_mov_b64 exec, %[m48]\n\t" SUM8("%[src]") SUM8B SUM8C
      "s_mov_b64 exec, %[own0]\n\tv_cvt_pk_bf16_f32 v148, v116, v117\n\tv_cvt_pk_bf16_f32 v150, v118, v119\n\t"
      "s_mov_b64 exec, %[own1]\n\tv_cvt_pk_bf16_f32 v152, v116, v117\n\tv_cvt_pk_bf16_f32 v154, v118, v119\n\t"
      "s_mov_b64 exec, -1"
      : "=&{v[148:151]}"(ra0), "=&{v[152:155]}"(ra1)
      : [cw] "v"(cw), [tag] "s"(tag), [for0] "s"(for0), [for1] "s"(for1), [p] "v"(pr), [own0] "s"(own0), [own1] "s"(own1),
        [m48] "s"(m48), [src] "v"(osrc)
      : CLOB_T, "memory");
  SCHED_FENCE();
  perm_wo();
  unsigned np;
  asm volatile(
      "s_mov_b32 %[n], 0\n\t"
      "s_mov_b64 exec, -1\n\tv_mov_b64 v[160:161], 0\n\tv_mov_b64 v[162:163], 0\n\tv_mov_b64 v[164:165], 0\n\t"
      "v_mov_b64 v[166:167], 0\n\ts_branch .Lrc%=\n"
      ".Lrp%=:\n\t"
      "s_add_u32 %[n], %[n], 1\n\ts_cmp_gt_u32 %[n], %[max]\n\ts_cbranch_scc1 .Lrd%=\n\t"
      "v_cmp_ne_u32 vcc, 0, v112\n\tv_cmp_ne_u32 s[98:99], 0, v113\n\ts_sleep " STR(RSLEEP) "\n\t"
      "s_mov_b64 exec, vcc\n\tglobal_load_dwordx4 v[148:151], %[p], off offset:-2304 sc1\n\t"
      "s_mov_b64 exec, s[98:99]\n\tglobal_load_dwordx4 v[152:155], %[p], off offset:2304 sc1\n\t"
      "s_mov_b64 exec, -1\n"
      ".Lrc%=:\n\t"
      "s_waitcnt vmcnt(0)\n\t"
      "v_bitop3_b32 v112, v149, v151, %[tag] bitop3:0x7e\n\tv_bitop3_b32 v113, v153, v155, %[tag] bitop3:0x7e\n\t"
      "v_max_f32 v115, v148, v152\n\tv_or_b32 v114, v112, v113\n\tv_max_f32 v116, v148, v152\n\t"
      "v_cmp_ne_u32 vcc, 0, v114\n\tv_lshlrev_b32 v122, 16, v148\n\ts_cbranch_vccnz .Lrp%=\n"
      ".Lrd%=:\n\t"
      "v_permlane16_swap_b32 v115, v116\n\tv_max_f32 v117, v115, v116\n\tv_max_f32 v118, v115, v116\n\t"
      "v_and_b32 v123, 0xffff0000, v148\n\tv_lshlrev_b32 v124, 16, v150\n\t"
      "v_permlane32_swap_b32 v117, v118\n\tv_max_f32 v117, v117, v118\n\t"
      "v_sub_f32 v115, v148, v117\n\tv_sub_f32 v116, v152, v117\n\tv_exp_f32 v115, v115\n\tv_exp_f32 v116, v116\n\t"
      "v_and_b32 v125, 0xffff0000, v150\n\tv_lshlrev_b32 v126, 16, v152\n\t"
      "v_add_f32 v117, v115, v116\n\tv_add_f32 v118, v115, v116\n\t"
      "v_mul_f32_dpp v122, v115, v122" BC12 "v_mul_f32_dpp v123, v115, v123" BC12
      "v_permlane16_swap_b32 v117, v118\n\tv_add_f32 v119, v117, v118\n\tv_add_f32 v120, v117, v118\n\t"
      "v_mul_f32_dpp v124, v115, v124" BC12 "v_mul_f32_dpp v125, v115, v125" BC12
      "v_permlane32_swap_b32 v119, v120\n\tv_add_f32 v119, v119, v120\n\tv_lshlrev_b32 v128, 16, v154\n\t"
      "v_rcp_f32 v119, v119\n\tv_and_b32 v127, 0xffff0000, v152\n\tv_and_b32 v129, 0xffff0000, v154\n\t"
      "v_fmac_f32_dpp v122, v116, v126" BC12 "v_fmac_f32_dpp v124, v116, v128" BC12
      "v_fmac_f32_dpp v123, v116, v127" BC12 "v_fmac_f32_dpp v125, v116, v129" BC12
      "v_permlane32_swap_b32 v122, v124\n\tv_add_f32 v122, v122, v124\n\t"
      "v_permlane32_swap_b32 v123, v125\n\tv_add_f32 v123, v123, v125\n\t"
      "v_mov_b32_dpp v120, v119" BC12 "s_nop 0\n\t"
      "v_permlane16_swap_b32 v122, v123\n\tv_add_f32 v122, v122, v123\n\t"
      "v_mul_f32 v123, v122, v120\n\ts_cmp_lt_u32 %[sp], 6\n\t"
      "v_mul_f32_dpp v124, v122, v120 quad_perm:[1,0,3,2] row_mask:0xf bank_mask:0xf\n\t"
      "v_cvt_pk_bf16_f32 v124, v123, v124\n\t"
      "s_cbranch_scc1 .Lyf%=\n\ts_nop 0\n\t"
      DOT8(64, 65, 66, 67, 68, 69, 70, 71, 0) DOT8(72, 73, 74, 75, 76, 77, 78, 79, 2) DOT8(80, 81, 82, 83, 84, 85, 86, 87, 4)
      DOT8(88, 89, 90, 91, 92, 93, 94, 95, 6) DOT8(96, 97, 98, 99, 100, 101, 102, 103, 8)
      DOT8(104, 105, 106, 107, 108, 109, 110, 111, 10) YRED
      "v_pk_add_f32 v[160:161], v[160:161], v[162:163]\n\tds_write_b64 %[ydl], v[160:161]\n"
      ".Lys%=:\n\t"
      "s_mov_b64 exec, 1\n\tds_add_rtn_u32 v112, %[cnt], %[one]\n\ts_mov_b64 exec, -1\n\t"
      "ds_read2st64_b64 v[116:119], %[ysrc] offset0:0 offset1:1\n\tds_read2st64_b64 v[120:123], %[ysrc] offset0:2 offset1:3\n\t"
      "s_waitcnt lgkmcnt(2)\n\tv_readfirstlane_b32 s98, v112\n\ts_waitcnt lgkmcnt(1)\n\t"
      "v_pk_add_f32 v[116:117], v[116:117], v[118:119]\n\ts_cmp_eq_u32 s98, 3\n\ts_cselect_b64 exec, %[my], 0\n\t"
      "s_waitcnt lgkmcnt(0)\n\tv_pk_add_f32 v[120:121], v[120:121], v[122:123]\n\ts_nop 0\n\t"
      "v_add_f32 v156, v116, v120\n\tv_add_f32 v158, v117, v121\n\t"
      "global_store_dwordx4 %[dst], v[156:159], off sc1\n\ts_mov_b64 exec, -1\n\ts_branch .Lye%=\n"
      ".Lyf%=:\n\ts_nop 0\n\t"
      "v_mov_b32_dpp v125, v124 row_newbcast:0 row_mask:0xf bank_mask:0x3\n\t"
      "v_mov_b32_dpp v126, v124 row_newbcast:2 row_mask:0xf bank_mask:0x3\n\t"
      "v_mov_b32_dpp v127, v124 row_newbcast:4 row_mask:0xf bank_mask:0x3\n\t"
      "v_mov_b32_dpp v125, v124 row_newbcast:6 row_mask:0xf bank_mask:0xc\n\t"
      "v_mov_b32_dpp v126, v124 row_newbcast:8 row_mask:0xf bank_mask:0xc\n\t"
      "v_mov_b32_dpp v127, v124 row_newbcast:10 row_mask:0xf bank_mask:0xc\n\t"
      DOT8F(88, 89, 90, 91, 92, 93, 94, 95, 125) DOT8F(96, 97, 98, 99, 100, 101, 102, 103, 126)
      DOT8F(104, 105, 106, 107, 108, 109, 110, 111, 127) YRED
      "v_add_f32 v160, v160, v162\n\tv_add_f32 v161, v161, v163\n\ts_nop 0\n\t"
      "v_add_f32_dpp v160, v160, v160 row_ror:8 row_mask:0xf bank_mask:0x3\n\t"
      "v_add_f32_dpp v160, v161, v161 row_ror:8 row_mask:0xf bank_mask:0xc\n\t"
      "ds_write_b32 %[ydl], v160\n\ts_branch .Lys%=\n"
      ".Lye%=:"
      : [n] "=&s"(np), "+{v[156:159]}"(rq), "+{v[148:151]}"(ra0), "+{v[152:155]}"(ra1)
      : [tag] "s"(tag), [p] "v"(pr), [max] "n"(SPIN_MAX), [sp] "s"(sp), [ydl] "v"(ydl), [cnt] "v"(cnt1), [one] "v"(one),
        [ysrc] "v"(ysrc), [my] "s"(my), [dst] "v"(ydst), "{v[64:71]}"(XP[0]), "{v[72:79]}"(XP[1]), "{v[80:87]}"(XP[2]),
        "{v[88:95]}"(WP[0]), "{v[96:103]}"(WP[1]), "{v[104:111]}"(WP[2])
      : CLOB_T, "v160", "v161", "v162", "v163", "v164", "v165", "v166", "v167", "s98", "s99", "vcc", "scc", "memory");
  STAMP(9);
  asm volatile("" ::"v"(dm0), "v"(dm1));
  STAMP_FLUSH();
  asm volatile(".Lcode_last:");
}

__global__ void __launch_bounds__(NT, 2)
fused_decode(const bf16* __restrict__ wq, const bf16* __restrict__ w1, const bf16* __restrict__ w3, u64* __restrict__ gw,
             const bf16* __restrict__ x, const bf16* __restrict__ kc, const bf16* __restrict__ vc,
             const bf16* __restrict__ wo, const bf16* __restrict__ w2, bf16* __restrict__ out) {
  __shared__ __attribute__((aligned(16))) float lds[L_END];
  const int b = blockIdx.x;
  const int xcd = b & 7, jj = b >> 3;
  const int h = xcd * 4 + (jj >> 3), sp = jj & 7;
  const int w = __builtin_amdgcn_readfirstlane(threadIdx.x >> 6);
  unsigned cpf = threadIdx.x;
  {
    u64 pc;
    unsigned len;
    asm volatile(
        "s_cmp_lt_u32 %[w], 4\n\ts_cbranch_scc1 .Lnocpf\n\t"
        "s_getpc_b64 %[pc]\n.Lcode_here:\n\tv_xor_b32 %[c], %[hi], %[c]\n\tv_lshlrev_b32 %[c], 6, %[c]\n\t"
        "s_mov_b32 %[len], .Lcode_last-.Lcode_here-4\n\ts_max_i32 %[len], %[len], .Lcode_lastb-.Lcode_here-4\n\t"
        "v_min_u32 %[c], %[len], %[c]\n\tglobal_load_dword %[c], %[c], %[pc]\n.Lnocpf:"
        : [c] "+v"(cpf), [pc] "=&s"(pc), [len] "=&s"(len) : [w] "s"(w), [hi] "s"((b & 24) << 5) : "scc", "memory");
  }
  SCHED_FENCE();
  if (w >= 4)
    body<false>(lds, h, sp, w, x, kc, vc, wq, wo, w1, w3, w2, gw, out, cpf);
  else
    body<true>(lds, h, sp, w, x, kc, vc, wq, wo, w1, w3, w2, gw, out, cpf);
}

}

extern "C" void kernel_launch(void* const* d_in, const int* in_sizes, int n_in, void* d_out, int out_size, void* d_ws,
                              size_t ws_size, hipStream_t stream) {
  const bf16* x = (const bf16*)d_in[0];
  const bf16* kc = (const bf16*)d_in[1];
  const bf16* vc = (const bf16*)d_in[2];
  const bf16* wq = (const bf16*)d_in[3];
  const bf16* wo = (const bf16*)d_in[4];
  const bf16* w1 = (const bf16*)d_in[6];
  const bf16* w3 = (const bf16*)d_in[7];
  const bf16* w2 = (const bf16*)d_in[8];
  u64* gw = (u64*)d_ws;
  fused_decode<<<NWG, NT, 0, stream>>>(wq, w1, w3, gw, x, kc, vc, wo, w2, (bf16*)d_out);
}
```

```cpp
#include <hip/hip_runtime.h>
#include <hip/hip_bf16.h>
#include <stdint.h>

typedef __hip_bfloat16 bf16;
typedef unsigned long long u64;
typedef unsigned u32x4 __attribute__((ext_vector_type(4)));
typedef unsigned u32x8 __attribute__((ext_vector_type(8)));
typedef float f32x4 __attribute__((ext_vector_type(4)));
typedef float f32x2 __attribute__((ext_vector_type(2)));
typedef __bf16 bf16x2_t __attribute__((ext_vector_type(2)));

namespace {

constexpr int D = 640, HQ = 32, DH = 192, SK = 32768, WIN = 4096, FF = 1536;
constexpr int NSP = 8;
constexpr int NWG = HQ * NSP;
constexpr int NT = 512;
constexpr float SCALE = 0.07216878364870322f;
constexpr float LOG2E = 1.4426950408889634f;
constexpr unsigned SPIN_MAX = 1u << 16;
constexpr int SC1 = 16;
constexpr int NOWHERE = 1 << 30;

#define PV_ 81
#define PK_ 82
#define PX_ PK_
#define TS_ 358
#define SB_ 246
#define LOC 30
#define WT_ 98
#define W2T 132
#define W1N 3
#define PT_ 215
#define NFL 7
#define CE 7
#define CL 8
#define AR 3
#define GR 5
#define GRW 2
#define RSLEEP 1
#define MW 4
#define PM 0x1440
#define MEAS 1
#define ATHR 20
#define ASH 4
#define AGX 7
#define UTHR 8
#define UTH2 40
#define RTHR 99
#define USH 4
#define UGX 9
#define ULIM 20
#define UMISS 60
#define PVL 1
#define PB0 0
#define LSP 16
#define GLP 1
#define E1P 1
#define RSH 4
#define USA 0
#define UTA UTHR
#define HROT 16
constexpr int t_round(int r) { return r <= 2 ? TS_ : t_round(r - 1) + (r > 8 ? PV_ : r == 8 ? PX_ : PK_); }
constexpr int T_LAST = t_round(15);
constexpr int t_late(int i) { return t_round(i); }
constexpr int t_early(int i) { return t_round(i); }

constexpr int QS = 144;
constexpr int YB = 2 * (16 * 32 + 16);
constexpr int RS = 144;
constexpr size_t GQ = 0;
constexpr size_t G2 = GQ + (size_t)NWG * QS;
constexpr size_t GY = G2 + (size_t)NWG * RS;
constexpr size_t GZ = GY + 16;
constexpr size_t GEP = GY + (size_t)NSP * 5 * YB;
constexpr size_t GL = GEP + 16;

__device__ __forceinline__ unsigned fbits(float x) { return __builtin_bit_cast(unsigned, x); }
__device__ __forceinline__ u64 gld(const u64* p) {
  return __hip_atomic_load(p, __ATOMIC_RELAXED, __HIP_MEMORY_SCOPE_AGENT);
}
__device__ __forceinline__ float bf_lo(unsigned u) { return __builtin_bit_cast(float, u << 16); }
__device__ __forceinline__ float bf_hi(unsigned u) { return __builtin_bit_cast(float, u & 0xffff0000u); }
__device__ __forceinline__ float bfr(float x) { return __bfloat162float(__float2bfloat16(x)); }
__device__ __forceinline__ unsigned short bfbits(float x) {
  return __builtin_bit_cast(unsigned short, __float2bfloat16(x));
}
__device__ __forceinline__ float dot2(unsigned a, unsigned b, float c) {
  return __builtin_amdgcn_fdot2_f32_bf16(__builtin_bit_cast(bf16x2_t, a), __builtin_bit_cast(bf16x2_t, b), c, false);
}
template <int CTRL>
__device__ __forceinline__ float dpp(float x) {
  return __builtin_bit_cast(float, __builtin_amdgcn_mov_dpp(__builtin_bit_cast(int, x), CTRL, 0xf, 0xf, true));
}
__device__ __forceinline__ float grp8_sum(float x) {
  x += dpp<0xB1>(x);
  x += dpp<0x4E>(x);
  x += dpp<0x141>(x);
  return x;
}
__device__ __forceinline__ float sw16(float a, float b) {
  auto s = __builtin_amdgcn_permlane16_swap(fbits(a), fbits(b), false, false);
  return __builtin_bit_cast(float, (unsigned)s[0]) + __builtin_bit_cast(float, (unsigned)s[1]);
}
__device__ __forceinline__ float sw32(float a, float b) {
  auto s = __builtin_amdgcn_permlane32_swap(fbits(a), fbits(b), false, false);
  return __builtin_bit_cast(float, (unsigned)s[0]) + __builtin_bit_cast(float, (unsigned)s[1]);
}
__device__ __forceinline__ float xg_sum(float x) {
  x += dpp<0x128>(x);
  x = sw16(x, x);
  return sw32(x, x);
}
__device__ __forceinline__ float xg_max(float x) {
  x = fmaxf(x, dpp<0x128>(x));
  auto s = __builtin_amdgcn_permlane16_swap(fbits(x), fbits(x), false, false);
  x = fmaxf(__builtin_bit_cast(float, (unsigned)s[0]), __builtin_bit_cast(float, (unsigned)s[1]));
  auto t = __builtin_amdgcn_permlane32_swap(fbits(x), fbits(x), false, false);
  return fmaxf(__builtin_bit_cast(float, (unsigned)t[0]), __builtin_bit_cast(float, (unsigned)t[1]));
}
__device__ __forceinline__ float wave_sum(float x) { return xg_sum(grp8_sum(x)); }
__device__ __forceinline__ float ex2(float x) { return __builtin_amdgcn_exp2f(x); }
__device__ __forceinline__ float silu(float x) { return x / (1.0f + __expf(-x)); }
__device__ __forceinline__ float xg_reduce8(const float (&a)[8], bool g0) {
  float r[4];
#pragma unroll
  for (int i = 0; i < 4; ++i) r[i] = sw32(a[i], a[i + 4]);
  const float s0 = sw16(r[0], r[2]), s1 = sw16(r[1], r[3]);
  const float keep = g0 ? s1 : s0, send = g0 ? s0 : s1;
  return keep + dpp<0x128>(send);
}
__device__ __forceinline__ __amdgpu_buffer_rsrc_t rsrc(const void* p, unsigned bytes) {
  return __builtin_amdgcn_make_buffer_rsrc(const_cast<void*>(p), 0, bytes, 0x00020000);
}
__device__ __forceinline__ void gst16(u64* p, unsigned v0, unsigned v1, unsigned tag) {
  const u32x4 o = {v0, tag, v1, tag};
  asm volatile("global_store_dwordx4 %0, %1, off sc1\n\ts_nop 1" ::"v"(p), "v"(o) : "memory");
}
__device__ __forceinline__ unsigned bfpack(float a, float b) {
  return __builtin_bit_cast(unsigned, __builtin_convertvector(f32x2{a, b}, bf16x2_t));
}
__device__ __forceinline__ float2 rows_reduce(const float (&v)[8]) {
  float r[4];
#pragma unroll
  for (int i = 0; i < 4; ++i) r[i] = sw32(v[i], v[i + 4]);
  return float2{sw16(r[0], r[2]), sw16(r[1], r[3])};
}
template <int OFF>
__device__ __forceinline__ void xg_store(const float (&v)[8], float f, unsigned a) {
  float p[4], q[4], t, u;
#pragma unroll
  for (int i = 0; i < 4; ++i) {
    auto s = __builtin_amdgcn_permlane32_swap(fbits(v[i]), fbits(v[i + 4]), false, false);
    p[i] = __builtin_bit_cast(float, (unsigned)s[0]);
    q[i] = __builtin_bit_cast(float, (unsigned)s[1]);
  }
  const f32x2 r01 = f32x2{p[0], p[1]} + f32x2{q[0], q[1]}, r23 = f32x2{p[2], p[3]} + f32x2{q[2], q[3]};
  auto s0 = __builtin_amdgcn_permlane16_swap(fbits(r01[0]), fbits(r23[0]), false, false);
  auto s1 = __builtin_amdgcn_permlane16_swap(fbits(r01[1]), fbits(r23[1]), false, false);
  asm volatile("v_add_f32 %0, %2, %3\n\tv_add_f32 %1, %4, %5\n\ts_nop 0\n\t"
               "v_add_f32_dpp %0, %0, %0 row_ror:8 row_mask:0xf bank_mask:0x3\n\t"
               "v_add_f32_dpp %0, %1, %1 row_ror:8 row_mask:0xf bank_mask:0xc\n\t"
               "v_mul_f32 %0, %6, %0\n\tds_write_b32 %7, %0 offset:%8"
               : "=&v"(t), "=&v"(u)
               : "v"((unsigned)s0[0]), "v"((unsigned)s0[1]), "v"((unsigned)s1[0]), "v"((unsigned)s1[1]), "v"(f), "v"(a), "n"(OFF)
               : "memory");
}
#define SUM8(a)                                                                                                          \
  "ds_read_b128 v[116:119], " a "\n\tds_read_b128 v[120:123], " a " offset:768\n\t"                                      \
  "ds_read_b128 v[124:127], " a " offset:1536\n\tds_read_b128 v[128:131], " a " offset:2304\n\t"                         \
  "ds_read_b128 v[132:135], " a " offset:3072\n\tds_read_b128 v[136:139], " a " offset:3840\n\t"                         \
  "ds_read_b128 v[140:143], " a " offset:4608\n\tds_read_b128 v[144:147], " a " offset:5376\n\ts_waitcnt lgkmcnt(6)\n\t"
#define SUM8B                                                                                                            \
  "v_pk_add_f32 v[116:117], v[116:117], v[120:121]\n\tv_pk_add_f32 v[118:119], v[118:119], v[122:123]\n\t"               \
  "s_waitcnt lgkmcnt(4)\n\t"                                                                                             \
  "v_pk_add_f32 v[124:125], v[124:125], v[128:129]\n\tv_pk_add_f32 v[126:127], v[126:127], v[130:131]\n\t"               \
  "v_pk_add_f32 v[116:117], v[116:117], v[124:125]\n\tv_pk_add_f32 v[118:119], v[118:119], v[126:127]\n\t"
#define SUM8C                                                                                                            \
  "s_waitcnt lgkmcnt(2)\n\t"                                                                                             \
  "v_pk_add_f32 v[132:133], v[132:133], v[136:137]\n\tv_pk_add_f32 v[134:135], v[134:135], v[138:139]\n\t"               \
  "s_waitcnt lgkmcnt(0)\n\t"                                                                                             \
  "v_pk_add_f32 v[140:141], v[140:141], v[144:145]\n\tv_pk_add_f32 v[142:143], v[142:143], v[146:147]\n\t"               \
  "v_pk_add_f32 v[132:133], v[132:133], v[140:141]\n\tv_pk_add_f32 v[134:135], v[134:135], v[142:143]\n\t"               \
  "v_pk_add_f32 v[116:117], v[116:117], v[132:133]\n\tv_pk_add_f32 v[118:119], v[118:119], v[134:135]\n\t"
#define BC12 " row_newbcast:12 row_mask:0xf bank_mask:0xf\n\t"
#define DOTB(y, a, n) "v_dot2c_f32_bf16_dpp v" #y ", v124, v" #a " row_newbcast:" #n " row_mask:0xf bank_mask:0xf\n\t"
#define DOT8(a, b, c, d, e, f, g, h, n) \
  DOTB(160, a, n) DOTB(161, b, n) DOTB(162, c, n) DOTB(163, d, n) DOTB(164, e, n) DOTB(165, f, n) DOTB(166, g, n) DOTB(167, h, n)
#define DOTF(y, a, n) "v_dot2c_f32_bf16 v" #y ", v" #n ", v" #a "\n\t"
#define DOT8F(a, b, c, d, e, f, g, h, n) \
  DOTF(160, a, n) DOTF(161, b, n) DOTF(162, c, n) DOTF(163, d, n) DOTF(164, e, n) DOTF(165, f, n) DOTF(166, g, n) DOTF(167, h, n)
#define YRED                                                                                                             \
  "v_permlane32_swap_b32 v160, v164\n\tv_permlane32_swap_b32 v161, v165\n\tv_permlane32_swap_b32 v162, v166\n\t"         \
  "v_permlane32_swap_b32 v163, v167\n\tv_pk_add_f32 v[160:161], v[160:161], v[164:165]\n\t"                              \
  "v_pk_add_f32 v[162:163], v[162:163], v[166:167]\n\ts_nop 1\n\t"                                                       \
  "v_permlane16_swap_b32 v160, v162\n\tv_permlane16_swap_b32 v161, v163\n\t"
#define CLOB_T                                                                                                     \
  "v112", "v113", "v114", "v115", "v116", "v117", "v118", "v119", "v120", "v121", "v122", "v123", "v124", "v125",        \
      "v126", "v127", "v128", "v129", "v130", "v131", "v132", "v133", "v134", "v135", "v136", "v137", "v138", "v139",    \
      "v140", "v141", "v142", "v143", "v144", "v145", "v146", "v147"
#define SCHED_FENCE() __builtin_amdgcn_sched_barrier(0)
#define STR2(x) #x
#define STR(x) STR2(x)
#define THRN(n) do { SCHED_FENCE(); asm volatile("s_waitcnt vmcnt(%0)" ::"n"(n) : "memory"); SCHED_FENCE(); } while (0)
#define NOWT() ((unsigned)__builtin_amdgcn_s_memrealtime())
template <int N>
struct IC {
  static constexpr int v = N;
};
template <int I, int E, class F>
__device__ __forceinline__ void sfor(F&& f) {
  if constexpr (I < E) {
    f(IC<I>{});
    sfor<I + 1, E>(f);
  }
}
#define LDS_DRAIN() asm volatile("s_waitcnt lgkmcnt(0)" ::: "memory")
__device__ __forceinline__ unsigned lds_add(float* lds, int idx) {
  return __hip_atomic_fetch_add(reinterpret_cast<unsigned*>(lds + idx), 1u, __ATOMIC_RELAXED, __HIP_MEMORY_SCOPE_WORKGROUP);
}
__device__ __forceinline__ void lds_wait(float* lds, int idx, unsigned n) {
  SCHED_FENCE();
#pragma unroll 1
  for (unsigned spin = 0; spin < (SPIN_MAX << 4); ++spin) {
    if (__hip_atomic_load(reinterpret_cast<unsigned*>(lds + idx), __ATOMIC_RELAXED, __HIP_MEMORY_SCOPE_WORKGROUP) >= n) break;
    __builtin_amdgcn_s_sleep(1);
  }
  asm volatile("" ::: "memory");
  SCHED_FENCE();
}
template <int T, int R>
__device__ __forceinline__ void pace(unsigned clk, int ph) {
  unsigned o;
  SCHED_FENCE();
  asm volatile(
      "s_branch .Lpb%=\n"
      ".Lpa%=:\n\ts_sleep 1\n"
      ".Lpb%=:\n\tds_read_b32 %0, %1 offset:%4\n\ts_memrealtime s[98:99]\n\ts_waitcnt lgkmcnt(0)\n\tv_readfirstlane_b32 s99, %0\n\t"
      "s_sub_u32 s98, s98, %2\n\ts_sub_u32 s98, s98, s99\n\ts_cmp_lt_i32 s98, %3\n\ts_cbranch_scc1 .Lpa%="
      : "=&v"(o) : "v"(clk), "s"(ph), "n"(T), "n"(4 * R) : "s98", "s99", "scc", "memory");
  SCHED_FENCE();
}

template <int T, int R>
__device__ __forceinline__ unsigned pace_m(unsigned clk, int ph) {
  unsigned o, tl;
  SCHED_FENCE();
  asm volatile(
      "s_mov_b32 %1, 0\n\ts_branch .Lpb%=\n"
      ".Lpa%=:\n\ts_sleep 1\n"
      ".Lpb%=:\n\tds_read_b32 %0, %2 offset:%5\n\ts_getreg_b32 s97, hwreg(HW_REG_IB_STS, 0, 4)\n\ts_memrealtime s[98:99]\n\t"
      "s_or_b32 s97, s97, %1\n\ts_waitcnt lgkmcnt(0)\n\ts_cselect_b32 %1, %1, s98\n\tv_readfirstlane_b32 s99, %0\n\t"
      "s_sub_u32 s98, s98, %3\n\ts_sub_u32 s98, s98, s99\n\ts_cmp_lt_i32 s98, %4\n\ts_cbranch_scc1 .Lpa%="
      : "=&v"(o), "=&s"(tl) : "v"(clk), "s"(ph), "n"(T), "n"(4 * R) : "s97", "s98", "s99", "scc", "memory");
  SCHED_FENCE();
  return tl;
}

#define STAMP(i)
#define STAMP_FLUSH()
#define TT(i)
constexpr int L_QP = 0;
constexpr int L_QS = L_QP + 4 * 128;
constexpr int L_PART = L_QS + 96;
constexpr int L_ML = L_PART + 8 * DH;
constexpr int L_Z = L_ML + 16;
constexpr int L_Y = L_Z + 320;
constexpr int L_H = L_Y + 512;
constexpr int L_SY = L_H + 64;
constexpr int L_CK = L_SY + 8;
constexpr int L_END = L_CK + 16;

#define PIN(x) asm volatile("" : "+v"(x))

__device__ __forceinline__ void wg_ml(const float* lds, float& M, float& L) {
  float mm[8], ll[8];
#pragma unroll
  for (int i = 0; i < 4; ++i) {
    const f32x4 t = *reinterpret_cast<const f32x4*>(lds + L_ML + 4 * i);
    mm[2 * i] = t[0];
    ll[2 * i] = t[1];
    mm[2 * i + 1] = t[2];
    ll[2 * i + 1] = t[3];
  }
  M = mm[0];
#pragma unroll
  for (int ww = 1; ww < 8; ++ww) M = fmaxf(M, mm[ww]);
  L = 0.f;
#pragma unroll
  for (int ww = 0; ww < 8; ++ww) L = fmaf(ex2((mm[ww] - M) * LOG2E), ll[ww], L);
}
__device__ __forceinline__ unsigned lds_addr(const float* p) {
  return (unsigned)(size_t)(const __attribute__((address_space(3))) float*)p;
}

template <bool early>
__device__ __forceinline__ void body(float* lds, const int h, const int sp, const int w, const bf16* __restrict__ x,
                                     const bf16* __restrict__ kc, const bf16* __restrict__ vc,
                                     const bf16* __restrict__ wq, const bf16* __restrict__ wo,
                                     const bf16* __restrict__ w1, const bf16* __restrict__ w3,
                                     const bf16* __restrict__ w2, u64* __restrict__ gw, bf16* __restrict__ out,
                                     unsigned cpf) {
  const int b = blockIdx.x;
  const int tid = threadIdx.x, lane = tid & 63;
  const int g = lane >> 3, c = lane & 7;
  const int rec = h * NSP + sp;

  STAMP(0);
  const bool isF = sp < 6;
  u32x4 PR[8], xa, xb, xq;
  unsigned epoch = 0, tk = 0;
  int ra = 0, RA0 = 0;
  bool va = false;
  if constexpr (early) {
    int s0_ = 240, s1_ = 80, s2_ = 240 * (sp & 1), s3_ = 80 * sp, s4_ = 64 * (sp >> 1), s5_ = 64 * w, s6_ = 24 * w;
    asm("" : "+s"(s0_), "+s"(s1_), "+s"(s2_), "+s"(s3_), "+s"(s4_), "+s"(s5_), "+s"(s6_));
    const int rw0 = isF ? s5_ : s6_;
    int lim = (isF ? s0_ : s1_) - rw0, lim2 = isF ? 64 : 24;
    asm("" : "+s"(lim), "+s"(lim2));
    lim = lim < lim2 ? lim : lim2;
    ra = rw0 + 8 * g;
    va = 8 * g < lim;
    RA0 = isF ? s2_ : s3_;
    int aoff = (int)__umul24(g, 8 * HQ * DH * 2) + 16 * c;
    PIN(aoff);
    aoff = va ? aoff : NOWHERE;
    int sa = ((RA0 + rw0) * (HQ * DH) + h * DH + (isF ? s4_ : 64)) * 2;
    PIN(aoff);
    asm volatile("" : "+s"(sa));
    SCHED_FENCE();
    const __amdgpu_buffer_rsrc_t rw = rsrc(wq, NOWHERE);
    PR[0] = __builtin_amdgcn_raw_buffer_load_b128(rw, aoff, sa, 2);
    SCHED_FENCE();
    tk = NOWT();
    SCHED_FENCE();
    asm volatile("s_load_dword %0, %1, %2" : "=&s"(epoch) : "s"(gw), "s"(E1P && w == 1 ? (int)GL * 8 + (sp * 8 + (b & 7)) * LSP : (int)GEP * 8) : "memory");
    SCHED_FENCE();
#pragma unroll
    for (int i = 1; i < 8; ++i) PR[i] = __builtin_amdgcn_raw_buffer_load_b128(rw, aoff, sa + i * (HQ * DH * 2), 2);
    SCHED_FENCE();
  }
  const unsigned short* xs = reinterpret_cast<const unsigned short*>(x);
  const size_t wgb = ((size_t)h * SK + (SK - WIN)) * DH;
  const __amdgpu_buffer_rsrc_t rk = rsrc(kc + wgb, WIN * DH * 2);
  const __amdgpu_buffer_rsrc_t rv = rsrc(vc + wgb, WIN * DH * 2);
  const int voff = (g * DH + 8 * c) * 2;
  auto tile = [&](int t) { return (t * 64 + h * HROT + w * 8 + sp) & 511; };
  u32x4 K[8][3], V[8][3];
  auto kld = [&](int t, int j) { K[t][j] = __builtin_amdgcn_raw_buffer_load_b128(rk, voff + 128 * j, tile(t) * 3072, 2); };
  auto vld = [&](int t, int j) { V[t][j] = __builtin_amdgcn_raw_buffer_load_b128(rv, voff + 128 * j, tile(t) * 3072, 2); };
  unsigned tag = 0;
  const unsigned clk = lds_addr(lds + L_CK);
  const int ph = (w * PV_) >> 3;
  const int r4 = lane >> 4, c16 = lane & 15;
  const int ch = c16 < 10 ? c16 : 9;
  const int fi = (h & 3) * 6 + (isF ? sp : 5), gl = fi >> 3, gI = (h >> 2) * 3 + gl, fr = fi & 7;
  u32x4 WO[6], X[6], W2[4];
  unsigned tch = 0;
  const int wooff = ((48 * w + r4 + (isF ? 24 * (g & 1) : 24)) * D + 64 * sp + 8 * c + (isF ? 0 : 128 * (g & 1))) * 2;
  typedef unsigned u32x2 __attribute__((ext_vector_type(2)));
  const __amdgpu_buffer_rsrc_t rl = rsrc(gw + GL, 16 * 64 * LSP);
  const bool pub = !((sp + (b >> 6)) & 3);
  const int lw_ = lane == 0 && pub ? (sp * 8 + (b & 7)) * LSP : NOWHERE;
  u32x2 lat = {0u, 0u};
  u64 own = 0, pre = 0;
  unsigned pro = 0, st1 = 0, old = 0;
  bool vslow = false, kslow = false;
  if constexpr (early) {
    u32x4 PF[5];
    const int F0 = (isF ? 160 * (fr >> 1) : 480) + 40 * w;
    const int fs = isF ? FF : HQ * DH;
    int s6_ = 64 * gI, s7_ = h * DH + 128 * (sp & 1);
    asm("" : "+s"(s6_), "+s"(s7_));
    const int boff = ((int)__umul24(g, fs) + 8 * c) * 2;
    const int sb = (F0 * fs + (isF ? s6_ : s7_)) * 2;
    const bf16* pf_ = (fr & 1) ? w3 : w1;
    asm("" : "+s"(pf_));
    const __amdgpu_buffer_rsrc_t rf = rsrc(isF ? pf_ : wq, NOWHERE);
    unsigned short xr[5];
#pragma unroll
    for (int i = 0; i < 5; ++i) PF[i] = __builtin_amdgcn_raw_buffer_load_b128(rf, boff, sb + i * 16 * fs, 2);
    SCHED_FENCE();
    if (!isF) {
#pragma unroll
      for (int i = 0; i < 6; ++i)
        X[i] = __builtin_amdgcn_raw_buffer_load_b128(rsrc(wo + (size_t)h * DH * D, DH * D * 2), wooff - 24 * D * 2 + i * (4 * D * 2), 0, 2);
    } else {
#pragma unroll
      for (int i = 0; i < 6; ++i) X[i] = u32x4{0u, 0u, 0u, 0u};
    }
    SCHED_FENCE();
    xa = *reinterpret_cast<const u32x4*>(x + 8 * lane);
    xb = *reinterpret_cast<const u32x4*>(x + 512 + 8 * (lane & 15));
    xq = *reinterpret_cast<const u32x4*>(x + RA0 + (va ? ra : 0));
#pragma unroll
    for (int i = 0; i < 5; ++i) xr[i] = xs[F0 + 8 * i + g];
    SCHED_FENCE();
    const unsigned epv = __hip_atomic_load(reinterpret_cast<unsigned*>(gw + GEP), __ATOMIC_RELAXED, __HIP_MEMORY_SCOPE_AGENT);
    SCHED_FENCE();
    if (tid < 16) reinterpret_cast<unsigned*>(lds)[L_CK + tid] = tk;
    asm volatile("s_waitcnt lgkmcnt(0)\n\ts_barrier" : "+s"(epoch)::"memory");
    STAMP(6);
    SCHED_FENCE();
#pragma unroll
    for (int t = 0; t < 3; ++t)
#pragma unroll
      for (int j = 0; j < 3; ++j) kld(t, j);
    SCHED_FENCE();
    float mb, den;
    {
      float xf[8], xg[8];
#pragma unroll
      for (int i = 0; i < 4; ++i) {
        xf[2 * i] = bf_lo(xa[i]);
        xf[2 * i + 1] = bf_hi(xa[i]);
        xg[2 * i] = bf_lo(xb[i]);
        xg[2 * i + 1] = bf_hi(xb[i]);
      }
      const bool lo16 = lane < 16;
      float sum = 0.f;
#pragma unroll
      for (int i = 0; i < 8; ++i) sum += xf[i] + (lo16 ? xg[i] : 0.f);
      sum = wave_sum(sum);
      const float mean = sum * (1.0f / D);
      float sq = 0.f;
#pragma unroll
      for (int i = 0; i < 8; ++i) {
        const float d1 = xf[i] - mean, d2 = xg[i] - mean;
        sq += d1 * d1 + (lo16 ? d2 * d2 : 0.f);
      }
      sq = wave_sum(sq);
      mb = bfr(mean);
      const float vb = bfr(sq * (1.0f / D));
      den = bfr(sqrtf(bfr(vb + 1e-5f)));
    }
    {
      unsigned xn[13];
#pragma unroll
      for (int i = 0; i < 4; ++i) {
        xn[2 * i] = fbits(bfr(bfr(bf_lo(xq[i]) - mb) / den));
        xn[2 * i + 1] = fbits(bfr(bfr(bf_hi(xq[i]) - mb) / den));
      }
#pragma unroll
      for (int i = 0; i < 5; ++i) xn[8 + i] = fbits(bfr(bfr(bf_lo(xr[i]) - mb) / den));
#pragma unroll
      for (int i = 0; i < 13; ++i) PIN(xn[i]);
      SCHED_FENCE();
      pace<t_round(2), 2>(clk, ph);
#pragma unroll
      for (int j = 0; j < 3; ++j) kld(3, j);
      TT(2);
      SCHED_FENCE();
      float fa[8], fg[8];
#pragma unroll
      for (int k = 0; k < 8; ++k) fa[k] = fg[k] = 0.f;
#pragma unroll
      for (int i = 0; i < 8; ++i)
#pragma unroll
        for (int k = 0; k < 4; ++k) {
          fa[2 * k] = dot2(PR[i][k], xn[i] >> 16, fa[2 * k]);
          fa[2 * k + 1] = dot2(PR[i][k], xn[i], fa[2 * k + 1]);
        }
      lds[L_QP + w * 128 + 8 * c + g] = xg_reduce8(fa, g & 1);
#pragma unroll
      for (int i = 0; i < 5; ++i)
#pragma unroll
        for (int k = 0; k < 4; ++k) {
          fg[2 * k] = dot2(PF[i][k], xn[8 + i] >> 16, fg[2 * k]);
          fg[2 * k + 1] = dot2(PF[i][k], xn[8 + i], fg[2 * k + 1]);
        }
      lds[L_QP + w * 128 + 64 + 8 * c + g] = xg_reduce8(fg, g & 1);
    }
    SCHED_FENCE();
    tag = __builtin_amdgcn_readfirstlane(epv) + 1u;
    if (tid == 0) reinterpret_cast<unsigned*>(lds)[L_SY + 3] = tag;
    LDS_DRAIN();
    if (lane == 0) old = lds_add(lds, L_SY);
    old = __builtin_amdgcn_readfirstlane(old);
    if (old == 3) {
      const float2* qp = reinterpret_cast<const float2*>(lds + L_QP) + lane;
      const float2 p0_ = qp[0], p1_ = qp[64], p2_ = qp[128], p3_ = qp[192];
      gst16(gw + GQ + (size_t)rec * QS + 2 * lane, fbits((p0_.x + p1_.x) + (p2_.x + p3_.x)),
            fbits((p0_.y + p1_.y) + (p2_.y + p3_.y)), tag);
    }
    STAMP(11);
  } else {
    if (tid >= NT - 8) reinterpret_cast<unsigned*>(lds)[L_SY + tid - (NT - 8)] = 0u;
    unsigned st0 = 0;
    {
      const bf16* tp = w == 4 ? kc + wgb : w == 5 ? (!isF ? wo + (size_t)h * DH * D : (fr & 1) ? w3 : w1) : w == 6 ? ((sp & 1) ? x : wq) : vc + wgb;
      asm volatile("s_waitcnt lgkmcnt(0)\n\ts_load_dword %0, %1, 0x0" : "=&s"(st0) : "s"(tp) : "memory");
    }
    SCHED_FENCE();
#pragma unroll
    for (int j = 0; j < 3; ++j) kld(0, j);
    SCHED_FENCE();
    asm volatile("s_barrier" ::: "memory");
    STAMP(6);
    SCHED_FENCE();
    if (w < 6) {
      const int l = lane < 42 ? lane : 41, k = l & 15;
      const int yg = 4 * (k < 8 || isF ? sp : sp + 2) + ((k >> 1) & 3);
      const size_t yo = l < 32 || !isF ? GY + (size_t)yg * YB + 32 * h + 16 * (h >> 4) + 8 * (k & 1)
                                       : GZ + (size_t)(fr * 5 + (k >> 1)) * YB + 32 * gI + 16 * (gI >> 4) + 8 * (k & 1);
      unsigned o4 = (unsigned)(l < 16 ? G2 + (size_t)rec * RS + 8 * l : yo);
      unsigned o5 = (unsigned)(GQ + (size_t)rec * QS + 8 * (lane < 16 ? lane : 15));
      if (GLP && pub && lane >= 16 && lane < 32 && ((PM >> (lane - 16)) & 1))
        o5 = (unsigned)GL + (unsigned)(((lane - 16) * 64 + sp * 8 + (b & 7)) * (LSP / 8));
      PIN(o4);
      PIN(o5);
      pre = gld(gw + (w == 4 ? o4 : o5));
    }
    if (w == 7 && h < 5)
      pro = __hip_atomic_load(reinterpret_cast<const unsigned*>(out + 80 * sp + 16 * h) + (lane & 7), __ATOMIC_RELAXED,
                              __HIP_MEMORY_SCOPE_AGENT);
    SCHED_FENCE();
    asm volatile("s_waitcnt lgkmcnt(0)" ::"s"(st0) : "memory");
#pragma unroll
    for (int j = 0; j < 3; ++j) {
      kld(1, j);
      THRN(5);
    }
    THRN(3);
    TT(1);
    own = __builtin_amdgcn_s_memrealtime() - SB_;
    __builtin_amdgcn_raw_buffer_store_b64(u32x2{(unsigned)own, (unsigned)(own >> 32)}, rl, w == 6 ? lw_ : NOWHERE, 0, SC1);
    SCHED_FENCE();
    if (LOC && w == 6 && lane < 16) {
      const unsigned o = (unsigned)own - LOC;
      if ((int)(o - reinterpret_cast<unsigned*>(lds)[L_CK + lane]) > 0) reinterpret_cast<unsigned*>(lds)[L_CK + lane] = o;
    }
    SCHED_FENCE();
    {
      const bf16* tp = (w & 1) ? wo + (size_t)h * DH * D : w2 + (size_t)gI * 64 * D;
      asm volatile("s_load_dword %0, %1, 0x0" : "=&s"(st1) : "s"(tp) : "memory");
    }
    SCHED_FENCE();
  }
  u32x4 qf[3];
  float sc[8], mw, lw, acc[24];
  unsigned pb[8];
  auto score = [&](int t) {
    float d = 0.f;
#pragma unroll
    for (int j = 0; j < 3; ++j)
#pragma unroll
      for (int i = 0; i < 4; ++i) d = dot2(K[t][j][i], qf[j][i], d);
    d = grp8_sum(d);
    sc[t] = bfr(bfr(d) * SCALE);
  };
  auto pv = [&](int t) {
    const unsigned plo = pb[t], phi = plo << 16;
#pragma unroll
    for (int j = 0; j < 3; ++j)
#pragma unroll
      for (int i = 0; i < 4; ++i) {
        acc[8 * j + 2 * i] = dot2(V[t][j][i], plo, t ? acc[8 * j + 2 * i] : 0.f);
        acc[8 * j + 2 * i + 1] = dot2(V[t][j][i], phi, t ? acc[8 * j + 2 * i + 1] : 0.f);
        PIN(acc[8 * j + 2 * i]);
        PIN(acc[8 * j + 2 * i + 1]);
      }
  };
  const bool hf = lane >> 5;
  const __amdgpu_buffer_rsrc_t rq_ = rsrc(gw + GQ, NWG * QS * 8);
  int go[9];
  {
    const int l16 = (lane & 31) * 16;
    const int qa = l16 + (8 * h + (hf ? 4 : 0)) * (QS * 8);
    go[0] = qa;
    go[1] = qa + QS * 8;
    go[2] = l16 + (8 * h + (hf ? 7 : 6)) * (QS * 8) + 512;
    go[3] = qa + 2 * QS * 8;
    go[4] = qa + 3 * QS * 8;
#pragma unroll
    for (int k = 0; k < 4; ++k) {
      const int m = 8 * gl + 2 * k + (hf ? 1 : 0);
      go[5 + k] = isF ? l16 + (32 * (h >> 2) + m + (((m * 43) >> 7) & 6)) * (QS * 8) + 512 : NOWHERE;
    }
  }
  const bool gath = old == GRW;
  u32x4 pa[9];
  constexpr int C0 = early ? CE : CL;
  static_assert(C0 + 3 < 15 && C0 > GR + 1 && C0 >= 6, "schedule");
  sfor<early ? 3 : 2, 16>([&](auto R) {
    constexpr int r = R.v, t = r >= 8 ? r - 8 : r + early;
    unsigned tl = 0;
    if constexpr (!early && MEAS && ((PM >> r) & 1)) tl = pace_m<t_round(r), r>(clk, ph); else pace<t_round(r), r>(clk, ph);
    if constexpr (!early && ((PM >> (r - 1)) & 1)) {
      lat = __builtin_amdgcn_raw_buffer_load_b64(rl, w == MW ? lane * LSP : NOWHERE, 64 * LSP * (r - 1), SC1);
      SCHED_FENCE();
    }
    if constexpr (r < 8 && t == 8) {
      const __amdgpu_buffer_rsrc_t rx = rsrc(w2 + ((size_t)gI * 64 + 16 * w) * D + 80 * fr, isF ? 16 * D * 2 : 0);
#pragma unroll
      for (int i = 0; i < 4; ++i)
        W2[i] = __builtin_amdgcn_raw_buffer_load_b128(rx, (r4 * D + 8 * ch) * 2 + i * (4 * D * 2), 0, 2);
      tch = __builtin_amdgcn_raw_buffer_load_b32(rsrc(wo + (size_t)h * DH * D, DH * D * 2), tid ? NOWHERE : wooff, 0, 2);
    } else {
#pragma unroll
      for (int j = 0; j < 3; ++j) {
        if (r >= 8) vld(t, j); else kld(t, j);
        if (r == 15 && j == 2) break;
        if (early && r == GR + 1) continue;
        THRN(early && r == 8 ? NFL + 2 : NFL);
      }
    }
    TT(r);
    SCHED_FENCE();
    if constexpr (early) {
      if (r == GR && gath) {
#pragma unroll
        for (int s = 0; s < 9; ++s) pa[s] = __builtin_amdgcn_raw_buffer_load_b128(rq_, go[s], 0, SC1);
        SCHED_FENCE();
      }
      if (r == GR + 1) {
        if (gath) {
          const unsigned tf = isF ? tag : 0u;
          bool ok = true;
#pragma unroll
          for (int s = 0; s < 9; ++s) ok = ok & (pa[s][1] == (s < 5 ? tag : tf)) & (pa[s][3] == (s < 5 ? tag : tf));
          if (!__all(ok)) {
            for (unsigned spin = 0;; ++spin) {
              ok = true;
              asm volatile("" ::: "memory");
#pragma unroll
              for (int s = 0; s < 9; ++s) {
                pa[s] = __builtin_amdgcn_raw_buffer_load_b128(rq_, go[s], 0, SC1);
                ok = ok & (pa[s][1] == (s < 5 ? tag : tf)) & (pa[s][3] == (s < 5 ? tag : tf));
              }
              if (__all(ok) || spin >= SPIN_MAX) break;
              __builtin_amdgcn_s_sleep(1);
            }
          }
          float qo[2], qm[2], hu[2];
#pragma unroll
          for (int k = 0; k < 2; ++k) {
            auto val = [&](int s) { return __builtin_bit_cast(float, (unsigned)pa[s][2 * k]); };
            qo[k] = (val(0) + val(1)) + val(2);
            const float t_ = val(3) + val(4);
            qm[k] = sw32(t_, t_);
            const float u = bfr((val(5) + val(6)) + (val(7) + val(8)));
            auto sw = __builtin_amdgcn_permlane32_swap(fbits(u), fbits(u), false, false);
            hu[k] = bfr(bfr(silu(__builtin_bit_cast(float, (unsigned)sw[0]))) * __builtin_bit_cast(float, (unsigned)sw[1]));
          }
          reinterpret_cast<unsigned*>(lds + L_QS)[lane + (hf ? 32 : 0)] = bfpack(qo[0], qo[1]);
          if (hf) {
            reinterpret_cast<unsigned*>(lds + L_QS)[lane] = bfpack(qm[0], qm[1]);
          } else {
            *reinterpret_cast<float2*>(lds + L_H + 2 * lane) = float2{hu[0], hu[1]};
          }
          LDS_DRAIN();
          if (lane == 0) lds_add(lds, L_SY + 2);
        }
        STAMP(2);
      }
    } else {
      if (r == 2) asm volatile("" ::"s"(st1), "v"(cpf), "v"(pre), "v"(pro));
      if (r == AR) lat = __builtin_amdgcn_raw_buffer_load_b64(rl, w == 6 ? lane * LSP : NOWHERE, 0, SC1);
      if (r == AR + 1) {
        const u64 d = (((u64)lat[1] << 32) | lat[0]) - own + 200u;
        const bool ok = d <= 400u;
        const float s_ = wave_sum(ok ? (float)(unsigned)d : 0.f), n = (float)__builtin_popcountll(__ballot(ok));
        const unsigned o = (unsigned)own + (n > 0.f ? (unsigned)(s_ * __builtin_amdgcn_rcpf(n)) - 200u : 0u);
        if (w == 6 && lane < 16) reinterpret_cast<unsigned*>(lds)[L_CK + lane] = o;
      }
      if ((PM >> r) & 1) {
        const unsigned org = __builtin_amdgcn_readfirstlane(reinterpret_cast<const unsigned*>(lds)[L_CK + r]);
        tag = __builtin_amdgcn_readfirstlane(reinterpret_cast<const unsigned*>(lds)[L_SY + 3]);
        THRN(3);
        const unsigned now = NOWT() - 9;
        const int l = (int)((tl ? tl : now) - org) - (t_round(r) + ph);
        __builtin_amdgcn_raw_buffer_store_b64(u32x2{(unsigned)(l < -64 ? 0 : l + 64), tag}, rl, w == MW && l < ULIM ? lw_ : NOWHERE,
                                              64 * LSP * r, SC1);
      }
      SCHED_FENCE();
    }
    if (r == C0) {
      lds_wait(lds, L_SY + 2, 1u);
      STAMP(3);
#pragma unroll
      for (int j = 0; j < 3; ++j) qf[j] = *reinterpret_cast<const u32x4*>(lds + L_QS + (c + 8 * j) * 4);
#pragma unroll
      for (int k = 0; k < 4; ++k) score(k);
    } else if (r == C0 + 1) {
#pragma unroll
      for (int k = 4; k < 7; ++k) score(k);
    } else if (r == C0 + 2) {
      score(7);
      mw = sc[0];
#pragma unroll
      for (int k = 1; k < 8; ++k) mw = fmaxf(mw, sc[k]);
      mw = xg_max(mw);
      lw = 0.f;
#pragma unroll
      for (int k = 0; k < 8; ++k) {
        const float p = ex2((sc[k] - mw) * LOG2E);
        lw += p;
        pb[k] = bfbits(p);
      }
      lw = xg_sum(lw);
      if (lane == 0) {
        lds[L_ML + 2 * w] = mw;
        lds[L_ML + 2 * w + 1] = lw;
      }
      if (early) {
        float z[8];
#pragma unroll
        for (int n = 0; n < 8; ++n) z[n] = 0.f;
#pragma unroll
        for (int r = 0; r < 4; ++r) {
          const unsigned chi = fbits(lds[L_H + 16 * w + 4 * r + r4]), clo = chi >> 16;
#pragma unroll
          for (int n = 0; n < 4; ++n) {
            z[2 * n] = dot2(W2[r][n], clo, z[2 * n]);
            z[2 * n + 1] = dot2(W2[r][n], chi, z[2 * n + 1]);
          }
        }
        const float2 zr = rows_reduce(z);
        if (c16 < 10) *reinterpret_cast<float2*>(lds + L_Z + 80 * w + 8 * c16 + 2 * r4) = zr;
      }
      LDS_DRAIN();
      if (lane == 0) lds_add(lds, L_SY + 4);
      STAMP(4);
    } else if (r == C0 + 3) {
#pragma unroll
      for (int k = 0; k < t + 1 - PVL; ++k) pv(k);
    } else if (r > C0 + 3 && r < 15) {
      pv(t - PVL);
    }
    if constexpr (!early && ((PM >> (r - 1)) & 1)) {
      SCHED_FENCE();
      constexpr int p = r - 1;
      const float m = wave_sum((float)(lat[1] == tag && lat[0] < UMISS + 64 ? lat[0] : UMISS + 64)) * (1.f / 64) - 64.f;
      int d = 0;
      if (p <= 8) {
        kslow = m >= ATHR;
        if (kslow) d = (int)(m * (ASH / 4.f)) + AGX * (lane <= p ? 0 : lane < 8 ? lane - p : 8 - p);
      } else if (!vslow && m >= (p <= 10 ? (kslow ? UTA : UTHR) : UTH2)) {
        vslow = true;
        d = (int)(fmaxf(m, 0.f) * (USH / 4.f)) + USA + (lane > p ? UGX * (lane - p) : 0);
      } else if (m >= RTHR) {
        d = (int)(m * (RSH / 4.f));
      }
      if (w == MW && lane < 16) reinterpret_cast<unsigned*>(lds)[L_CK + lane] += d;
    }
  });
  asm volatile("" ::"v"(tch));
  SCHED_FENCE();
  const __amdgpu_buffer_rsrc_t rwo = rsrc(wo + (size_t)h * DH * D, DH * D * 2);
  lds_wait(lds, L_SY + 4, 8u);
  float f, M, L;
  wg_ml(lds, M, L);
  f = ex2((mw - M) * LOG2E) / L;
  const unsigned cw = fbits(fmaf(M, LOG2E, __builtin_amdgcn_logf(L)));
  if (!early) tag = __builtin_amdgcn_readfirstlane(reinterpret_cast<const unsigned*>(lds)[L_SY + 3]);
  PIN(f);
  SCHED_FENCE();
  const int l40 = lane < 40 ? lane : 39;
  u64* rdst = gw + G2 + (size_t)rec * RS + 32 * r4 + 2 * c16;
  u64* zdst = gw + GZ + (size_t)(fr * 5 + (l40 >> 3)) * YB + 32 * gI + 16 * (gI >> 4) + 2 * (l40 & 7);
  unsigned pdst = lds_addr(lds + L_PART + w * DH + 8 * c + g), psrc = lds_addr(lds + L_PART + 48 * r4 + 4 * c16);
  unsigned cnt6 = lds_addr(lds + L_SY + 6), one = 1u;
  u32x4 rq = {cw, tag, cw, tag};
  u64 m48 = 0x0fff0fff0fff0fffull, m52 = 0x1fff1fff1fff1fffull;
  asm volatile("" : "+s"(m48), "+s"(m52), "+{v[156:159]}"(rq));
  PIN(rdst);
  PIN(zdst);
  PIN(pdst);
  PIN(psrc);
  PIN(cnt6);
  PIN(one);
#pragma unroll
  for (int k = 7 - PVL; k < 7; ++k) pv(k);
  auto wo1 = [&]() {
    if (WT_) pace<T_LAST + WT_, 15>(clk, ph);
    SCHED_FENCE();
#pragma unroll
    for (int i = 0; i < W1N; ++i) WO[i] = __builtin_amdgcn_raw_buffer_load_b128(rwo, wooff, i * (4 * D * 2), 2);
    SCHED_FENCE();
  };
  {
    unsigned plo = pb[7], phi = plo << 16;
    PIN(plo);
    PIN(phi);
    sfor<0, 3>([&](auto J) {
      constexpr int j = J.v;
      SCHED_FENCE();
      float a[8];
#pragma unroll
      for (int i = 0; i < 4; ++i) {
        a[2 * i] = dot2(V[7][j][i], plo, acc[8 * j + 2 * i]);
        a[2 * i + 1] = dot2(V[7][j][i], phi, acc[8 * j + 2 * i + 1]);
      }
      xg_store<256 * j>(a, f, pdst);
    });
    if (early) wo1();
  }
  STAMP(5);
  unsigned arr;
  asm volatile(
      "s_mov_b64 exec, 1\n\tds_add_rtn_u32 v112, %[cnt], %[one]\n\ts_mov_b64 exec, %[m48]\n\t" SUM8("%[src]")
      "v_readfirstlane_b32 %[arr], v112\n\t" SUM8B
      "s_cmp_eq_u32 %[arr], 7\n\t" SUM8C
      "v_cvt_pk_bf16_f32 v156, v116, v117\n\tv_cvt_pk_bf16_f32 v158, v118, v119\n\t"
      "s_cselect_b64 exec, %[m52], 0\n\tglobal_store_dwordx4 %[dst], v[156:159], off sc1\n\ts_mov_b64 exec, -1\n\ts_nop 0"
      : [arr] "=&s"(arr), "+{v[156:159]}"(rq)
      : [cnt] "v"(cnt6), [one] "v"(one), [m48] "s"(m48), [m52] "s"(m52), [src] "v"(psrc), [dst] "v"(rdst)
      : CLOB_T, "scc", "memory");
  if (arr == 0 && isF) {
    const float2* zp = reinterpret_cast<const float2*>(lds + L_Z) + l40;
    const float2 z0 = zp[0], z1 = zp[40], z2 = zp[80], z3 = zp[120];
    gst16(zdst, fbits((z0.x + z1.x) + (z2.x + z3.x)), fbits((z0.y + z1.y) + (z2.y + z3.y)), tag);
  }
  if constexpr (!early) {
    if (w == 7 && h < 5) {
      const int col = 80 * sp + 16 * h + 2 * c + (lane >> 5);
      const float xv = bf_lo(xs[col]);
      const u64* py = gw + GZ + (size_t)(sp * 5 + h) * YB + 32 * g + 2 * c + 392;
      bf16* po = out + col;
      float gt = 0.f;
      u64 m3 = 0ull, ms = 0ull;
      PIN(po);
#pragma unroll 1
      for (int pass = 0; pass < 2; ++pass) {
        lds_wait(lds, L_SY + (pass ? 1 : 6), pass ? 4u : 8u);
        STAMP(8);
        asm volatile("" : "+s"(m3), "+s"(ms));
        float t;
        unsigned n;
        asm volatile(
            "v_mov_b32 v124, 0\n\tv_mov_b32 v125, %[tag]\n\tv_mov_b32 v126, 0\n\tv_mov_b32 v127, %[tag]\n\t"
            "s_mov_b64 vcc, -1\n\ts_mov_b32 %[n], 0\n"
            ".Lfp%=:\n\t"
            "s_add_u32 %[n], %[n], 1\n\ts_cmp_gt_u32 %[n], %[max]\n\ts_cbranch_scc1 .Lfd%=\n\t"
            "s_mov_b64 exec, vcc\n\t"
            "global_load_dwordx4 v[112:115], %[p], off offset:-3136 sc1\n\t"
            "global_load_dwordx4 v[116:119], %[p], off offset:-1088 sc1\n\t"
            "global_load_dwordx4 v[120:123], %[p], off offset:1088 sc1\n\t"
            "s_and_b64 exec, vcc, %[m3]\n\t"
            "global_load_dwordx4 v[124:127], %[p], off offset:3136 sc1\n\t"
            "s_mov_b64 exec, -1\n\ts_waitcnt vmcnt(0)\n\t"
            "v_bitop3_b32 v128, v113, v115, %[tag] bitop3:0x7e\n\tv_bitop3_b32 v129, v117, v119, %[tag] bitop3:0x7e\n\t"
            "v_bitop3_b32 v130, v121, v123, %[tag] bitop3:0x7e\n\tv_bitop3_b32 v131, v125, v127, %[tag] bitop3:0x7e\n\t"
            "v_or3_b32 v128, v128, v129, v130\n\tv_add_f32 v132, v112, v116\n\tv_or_b32 v128, v128, v131\n\t"
            "v_add_f32 v134, v120, v124\n\tv_cmp_ne_u32 vcc, 0, v128\n\tv_add_f32 v133, v114, v118\n\t"
            "s_cbranch_vccnz .Lfp%=\n"
            ".Lfd%=:\n\t"
            "v_add_f32 v135, v122, v126\n\tv_add_f32 v132, v132, v134\n\tv_add_f32 v133, v133, v135\n\ts_nop 1\n\t"
            "v_permlane32_swap_b32 v132, v133\n\tv_add_f32 v134, v132, v133\n\tv_add_f32 v135, v132, v133\n\ts_nop 1\n\t"
            "v_permlane16_swap_b32 v134, v135\n\tv_add_f32 v134, v134, v135\n\ts_nop 1\n\t"
            "v_add_f32_dpp v134, v134, v134 row_ror:8 row_mask:0xf bank_mask:0xf\n\t"
            "v_cvt_pk_bf16_f32 v134, v134, v134\n\tv_lshlrev_b32 %[t], 16, v134\n\t"
            "v_mul_f32 v134, %[t], %[gt]\n\tv_cvt_pk_bf16_f32 v134, v134, v134\n\tv_lshlrev_b32 v134, 16, v134\n\t"
            "v_add_f32 v134, %[xv], v134\n\tv_cvt_pk_bf16_f32 v134, v134, v134\n\t"
            "s_mov_b64 exec, %[ms]\n\tglobal_store_short %[po], v134, off sc1\n\ts_mov_b64 exec, -1"
            : [t] "=&v"(t), [n] "=&s"(n)
            : [tag] "s"(tag), [max] "n"(SPIN_MAX), [p] "v"(py), [m3] "s"(m3), [ms] "s"(ms), [gt] "v"(gt), [xv] "v"(xv), [po] "v"(po)
            : CLOB_T, "vcc", "scc", "memory");
        if (pass) break;
        gt = bfr(silu(t));
        py -= GZ - GY;
        m3 = ~0ull;
        ms = 0x000000ff000000ffull;
      }
      if (b == 0 && lane == 0)
        __hip_atomic_store(reinterpret_cast<unsigned*>(gw + GEP), tag, __ATOMIC_RELAXED, __HIP_MEMORY_SCOPE_AGENT);
      STAMP(10);
    }
    STAMP_FLUSH();
    asm volatile(".Lcode_lastb:");
    return;
  }
  pace<T_LAST + W2T, 15>(clk, ph);
#pragma unroll
  for (int i = W1N; i < 6; ++i) WO[i] = __builtin_amdgcn_raw_buffer_load_b128(rwo, wooff, i * (4 * D * 2), 2);
  unsigned dm0 = __builtin_amdgcn_raw_buffer_load_b32(rwo, NOWHERE, 0, 2), dm1 = __builtin_amdgcn_raw_buffer_load_b32(rwo, NOWHERE, 4, 2);
  STAMP(3);
  SCHED_FENCE();
  const int ly = isF ? lane & 31 : lane;
  u64* ydst = gw + GY + (size_t)(4 * sp + (ly >> 3) + 4 * (ly >> 5)) * YB + 32 * h + 16 * (h >> 4) + 2 * (ly & 7);
  static_assert(RS * 8 * 4 == 4608, "record r4 + 4 is 4608 bytes behind record r4");
  const u64* pr = gw + G2 + (size_t)(h * NSP + r4 + 2) * RS + 32 * w + 2 * c16;
  const u64 ownm = 0xffffull << (16 * (sp & 3));
  u64 own0 = sp < 4 ? ownm : 0ull, own1 = sp < 4 ? 0ull : ownm, my = isF ? 0xffffffffull : ~0ull;
  u64 for0 = m52 & ~own0, for1 = m52 & ~own1;
  own0 &= m48;
  own1 &= m48;
  unsigned osrc = lds_addr(lds + L_PART + 48 * w + 4 * c16), ysrc = lds_addr(lds + L_Y + 2 * ly), cnt1 = lds_addr(lds + L_SY + 1);
  unsigned ydl = lds_addr(lds + L_Y + 128 * w + (isF ? 8 * c + g : 8 * c16 + 2 * r4));
  asm volatile("" : "+s"(own0), "+s"(own1), "+s"(for0), "+s"(for1), "+s"(my));
  PIN(ydst);
  PIN(pr);
  PIN(osrc);
  PIN(ysrc);
  PIN(cnt1);
  PIN(ydl);
  u32x8 WP[3], XP[3];
#pragma unroll
  for (int p = 0; p < 3; ++p)
#pragma unroll
    for (int k = 0; k < 4; ++k) {
      XP[p][2 * k] = __builtin_amdgcn_perm(X[2 * p + 1][k], X[2 * p][k], 0x05040100u);
      XP[p][2 * k + 1] = __builtin_amdgcn_perm(X[2 * p + 1][k], X[2 * p][k], 0x07060302u);
    }
  asm volatile("" : "+{v[64:71]}"(XP[0]), "+{v[72:79]}"(XP[1]), "+{v[80:87]}"(XP[2]));
  SCHED_FENCE();
  auto perm_wo = [&]() {
#pragma unroll
    for (int p = 0; p < 3; ++p)
#pragma unroll
      for (int k = 0; k < 4; ++k) {
        WP[p][2 * k] = __builtin_amdgcn_perm(WO[2 * p + 1][k], WO[2 * p][k], 0x05040100u);
        WP[p][2 * k + 1] = __builtin_amdgcn_perm(WO[2 * p + 1][k], WO[2 * p][k], 0x07060302u);
      }
    asm volatile("" : "+{v[88:95]}"(WP[0]), "+{v[96:103]}"(WP[1]), "+{v[104:111]}"(WP[2]));
    SCHED_FENCE();
    STAMP(1);
  };
  lds_wait(lds, L_SY + 6, 8u);
  if (PT_) pace<T_LAST + PT_, 15>(clk, 0);
  STAMP(4);
  u32x4 ra0, ra1;
  asm volatile(
      "v_mov_b32 v148, %[cw]\n\tv_mov_b32 v149, %[tag]\n\tv_mov_b32 v151, %[tag]\n\t"
      "v_mov_b32 v152, %[cw]\n\tv_mov_b32 v153, %[tag]\n\tv_mov_b32 v155, %[tag]\n\t"
      "s_mov_b64 exec, %[for0]\n\tglobal_load_dwordx4 v[148:151], %[p], off offset:-2304 sc1\n\t"
      "s_mov_b64 exec, %[for1]\n\tglobal_load_dwordx4 v[152:155], %[p], off offset:2304 sc1\n\t"
      "s_mov_b64 exec, %[m48]\n\t" SUM8("%[src]") SUM8B SUM8C
      "s_mov_b64 exec, %[own0]\n\tv_cvt_pk_bf16_f32 v148, v116, v117\n\tv_cvt_pk_bf16_f32 v150, v118, v119\n\t"
      "s_mov_b64 exec, %[own1]\n\tv_cvt_pk_bf16_f32 v152, v116, v117\n\tv_cvt_pk_bf16_f32 v154, v118, v119\n\t"
      "s_mov_b64 exec, -1"
      : "=&{v[148:151]}"(ra0), "=&{v[152:155]}"(ra1)
      : [cw] "v"(cw), [tag] "s"(tag), [for0] "s"(for0), [for1] "s"(for1), [p] "v"(pr), [own0] "s"(own0), [own1] "s"(own1),
        [m48] "s"(m48), [src] "v"(osrc)
      : CLOB_T, "memory");
  SCHED_FENCE();
  perm_wo();
  unsigned np;
  asm volatile(
      "s_mov_b32 %[n], 0\n\t"
      "s_mov_b64 exec, -1\n\tv_mov_b64 v[160:161], 0\n\tv_mov_b64 v[162:163], 0\n\tv_mov_b64 v[164:165], 0\n\t"
      "v_mov_b64 v[166:167], 0\n\ts_branch .Lrc%=\n"
      ".Lrp%=:\n\t"
      "s_add_u32 %[n], %[n], 1\n\ts_cmp_gt_u32 %[n], %[max]\n\ts_cbranch_scc1 .Lrd%=\n\t"
      "v_cmp_ne_u32 vcc, 0, v112\n\tv_cmp_ne_u32 s[98:99], 0, v113\n\ts_sleep " STR(RSLEEP) "\n\t"
      "s_mov_b64 exec, vcc\n\tglobal_load_dwordx4 v[148:151], %[p], off offset:-2304 sc1\n\t"
      "s_mov_b64 exec, s[98:99]\n\tglobal_load_dwordx4 v[152:155], %[p], off offset:2304 sc1\n\t"
      "s_mov_b64 exec, -1\n"
      ".Lrc%=:\n\t"
      "s_waitcnt vmcnt(0)\n\t"
      "v_bitop3_b32 v112, v149, v151, %[tag] bitop3:0x7e\n\tv_bitop3_b32 v113, v153, v155, %[tag] bitop3:0x7e\n\t"
      "v_max_f32 v115, v148, v152\n\tv_or_b32 v114, v112, v113\n\tv_max_f32 v116, v148, v152\n\t"
      "v_cmp_ne_u32 vcc, 0, v114\n\tv_lshlrev_b32 v122, 16, v148\n\ts_cbranch_vccnz .Lrp%=\n"
      ".Lrd%=:\n\t"
      "v_permlane16_swap_b32 v115, v116\n\tv_max_f32 v117, v115, v116\n\tv_max_f32 v118, v115, v116\n\t"
      "v_and_b32 v123, 0xffff0000, v148\n\tv_lshlrev_b32 v124, 16, v150\n\t"
      "v_permlane32_swap_b32 v117, v118\n\tv_max_f32 v117, v117, v118\n\t"
      "v_sub_f32 v115, v148, v117\n\tv_sub_f32 v116, v152, v117\n\tv_exp_f32 v115, v115\n\tv_exp_f32 v116, v116\n\t"
      "v_and_b32 v125, 0xffff0000, v150\n\tv_lshlrev_b32 v126, 16, v152\n\t"
      "v_add_f32 v117, v115, v116\n\tv_add_f32 v118, v115, v116\n\t"
      "v_mul_f32_dpp v122, v115, v122" BC12 "v_mul_f32_dpp v123, v115, v123" BC12
      "v_permlane16_swap_b32 v117, v118\n\tv_add_f32 v119, v117, v118\n\tv_add_f32 v120, v117, v118\n\t"
      "v_mul_f32_dpp v124, v115, v124" BC12 "v_mul_f32_dpp v125, v115, v125" BC12
      "v_permlane32_swap_b32 v119, v120\n\tv_add_f32 v119, v119, v120\n\tv_lshlrev_b32 v128, 16, v154\n\t"
      "v_rcp_f32 v119, v119\n\tv_and_b32 v127, 0xffff0000, v152\n\tv_and_b32 v129, 0xffff0000, v154\n\t"
      "v_fmac_f32_dpp v122, v116, v126" BC12 "v_fmac_f32_dpp v124, v116, v128" BC12
      "v_fmac_f32_dpp v123, v116, v127" BC12 "v_fmac_f32_dpp v125, v116, v129" BC12
      "v_permlane32_swap_b32 v122, v124\n\tv_add_f32 v122, v122, v124\n\t"
      "v_permlane32_swap_b32 v123, v125\n\tv_add_f32 v123, v123, v125\n\t"
      "v_mov_b32_dpp v120, v119" BC12 "s_nop 0\n\t"
      "v_permlane16_swap_b32 v122, v123\n\tv_add_f32 v122, v122, v123\n\t"
      "v_mul_f32 v123, v122, v120\n\ts_cmp_lt_u32 %[sp], 6\n\t"
      "v_mul_f32_dpp v124, v122, v120 quad_perm:[1,0,3,2] row_mask:0xf bank_mask:0xf\n\t"
      "v_cvt_pk_bf16_f32 v124, v123, v124\n\t"
      "s_cbranch_scc1 .Lyf%=\n\ts_nop 0\n\t"
      DOT8(64, 65, 66, 67, 68, 69, 70, 71, 0) DOT8(72, 73, 74, 75, 76, 77, 78, 79, 2) DOT8(80, 81, 82, 83, 84, 85, 86, 87, 4)
      DOT8(88, 89, 90, 91, 92, 93, 94, 95, 6) DOT8(96, 97, 98, 99, 100, 101, 102, 103, 8)
      DOT8(104, 105, 106, 107, 108, 109, 110, 111, 10) YRED
      "v_pk_add_f32 v[160:161], v[160:161], v[162:163]\n\tds_write_b64 %[ydl], v[160:161]\n"
      ".Lys%=:\n\t"
      "s_mov_b64 exec, 1\n\tds_add_rtn_u32 v112, %[cnt], %[one]\n\ts_mov_b64 exec, -1\n\t"
      "ds_read2st64_b64 v[116:119], %[ysrc] offset0:0 offset1:1\n\tds_read2st64_b64 v[120:123], %[ysrc] offset0:2 offset1:3\n\t"
      "s_waitcnt lgkmcnt(2)\n\tv_readfirstlane_b32 s98, v112\n\ts_waitcnt lgkmcnt(1)\n\t"
      "v_pk_add_f32 v[116:117], v[116:117], v[118:119]\n\ts_cmp_eq_u32 s98, 3\n\ts_cselect_b64 exec, %[my], 0\n\t"
      "s_waitcnt lgkmcnt(0)\n\tv_pk_add_f32 v[120:121], v[120:121], v[122:123]\n\ts_nop 0\n\t"
      "v_add_f32 v156, v116, v120\n\tv_add_f32 v158, v117, v121\n\t"
      "global_store_dwordx4 %[dst], v[156:159], off sc1\n\ts_mov_b64 exec, -1\n\ts_branch .Lye%=\n"
      ".Lyf%=:\n\ts_nop 0\n\t"
      "v_mov_b32_dpp v125, v124 row_newbcast:0 row_mask:0xf bank_mask:0x3\n\t"
      "v_mov_b32_dpp v126, v124 row_newbcast:2 row_mask:0xf bank_mask:0x3\n\t"
      "v_mov_b32_dpp v127, v124 row_newbcast:4 row_mask:0xf bank_mask:0x3\n\t"
      "v_mov_b32_dpp v125, v124 row_newbcast:6 row_mask:0xf bank_mask:0xc\n\t"
      "v_mov_b32_dpp v126, v124 row_newbcast:8 row_mask:0xf bank_mask:0xc\n\t"
      "v_mov_b32_dpp v127, v124 row_newbcast:10 row_mask:0xf bank_mask:0xc\n\t"
      DOT8F(88, 89, 90, 91, 92, 93, 94, 95, 125) DOT8F(96, 97, 98, 99, 100, 101, 102, 103, 126)
      DOT8F(104, 105, 106, 107, 108, 109, 110, 111, 127) YRED
      "v_add_f32 v160, v160, v162\n\tv_add_f32 v161, v161, v163\n\ts_nop 0\n\t"
      "v_add_f32_dpp v160, v160, v160 row_ror:8 row_mask:0xf bank_mask:0x3\n\t"
      "v_add_f32_dpp v160, v161, v161 row_ror:8 row_mask:0xf bank_mask:0xc\n\t"
      "ds_write_b32 %[ydl], v160\n\ts_branch .Lys%=\n"
      ".Lye%=:"
      : [n] "=&s"(np), "+{v[156:159]}"(rq), "+{v[148:151]}"(ra0), "+{v[152:155]}"(ra1)
      : [tag] "s"(tag), [p] "v"(pr), [max] "n"(SPIN_MAX), [sp] "s"(sp), [ydl] "v"(ydl), [cnt] "v"(cnt1), [one] "v"(one),
        [ysrc] "v"(ysrc), [my] "s"(my), [dst] "v"(ydst), "{v[64:71]}"(XP[0]), "{v[72:79]}"(XP[1]), "{v[80:87]}"(XP[2]),
        "{v[88:95]}"(WP[0]), "{v[96:103]}"(WP[1]), "{v[104:111]}"(WP[2])
      : CLOB_T, "v160", "v161", "v162", "v163", "v164", "v165", "v166", "v167", "s98", "s99", "vcc", "scc", "memory");
  STAMP(9);
  asm volatile("" ::"v"(dm0), "v"(dm1));
  STAMP_FLUSH();
  asm volatile(".Lcode_last:");
}

__global__ void __launch_bounds__(NT, 2)
fused_decode(const bf16* __restrict__ wq, const bf16* __restrict__ w1, const bf16* __restrict__ w3, u64* __restrict__ gw,
             const bf16* __restrict__ x, const bf16* __restrict__ kc, const bf16* __restrict__ vc,
             const bf16* __restrict__ wo, const bf16* __restrict__ w2, bf16* __restrict__ out) {
  __shared__ __attribute__((aligned(16))) float lds[L_END];
  const int b = blockIdx.x;
  const int xcd = b & 7, jj = b >> 3;
  const int h = xcd * 4 + (jj >> 3), sp = jj & 7;
  const int w = __builtin_amdgcn_readfirstlane(threadIdx.x >> 6);
  unsigned cpf = threadIdx.x;
  {
    u64 pc;
    unsigned len;
    asm volatile(
        "s_cmp_lt_u32 %[w], 4\n\ts_cbranch_scc1 .Lnocpf\n\t"
        "s_getpc_b64 %[pc]\n.Lcode_here:\n\tv_xor_b32 %[c], %[hi], %[c]\n\tv_lshlrev_b32 %[c], 6, %[c]\n\t"
        "s_mov_b32 %[len], .Lcode_last-.Lcode_here-4\n\ts_max_i32 %[len], %[len], .Lcode_lastb-.Lcode_here-4\n\t"
        "v_min_u32 %[c], %[len], %[c]\n\tglobal_load_dword %[c], %[c], %[pc]\n.Lnocpf:"
        : [c] "+v"(cpf), [pc] "=&s"(pc), [len] "=&s"(len) : [w] "s"(w), [hi] "s"((b & 24) << 5) : "scc", "memory");
  }
  SCHED_FENCE();
  if (w >= 4)
    body<false>(lds, h, sp, w, x, kc, vc, wq, wo, w1, w3, w2, gw, out, cpf);
  else
    body<true>(lds, h, sp, w, x, kc, vc, wq, wo, w1, w3, w2, gw, out, cpf);
}

}

extern "C" void kernel_launch(void* const* d_in, const int* in_sizes, int n_in, void* d_out, int out_size, void* d_ws,
                              size_t ws_size, hipStream_t stream) {
  const bf16* x = (const bf16*)d_in[0];
  const bf16* kc = (const bf16*)d_in[1];
  const bf16* vc = (const bf16*)d_in[2];
  const bf16* wq = (const bf16*)d_in[3];
  const bf16* wo = (const bf16*)d_in[4];
  const bf16* w1 = (const bf16*)d_in[6];
  const bf16* w3 = (const bf16*)d_in[7];
  const bf16* w2 = (const bf16*)d_in[8];
  u64* gw = (u64*)d_ws;
  fused_decode<<<NWG, NT, 0, stream>>>(wq, w1, w3, gw, x, kc, vc, wo, w2, (bf16*)d_out);
}
```

```cpp
#define TEL 2
#include <hip/hip_runtime.h>
#include <hip/hip_bf16.h>
#include <stdint.h>

typedef __hip_bfloat16 bf16;
typedef unsigned long long u64;
typedef unsigned u32x4 __attribute__((ext_vector_type(4)));
typedef unsigned u32x8 __attribute__((ext_vector_type(8)));
typedef float f32x4 __attribute__((ext_vector_type(4)));
typedef float f32x2 __attribute__((ext_vector_type(2)));
typedef __bf16 bf16x2_t __attribute__((ext_vector_type(2)));

namespace {

constexpr int D = 640, HQ = 32, DH = 192, SK = 32768, WIN = 4096, FF = 1536;
constexpr int NSP = 8;
constexpr int NWG = HQ * NSP;
constexpr int NT = 512;
constexpr float SCALE = 0.07216878364870322f;
constexpr float LOG2E = 1.4426950408889634f;
constexpr unsigned SPIN_MAX = 1u << 16;
constexpr int SC1 = 16;
constexpr int NOWHERE = 1 << 30;

#define PV_ 81
#define PK_ 82
#define PX_ PK_
#define TS_ 358
#define SB_ 246
#define LOC 30
#define WT_ 98
#define W2T 132
#define W1N 3
#define PT_ 215
#define NFL 7
#define CE 7
#define CL 8
#define AR 3
#define GR 5
#define GRW 2
#define RSLEEP 1
#define MW 4
#define PM 0x1440
#define MEAS 1
#define ATHR 20
#define ASH 4
#define AGX 7
#define UTHR 8
#define UTH2 40
#define USH 4
#define UGX 9
#define ULIM 20
#define UMISS 60
#define PVL 1
#define PB0 0
#define LSP 16
#define GLP 1
#define E1P 1
#define UTA 3
#define PRI 0
#define USH2 3
#define CT 10
#define HROT 16
constexpr int t_round(int r) { return r <= 2 ? TS_ : t_round(r - 1) + (r > 8 ? PV_ : r == 8 ? PX_ : PK_); }
constexpr int T_LAST = t_round(15);
constexpr int t_late(int i) { return t_round(i); }
constexpr int t_early(int i) { return t_round(i); }

constexpr int QS = 144;
constexpr int YB = 2 * (16 * 32 + 16);
constexpr int RS = 144;
constexpr size_t GQ = 0;
constexpr size_t G2 = GQ + (size_t)NWG * QS;
constexpr size_t GY = G2 + (size_t)NWG * RS;
constexpr size_t GZ = GY + 16;
constexpr size_t GEP = GY + (size_t)NSP * 5 * YB;
constexpr size_t GL = GEP + 16;

__device__ __forceinline__ unsigned fbits(float x) { return __builtin_bit_cast(unsigned, x); }
__device__ __forceinline__ u64 gld(const u64* p) {
  return __hip_atomic_load(p, __ATOMIC_RELAXED, __HIP_MEMORY_SCOPE_AGENT);
}
__device__ __forceinline__ float bf_lo(unsigned u) { return __builtin_bit_cast(float, u << 16); }
__device__ __forceinline__ float bf_hi(unsigned u) { return __builtin_bit_cast(float, u & 0xffff0000u); }
__device__ __forceinline__ float bfr(float x) { return __bfloat162float(__float2bfloat16(x)); }
__device__ __forceinline__ unsigned short bfbits(float x) {
  return __builtin_bit_cast(unsigned short, __float2bfloat16(x));
}
__device__ __forceinline__ float dot2(unsigned a, unsigned b, float c) {
  return __builtin_amdgcn_fdot2_f32_bf16(__builtin_bit_cast(bf16x2_t, a), __builtin_bit_cast(bf16x2_t, b), c, false);
}
template <int CTRL>
__device__ __forceinline__ float dpp(float x) {
  return __builtin_bit_cast(float, __builtin_amdgcn_mov_dpp(__builtin_bit_cast(int, x), CTRL, 0xf, 0xf, true));
}
__device__ __forceinline__ float grp8_sum(float x) {
  x += dpp<0xB1>(x);
  x += dpp<0x4E>(x);
  x += dpp<0x141>(x);
  return x;
}
__device__ __forceinline__ float sw16(float a, float b) {
  auto s = __builtin_amdgcn_permlane16_swap(fbits(a), fbits(b), false, false);
  return __builtin_bit_cast(float, (unsigned)s[0]) + __builtin_bit_cast(float, (unsigned)s[1]);
}
__device__ __forceinline__ float sw32(float a, float b) {
  auto s = __builtin_amdgcn_permlane32_swap(fbits(a), fbits(b), false, false);
  return __builtin_bit_cast(float, (unsigned)s[0]) + __builtin_bit_cast(float, (unsigned)s[1]);
}
__device__ __forceinline__ float xg_sum(float x) {
  x += dpp<0x128>(x);
  x = sw16(x, x);
  return sw32(x, x);
}
__device__ __forceinline__ float xg_max(float x) {
  x = fmaxf(x, dpp<0x128>(x));
  auto s = __builtin_amdgcn_permlane16_swap(fbits(x), fbits(x), false, false);
  x = fmaxf(__builtin_bit_cast(float, (unsigned)s[0]), __builtin_bit_cast(float, (unsigned)s[1]));
  auto t = __builtin_amdgcn_permlane32_swap(fbits(x), fbits(x), false, false);
  return fmaxf(__builtin_bit_cast(float, (unsigned)t[0]), __builtin_bit_cast(float, (unsigned)t[1]));
}
__device__ __forceinline__ float wave_sum(float x) { return xg_sum(grp8_sum(x)); }
__device__ __forceinline__ float ex2(float x) { return __builtin_amdgcn_exp2f(x); }
__device__ __forceinline__ float silu(float x) { return x / (1.0f + __expf(-x)); }
__device__ __forceinline__ float xg_reduce8(const float (&a)[8], bool g0) {
  float r[4];
#pragma unroll
  for (int i = 0; i < 4; ++i) r[i] = sw32(a[i], a[i + 4]);
  const float s0 = sw16(r[0], r[2]), s1 = sw16(r[1], r[3]);
  const float keep = g0 ? s1 : s0, send = g0 ? s0 : s1;
  return keep + dpp<0x128>(send);
}
__device__ __forceinline__ __amdgpu_buffer_rsrc_t rsrc(const void* p, unsigned bytes) {
  return __builtin_amdgcn_make_buffer_rsrc(const_cast<void*>(p), 0, bytes, 0x00020000);
}
__device__ __forceinline__ void gst16(u64* p, unsigned v0, unsigned v1, unsigned tag) {
  const u32x4 o = {v0, tag, v1, tag};
  asm volatile("global_store_dwordx4 %0, %1, off sc1\n\ts_nop 1" ::"v"(p), "v"(o) : "memory");
}
__device__ __forceinline__ unsigned bfpack(float a, float b) {
  return __builtin_bit_cast(unsigned, __builtin_convertvector(f32x2{a, b}, bf16x2_t));
}
__device__ __forceinline__ float2 rows_reduce(const float (&v)[8]) {
  float r[4];
#pragma unroll
  for (int i = 0; i < 4; ++i) r[i] = sw32(v[i], v[i + 4]);
  return float2{sw16(r[0], r[2]), sw16(r[1], r[3])};
}
template <int OFF>
__device__ __forceinline__ void xg_store(const float (&v)[8], float f, unsigned a) {
  float p[4], q[4], t, u;
#pragma unroll
  for (int i = 0; i < 4; ++i) {
    auto s = __builtin_amdgcn_permlane32_swap(fbits(v[i]), fbits(v[i + 4]), false, false);
    p[i] = __builtin_bit_cast(float, (unsigned)s[0]);
    q[i] = __builtin_bit_cast(float, (unsigned)s[1]);
  }
  const f32x2 r01 = f32x2{p[0], p[1]} + f32x2{q[0], q[1]}, r23 = f32x2{p[2], p[3]} + f32x2{q[2], q[3]};
  auto s0 = __builtin_amdgcn_permlane16_swap(fbits(r01[0]), fbits(r23[0]), false, false);
  auto s1 = __builtin_amdgcn_permlane16_swap(fbits(r01[1]), fbits(r23[1]), false, false);
  asm volatile("v_add_f32 %0, %2, %3\n\tv_add_f32 %1, %4, %5\n\ts_nop 0\n\t"
               "v_add_f32_dpp %0, %0, %0 row_ror:8 row_mask:0xf bank_mask:0x3\n\t"
               "v_add_f32_dpp %0, %1, %1 row_ror:8 row_mask:0xf bank_mask:0xc\n\t"
               "v_mul_f32 %0, %6, %0\n\tds_write_b32 %7, %0 offset:%8"
               : "=&v"(t), "=&v"(u)
               : "v"((unsigned)s0[0]), "v"((unsigned)s0[1]), "v"((unsigned)s1[0]), "v"((unsigned)s1[1]), "v"(f), "v"(a), "n"(OFF)
               : "memory");
}
#define SUM8(a)                                                                                                          \
  "ds_read_b128 v[116:119], " a "\n\tds_read_b128 v[120:123], " a " offset:768\n\t"                                      \
  "ds_read_b128 v[124:127], " a " offset:1536\n\tds_read_b128 v[128:131], " a " offset:2304\n\t"                         \
  "ds_read_b128 v[132:135], " a " offset:3072\n\tds_read_b128 v[136:139], " a " offset:3840\n\t"                         \
  "ds_read_b128 v[140:143], " a " offset:4608\n\tds_read_b128 v[144:147], " a " offset:5376\n\ts_waitcnt lgkmcnt(6)\n\t"
#define SUM8B                                                                                                            \
  "v_pk_add_f32 v[116:117], v[116:117], v[120:121]\n\tv_pk_add_f32 v[118:119], v[118:119], v[122:123]\n\t"               \
  "s_waitcnt lgkmcnt(4)\n\t"                                                                                             \
  "v_pk_add_f32 v[124:125], v[124:125], v[128:129]\n\tv_pk_add_f32 v[126:127], v[126:127], v[130:131]\n\t"               \
  "v_pk_add_f32 v[116:117], v[116:117], v[124:125]\n\tv_pk_add_f32 v[118:119], v[118:119], v[126:127]\n\t"
#define SUM8C                                                                                                            \
  "s_waitcnt lgkmcnt(2)\n\t"                                                                                             \
  "v_pk_add_f32 v[132:133], v[132:133], v[136:137]\n\tv_pk_add_f32 v[134:135], v[134:135], v[138:139]\n\t"               \
  "s_waitcnt lgkmcnt(0)\n\t"                                                                                             \
  "v_pk_add_f32 v[140:141], v[140:141], v[144:145]\n\tv_pk_add_f32 v[142:143], v[142:143], v[146:147]\n\t"               \
  "v_pk_add_f32 v[132:133], v[132:133], v[140:141]\n\tv_pk_add_f32 v[134:135], v[134:135], v[142:143]\n\t"               \
  "v_pk_add_f32 v[116:117], v[116:117], v[132:133]\n\tv_pk_add_f32 v[118:119], v[118:119], v[134:135]\n\t"
#define BC12 " row_newbcast:12 row_mask:0xf bank_mask:0xf\n\t"
#define DOTB(y, a, n) "v_dot2c_f32_bf16_dpp v" #y ", v124, v" #a " row_newbcast:" #n " row_mask:0xf bank_mask:0xf\n\t"
#define DOT8(a, b, c, d, e, f, g, h, n) \
  DOTB(160, a, n) DOTB(161, b, n) DOTB(162, c, n) DOTB(163, d, n) DOTB(164, e, n) DOTB(165, f, n) DOTB(166, g, n) DOTB(167, h, n)
#define DOTF(y, a, n) "v_dot2c_f32_bf16 v" #y ", v" #n ", v" #a "\n\t"
#define DOT8F(a, b, c, d, e, f, g, h, n) \
  DOTF(160, a, n) DOTF(161, b, n) DOTF(162, c, n) DOTF(163, d, n) DOTF(164, e, n) DOTF(165, f, n) DOTF(166, g, n) DOTF(167, h, n)
#define YRED                                                                                                             \
  "v_permlane32_swap_b32 v160, v164\n\tv_permlane32_swap_b32 v161, v165\n\tv_permlane32_swap_b32 v162, v166\n\t"         \
  "v_permlane32_swap_b32 v163, v167\n\tv_pk_add_f32 v[160:161], v[160:161], v[164:165]\n\t"                              \
  "v_pk_add_f32 v[162:163], v[162:163], v[166:167]\n\ts_nop 1\n\t"                                                       \
  "v_permlane16_swap_b32 v160, v162\n\tv_permlane16_swap_b32 v161, v163\n\t"
#define CLOB_T                                                                                                     \
  "v112", "v113", "v114", "v115", "v116", "v117", "v118", "v119", "v120", "v121", "v122", "v123", "v124", "v125",        \
      "v126", "v127", "v128", "v129", "v130", "v131", "v132", "v133", "v134", "v135", "v136", "v137", "v138", "v139",    \
      "v140", "v141", "v142", "v143", "v144", "v145", "v146", "v147"
#define SCHED_FENCE() __builtin_amdgcn_sched_barrier(0)
#define STR2(x) #x
#define STR(x) STR2(x)
#define THRN(n) do { SCHED_FENCE(); asm volatile("s_waitcnt vmcnt(%0)" ::"n"(n) : "memory"); SCHED_FENCE(); } while (0)
#define NOWT() ((unsigned)__builtin_amdgcn_s_memrealtime())
template <int N>
struct IC {
  static constexpr int v = N;
};
template <int I, int E, class F>
__device__ __forceinline__ void sfor(F&& f) {
  if constexpr (I < E) {
    f(IC<I>{});
    sfor<I + 1, E>(f);
  }
}
#define LDS_DRAIN() asm volatile("s_waitcnt lgkmcnt(0)" ::: "memory")
__device__ __forceinline__ unsigned lds_add(float* lds, int idx) {
  return __hip_atomic_fetch_add(reinterpret_cast<unsigned*>(lds + idx), 1u, __ATOMIC_RELAXED, __HIP_MEMORY_SCOPE_WORKGROUP);
}
__device__ __forceinline__ void lds_wait(float* lds, int idx, unsigned n) {
  SCHED_FENCE();
#pragma unroll 1
  for (unsigned spin = 0; spin < (SPIN_MAX << 4); ++spin) {
    if (__hip_atomic_load(reinterpret_cast<unsigned*>(lds + idx), __ATOMIC_RELAXED, __HIP_MEMORY_SCOPE_WORKGROUP) >= n) break;
    __builtin_amdgcn_s_sleep(1);
  }
  asm volatile("" ::: "memory");
  SCHED_FENCE();
}
template <int T, int R>
__device__ __forceinline__ void pace(unsigned clk, int ph) {
  unsigned o;
  SCHED_FENCE();
  asm volatile(
      "s_branch .Lpb%=\n"
      ".Lpa%=:\n\ts_sleep 1\n"
      ".Lpb%=:\n\tds_read_b32 %0, %1 offset:%4\n\ts_memrealtime s[98:99]\n\ts_waitcnt lgkmcnt(0)\n\tv_readfirstlane_b32 s99, %0\n\t"
      "s_sub_u32 s98, s98, %2\n\ts_sub_u32 s98, s98, s99\n\ts_cmp_lt_i32 s98, %3\n\ts_cbranch_scc1 .Lpa%="
      : "=&v"(o) : "v"(clk), "s"(ph), "n"(T), "n"(4 * R) : "s98", "s99", "scc", "memory");
  SCHED_FENCE();
}

template <int T, int R>
__device__ __forceinline__ unsigned pace_m(unsigned clk, int ph) {
  unsigned o, tl;
  SCHED_FENCE();
  asm volatile(
      "s_mov_b32 %1, 0\n\ts_branch .Lpb%=\n"
      ".Lpa%=:\n\ts_sleep 1\n"
      ".Lpb%=:\n\tds_read_b32 %0, %2 offset:%5\n\ts_getreg_b32 s97, hwreg(HW_REG_IB_STS, 0, 4)\n\ts_memrealtime s[98:99]\n\t"
      "s_or_b32 s97, s97, %1\n\ts_waitcnt lgkmcnt(0)\n\ts_cselect_b32 %1, %1, s98\n\tv_readfirstlane_b32 s99, %0\n\t"
      "s_sub_u32 s98, s98, %3\n\ts_sub_u32 s98, s98, s99\n\ts_cmp_lt_i32 s98, %4\n\ts_cbranch_scc1 .Lpa%="
      : "=&v"(o), "=&s"(tl) : "v"(clk), "s"(ph), "n"(T), "n"(4 * R) : "s97", "s98", "s99", "scc", "memory");
  SCHED_FENCE();
  return tl;
}

#define STAMP(i)
#define STAMP_FLUSH()
#define TT(i)
constexpr int L_QP = 0;
constexpr int L_QS = L_QP + 4 * 128;
constexpr int L_PART = L_QS + 96;
constexpr int L_ML = L_PART + 8 * DH;
constexpr int L_Z = L_ML + 16;
constexpr int L_Y = L_Z + 320;
constexpr int L_H = L_Y + 512;
constexpr int L_SY = L_H + 64;
constexpr int L_CK = L_SY + 8;
constexpr int L_TE = L_CK + 16;
constexpr int L_END = L_TE + 8;

#define PIN(x) asm volatile("" : "+v"(x))

__device__ __forceinline__ void wg_ml(const float* lds, float& M, float& L) {
  float mm[8], ll[8];
#pragma unroll
  for (int i = 0; i < 4; ++i) {
    const f32x4 t = *reinterpret_cast<const f32x4*>(lds + L_ML + 4 * i);
    mm[2 * i] = t[0];
    ll[2 * i] = t[1];
    mm[2 * i + 1] = t[2];
    ll[2 * i + 1] = t[3];
  }
  M = mm[0];
#pragma unroll
  for (int ww = 1; ww < 8; ++ww) M = fmaxf(M, mm[ww]);
  L = 0.f;
#pragma unroll
  for (int ww = 0; ww < 8; ++ww) L = fmaf(ex2((mm[ww] - M) * LOG2E), ll[ww], L);
}
__device__ __forceinline__ unsigned lds_addr(const float* p) {
  return (unsigned)(size_t)(const __attribute__((address_space(3))) float*)p;
}

template <bool early>
__device__ __forceinline__ void body(float* lds, const int h, const int sp, const int w, const bf16* __restrict__ x,
                                     const bf16* __restrict__ kc, const bf16* __restrict__ vc,
                                     const bf16* __restrict__ wq, const bf16* __restrict__ wo,
                                     const bf16* __restrict__ w1, const bf16* __restrict__ w3,
                                     const bf16* __restrict__ w2, u64* __restrict__ gw, bf16* __restrict__ out,
                                     unsigned cpf) {
  const int b = blockIdx.x;
  const int tid = threadIdx.x, lane = tid & 63;
  const int g = lane >> 3, c = lane & 7;
  const int rec = h * NSP + sp;

  STAMP(0);
  const bool isF = sp < 6;
  u32x4 PR[8], xa, xb, xq;
  unsigned epoch = 0, tk = 0;
  int ra = 0, RA0 = 0;
  bool va = false;
  if constexpr (early) {
    int s0_ = 240, s1_ = 80, s2_ = 240 * (sp & 1), s3_ = 80 * sp, s4_ = 64 * (sp >> 1), s5_ = 64 * w, s6_ = 24 * w;
    asm("" : "+s"(s0_), "+s"(s1_), "+s"(s2_), "+s"(s3_), "+s"(s4_), "+s"(s5_), "+s"(s6_));
    const int rw0 = isF ? s5_ : s6_;
    int lim = (isF ? s0_ : s1_) - rw0, lim2 = isF ? 64 : 24;
    asm("" : "+s"(lim), "+s"(lim2));
    lim = lim < lim2 ? lim : lim2;
    ra = rw0 + 8 * g;
    va = 8 * g < lim;
    RA0 = isF ? s2_ : s3_;
    int aoff = (int)__umul24(g, 8 * HQ * DH * 2) + 16 * c;
    PIN(aoff);
    aoff = va ? aoff : NOWHERE;
    int sa = ((RA0 + rw0) * (HQ * DH) + h * DH + (isF ? s4_ : 64)) * 2;
    PIN(aoff);
    asm volatile("" : "+s"(sa));
    SCHED_FENCE();
    const __amdgpu_buffer_rsrc_t rw = rsrc(wq, NOWHERE);
    PR[0] = __builtin_amdgcn_raw_buffer_load_b128(rw, aoff, sa, 2);
    SCHED_FENCE();
    tk = NOWT();
    SCHED_FENCE();
    asm volatile("s_load_dword %0, %1, %2" : "=&s"(epoch) : "s"(gw), "s"(E1P && w == 1 ? (int)GL * 8 + (sp * 8 + (b & 7)) * LSP : (int)GEP * 8) : "memory");
    SCHED_FENCE();
#pragma unroll
    for (int i = 1; i < 8; ++i) PR[i] = __builtin_amdgcn_raw_buffer_load_b128(rw, aoff, sa + i * (HQ * DH * 2), 2);
    SCHED_FENCE();
  }
  const unsigned short* xs = reinterpret_cast<const unsigned short*>(x);
  const size_t wgb = ((size_t)h * SK + (SK - WIN)) * DH;
  const __amdgpu_buffer_rsrc_t rk = rsrc(kc + wgb, WIN * DH * 2);
  const __amdgpu_buffer_rsrc_t rv = rsrc(vc + wgb, WIN * DH * 2);
  const int voff = (g * DH + 8 * c) * 2;
  auto tile = [&](int t) { return (t * 64 + h * HROT + w * 8 + sp) & 511; };
  u32x4 K[8][3], V[8][3];
  auto kld = [&](int t, int j) { K[t][j] = __builtin_amdgcn_raw_buffer_load_b128(rk, voff + 128 * j, tile(t) * 3072, 2); };
  auto vld = [&](int t, int j) { V[t][j] = __builtin_amdgcn_raw_buffer_load_b128(rv, voff + 128 * j, tile(t) * 3072, 2); };
  unsigned tag = 0;
  const unsigned clk = lds_addr(lds + L_CK);
  const int ph = (w * PV_) >> 3;
  const int r4 = lane >> 4, c16 = lane & 15;
  const int ch = c16 < 10 ? c16 : 9;
  const int fi = (h & 3) * 6 + (isF ? sp : 5), gl = fi >> 3, gI = (h >> 2) * 3 + gl, fr = fi & 7;
  u32x4 WO[6], X[6], W2[4];
  unsigned tch = 0;
  const int wooff = ((48 * w + r4 + (isF ? 24 * (g & 1) : 24)) * D + 64 * sp + 8 * c + (isF ? 0 : 128 * (g & 1))) * 2;
  typedef unsigned u32x2 __attribute__((ext_vector_type(2)));
  const __amdgpu_buffer_rsrc_t rl = rsrc(gw + GL, 16 * 64 * LSP);
  const bool pub = !((sp + (b >> 6)) & 3);
  const int lw_ = lane == 0 && pub ? (sp * 8 + (b & 7)) * LSP : NOWHERE;
  u32x2 lat = {0u, 0u};
  u64 own = 0, pre = 0;
  unsigned pro = 0, st1 = 0, old = 0;
  bool vslow = false, kslow = false;
  if constexpr (early) {
    u32x4 PF[5];
    const int F0 = (isF ? 160 * (fr >> 1) : 480) + 40 * w;
    const int fs = isF ? FF : HQ * DH;
    int s6_ = 64 * gI, s7_ = h * DH + 128 * (sp & 1);
    asm("" : "+s"(s6_), "+s"(s7_));
    const int boff = ((int)__umul24(g, fs) + 8 * c) * 2;
    const int sb = (F0 * fs + (isF ? s6_ : s7_)) * 2;
    const bf16* pf_ = (fr & 1) ? w3 : w1;
    asm("" : "+s"(pf_));
    const __amdgpu_buffer_rsrc_t rf = rsrc(isF ? pf_ : wq, NOWHERE);
    unsigned short xr[5];
#pragma unroll
    for (int i = 0; i < 5; ++i) PF[i] = __builtin_amdgcn_raw_buffer_load_b128(rf, boff, sb + i * 16 * fs, 2);
    SCHED_FENCE();
    if (!isF) {
#pragma unroll
      for (int i = 0; i < 6; ++i)
        X[i] = __builtin_amdgcn_raw_buffer_load_b128(rsrc(wo + (size_t)h * DH * D, DH * D * 2), wooff - 24 * D * 2 + i * (4 * D * 2), 0, 2);
    } else {
#pragma unroll
      for (int i = 0; i < 6; ++i) X[i] = u32x4{0u, 0u, 0u, 0u};
    }
    SCHED_FENCE();
    xa = *reinterpret_cast<const u32x4*>(x + 8 * lane);
    xb = *reinterpret_cast<const u32x4*>(x + 512 + 8 * (lane & 15));
    xq = *reinterpret_cast<const u32x4*>(x + RA0 + (va ? ra : 0));
#pragma unroll
    for (int i = 0; i < 5; ++i) xr[i] = xs[F0 + 8 * i + g];
    SCHED_FENCE();
    const unsigned epv = __hip_atomic_load(reinterpret_cast<unsigned*>(gw + GEP), __ATOMIC_RELAXED, __HIP_MEMORY_SCOPE_AGENT);
    SCHED_FENCE();
    if (tid < 17) reinterpret_cast<unsigned*>(lds)[L_CK + tid] = tk;
    asm volatile("s_waitcnt lgkmcnt(0)\n\ts_barrier" : "+s"(epoch)::"memory");
    STAMP(6);
    SCHED_FENCE();
#pragma unroll
    for (int t = 0; t < 3; ++t)
#pragma unroll
      for (int j = 0; j < 3; ++j) kld(t, j);
    SCHED_FENCE();
    float mb, den;
    {
      float xf[8], xg[8];
#pragma unroll
      for (int i = 0; i < 4; ++i) {
        xf[2 * i] = bf_lo(xa[i]);
        xf[2 * i + 1] = bf_hi(xa[i]);
        xg[2 * i] = bf_lo(xb[i]);
        xg[2 * i + 1] = bf_hi(xb[i]);
      }
      const bool lo16 = lane < 16;
      float sum = 0.f;
#pragma unroll
      for (int i = 0; i < 8; ++i) sum += xf[i] + (lo16 ? xg[i] : 0.f);
      sum = wave_sum(sum);
      const float mean = sum * (1.0f / D);
      float sq = 0.f;
#pragma unroll
      for (int i = 0; i < 8; ++i) {
        const float d1 = xf[i] - mean, d2 = xg[i] - mean;
        sq += d1 * d1 + (lo16 ? d2 * d2 : 0.f);
      }
      sq = wave_sum(sq);
      mb = bfr(mean);
      const float vb = bfr(sq * (1.0f / D));
      den = bfr(sqrtf(bfr(vb + 1e-5f)));
    }
    {
      unsigned xn[13];
#pragma unroll
      for (int i = 0; i < 4; ++i) {
        xn[2 * i] = fbits(bfr(bfr(bf_lo(xq[i]) - mb) / den));
        xn[2 * i + 1] = fbits(bfr(bfr(bf_hi(xq[i]) - mb) / den));
      }
#pragma unroll
      for (int i = 0; i < 5; ++i) xn[8 + i] = fbits(bfr(bfr(bf_lo(xr[i]) - mb) / den));
#pragma unroll
      for (int i = 0; i < 13; ++i) PIN(xn[i]);
      SCHED_FENCE();
      pace<t_round(2), 2>(clk, ph);
#pragma unroll
      for (int j = 0; j < 3; ++j) kld(3, j);
      TT(2);
      SCHED_FENCE();
      float fa[8], fg[8];
#pragma unroll
      for (int k = 0; k < 8; ++k) fa[k] = fg[k] = 0.f;
#pragma unroll
      for (int i = 0; i < 8; ++i)
#pragma unroll
        for (int k = 0; k < 4; ++k) {
          fa[2 * k] = dot2(PR[i][k], xn[i] >> 16, fa[2 * k]);
          fa[2 * k + 1] = dot2(PR[i][k], xn[i], fa[2 * k + 1]);
        }
      lds[L_QP + w * 128 + 8 * c + g] = xg_reduce8(fa, g & 1);
#pragma unroll
      for (int i = 0; i < 5; ++i)
#pragma unroll
        for (int k = 0; k < 4; ++k) {
          fg[2 * k] = dot2(PF[i][k], xn[8 + i] >> 16, fg[2 * k]);
          fg[2 * k + 1] = dot2(PF[i][k], xn[8 + i], fg[2 * k + 1]);
        }
      lds[L_QP + w * 128 + 64 + 8 * c + g] = xg_reduce8(fg, g & 1);
    }
    SCHED_FENCE();
    tag = __builtin_amdgcn_readfirstlane(epv) + 1u;
    if (tid == 0) reinterpret_cast<unsigned*>(lds)[L_SY + 3] = tag;
    LDS_DRAIN();
    if (lane == 0) old = lds_add(lds, L_SY);
    old = __builtin_amdgcn_readfirstlane(old);
    if (old == 3) {
      const float2* qp = reinterpret_cast<const float2*>(lds + L_QP) + lane;
      const float2 p0_ = qp[0], p1_ = qp[64], p2_ = qp[128], p3_ = qp[192];
      gst16(gw + GQ + (size_t)rec * QS + 2 * lane, fbits((p0_.x + p1_.x) + (p2_.x + p3_.x)),
            fbits((p0_.y + p1_.y) + (p2_.y + p3_.y)), tag);
    }
    STAMP(11);
  } else {
    if (tid >= NT - 8) reinterpret_cast<unsigned*>(lds)[L_SY + tid - (NT - 8)] = 0u;
    unsigned st0 = 0;
    {
      const bf16* tp = w == 4 ? kc + wgb : w == 5 ? (!isF ? wo + (size_t)h * DH * D : (fr & 1) ? w3 : w1) : w == 6 ? ((sp & 1) ? x : wq) : vc + wgb;
      asm volatile("s_waitcnt lgkmcnt(0)\n\ts_load_dword %0, %1, 0x0" : "=&s"(st0) : "s"(tp) : "memory");
    }
    SCHED_FENCE();
#pragma unroll
    for (int j = 0; j < 3; ++j) kld(0, j);
    SCHED_FENCE();
    asm volatile("s_barrier" ::: "memory");
    STAMP(6);
    SCHED_FENCE();
    if (w < 6) {
      const int l = lane < 42 ? lane : 41, k = l & 15;
      const int yg = 4 * (k < 8 || isF ? sp : sp + 2) + ((k >> 1) & 3);
      const size_t yo = l < 32 || !isF ? GY + (size_t)yg * YB + 32 * h + 16 * (h >> 4) + 8 * (k & 1)
                                       : GZ + (size_t)(fr * 5 + (k >> 1)) * YB + 32 * gI + 16 * (gI >> 4) + 8 * (k & 1);
      unsigned o4 = (unsigned)(l < 16 ? G2 + (size_t)rec * RS + 8 * l : yo);
      unsigned o5 = (unsigned)(GQ + (size_t)rec * QS + 8 * (lane < 16 ? lane : 15));
      if (GLP && pub && lane >= 16 && lane < 32 && ((PM >> (lane - 16)) & 1))
        o5 = (unsigned)GL + (unsigned)(((lane - 16) * 64 + sp * 8 + (b & 7)) * (LSP / 8));
      PIN(o4);
      PIN(o5);
      pre = gld(gw + (w == 4 ? o4 : o5));
    }
    if (w == 7 && h < 5)
      pro = __hip_atomic_load(reinterpret_cast<const unsigned*>(out + 80 * sp + 16 * h) + (lane & 7), __ATOMIC_RELAXED,
                              __HIP_MEMORY_SCOPE_AGENT);
    SCHED_FENCE();
    asm volatile("s_waitcnt lgkmcnt(0)" ::"s"(st0) : "memory");
#pragma unroll
    for (int j = 0; j < 3; ++j) {
      kld(1, j);
      THRN(5);
    }
    THRN(3);
    TT(1);
    own = __builtin_amdgcn_s_memrealtime() - SB_;
    __builtin_amdgcn_raw_buffer_store_b64(u32x2{(unsigned)own, (unsigned)(own >> 32)}, rl, w == 6 ? lw_ : NOWHERE, 0, SC1);
    SCHED_FENCE();
    if (LOC && w == 6 && lane < 16) {
      const unsigned o = (unsigned)own - LOC;
      if ((int)(o - reinterpret_cast<unsigned*>(lds)[L_CK + lane]) > 0) reinterpret_cast<unsigned*>(lds)[L_CK + lane] = o;
    }
    SCHED_FENCE();
    {
      const bf16* tp = (w & 1) ? wo + (size_t)h * DH * D : w2 + (size_t)gI * 64 * D;
      asm volatile("s_load_dword %0, %1, 0x0" : "=&s"(st1) : "s"(tp) : "memory");
    }
    SCHED_FENCE();
  }
  u32x4 qf[3];
  float sc[8], mw, lw, acc[24];
  unsigned pb[8];
  auto score = [&](int t) {
    float d = 0.f;
#pragma unroll
    for (int j = 0; j < 3; ++j)
#pragma unroll
      for (int i = 0; i < 4; ++i) d = dot2(K[t][j][i], qf[j][i], d);
    d = grp8_sum(d);
    sc[t] = bfr(bfr(d) * SCALE);
  };
  auto pv = [&](int t) {
    const unsigned plo = pb[t], phi = plo << 16;
#pragma unroll
    for (int j = 0; j < 3; ++j)
#pragma unroll
      for (int i = 0; i < 4; ++i) {
        acc[8 * j + 2 * i] = dot2(V[t][j][i], plo, t ? acc[8 * j + 2 * i] : 0.f);
        acc[8 * j + 2 * i + 1] = dot2(V[t][j][i], phi, t ? acc[8 * j + 2 * i + 1] : 0.f);
        PIN(acc[8 * j + 2 * i]);
        PIN(acc[8 * j + 2 * i + 1]);
      }
  };
  const bool hf = lane >> 5;
  const __amdgpu_buffer_rsrc_t rq_ = rsrc(gw + GQ, NWG * QS * 8);
  int go[9];
  {
    const int l16 = (lane & 31) * 16;
    const int qa = l16 + (8 * h + (hf ? 4 : 0)) * (QS * 8);
    go[0] = qa;
    go[1] = qa + QS * 8;
    go[2] = l16 + (8 * h + (hf ? 7 : 6)) * (QS * 8) + 512;
    go[3] = qa + 2 * QS * 8;
    go[4] = qa + 3 * QS * 8;
#pragma unroll
    for (int k = 0; k < 4; ++k) {
      const int m = 8 * gl + 2 * k + (hf ? 1 : 0);
      go[5 + k] = isF ? l16 + (32 * (h >> 2) + m + (((m * 43) >> 7) & 6)) * (QS * 8) + 512 : NOWHERE;
    }
  }
  const bool gath = old == GRW;
  u32x4 pa[9];
  constexpr int C0 = early ? CE : CL;
  static_assert(C0 + 3 < 15 && C0 > GR + 1 && C0 >= 6, "schedule");
  sfor<early ? 3 : 2, 16>([&](auto R) {
    constexpr int r = R.v, t = r >= 8 ? r - 8 : r + early;
    unsigned tl = 0;
    if constexpr (!early && MEAS && ((PM >> r) & 1)) tl = pace_m<t_round(r), r>(clk, ph); else pace<t_round(r), r>(clk, ph);
    if constexpr (!early && ((PM >> (r - 1)) & 1)) {
      lat = __builtin_amdgcn_raw_buffer_load_b64(rl, w == MW ? lane * LSP : NOWHERE, 64 * LSP * (r - 1), SC1);
      SCHED_FENCE();
    }
    if constexpr (r < 8 && t == 8) {
      const __amdgpu_buffer_rsrc_t rx = rsrc(w2 + ((size_t)gI * 64 + 16 * w) * D + 80 * fr, isF ? 16 * D * 2 : 0);
#pragma unroll
      for (int i = 0; i < 4; ++i)
        W2[i] = __builtin_amdgcn_raw_buffer_load_b128(rx, (r4 * D + 8 * ch) * 2 + i * (4 * D * 2), 0, 2);
      tch = __builtin_amdgcn_raw_buffer_load_b32(rsrc(wo + (size_t)h * DH * D, DH * D * 2), tid ? NOWHERE : wooff, 0, 2);
    } else {
#pragma unroll
      for (int j = 0; j < 3; ++j) {
        if (r >= 8) vld(t, j); else kld(t, j);
        if (r == 15 && j == 2) break;
        if (early && r == GR + 1) continue;
        THRN(early && r == 8 ? NFL + 2 : NFL);
      }
    }
    TT(r);
    SCHED_FENCE();
    if constexpr (early) {
      if (r == GR && gath) {
#pragma unroll
        for (int s = 0; s < 9; ++s) pa[s] = __builtin_amdgcn_raw_buffer_load_b128(rq_, go[s], 0, SC1);
        SCHED_FENCE();
      }
      if (r == GR + 1) {
        if (gath) {
          const unsigned tf = isF ? tag : 0u;
          bool ok = true;
#pragma unroll
          for (int s = 0; s < 9; ++s) ok = ok & (pa[s][1] == (s < 5 ? tag : tf)) & (pa[s][3] == (s < 5 ? tag : tf));
          if (!__all(ok)) {
            for (unsigned spin = 0;; ++spin) {
              ok = true;
              asm volatile("" ::: "memory");
#pragma unroll
              for (int s = 0; s < 9; ++s) {
                pa[s] = __builtin_amdgcn_raw_buffer_load_b128(rq_, go[s], 0, SC1);
                ok = ok & (pa[s][1] == (s < 5 ? tag : tf)) & (pa[s][3] == (s < 5 ? tag : tf));
              }
              if (__all(ok) || spin >= SPIN_MAX) break;
              __builtin_amdgcn_s_sleep(1);
            }
          }
          float qo[2], qm[2], hu[2];
#pragma unroll
          for (int k = 0; k < 2; ++k) {
            auto val = [&](int s) { return __builtin_bit_cast(float, (unsigned)pa[s][2 * k]); };
            qo[k] = (val(0) + val(1)) + val(2);
            const float t_ = val(3) + val(4);
            qm[k] = sw32(t_, t_);
            const float u = bfr((val(5) + val(6)) + (val(7) + val(8)));
            auto sw = __builtin_amdgcn_permlane32_swap(fbits(u), fbits(u), false, false);
            hu[k] = bfr(bfr(silu(__builtin_bit_cast(float, (unsigned)sw[0]))) * __builtin_bit_cast(float, (unsigned)sw[1]));
          }
          reinterpret_cast<unsigned*>(lds + L_QS)[lane + (hf ? 32 : 0)] = bfpack(qo[0], qo[1]);
          if (hf) {
            reinterpret_cast<unsigned*>(lds + L_QS)[lane] = bfpack(qm[0], qm[1]);
          } else {
            *reinterpret_cast<float2*>(lds + L_H + 2 * lane) = float2{hu[0], hu[1]};
          }
          LDS_DRAIN();
          if (lane == 0) lds_add(lds, L_SY + 2);
        }
        STAMP(2);
      }
    } else {
      if (r == 2) asm volatile("" ::"s"(st1), "v"(cpf), "v"(pre), "v"(pro));
      if (r == AR) lat = __builtin_amdgcn_raw_buffer_load_b64(rl, w == 6 ? lane * LSP : NOWHERE, 0, SC1);
      if (r == AR + 1) {
        const u64 d = (((u64)lat[1] << 32) | lat[0]) - own + 200u;
        const bool ok = d <= 400u;
        const float s_ = wave_sum(ok ? (float)(unsigned)d : 0.f), n = (float)__builtin_popcountll(__ballot(ok));
        const unsigned o = (unsigned)own + (n > 0.f ? (unsigned)(s_ * __builtin_amdgcn_rcpf(n)) - 200u : 0u);
        if (w == 6 && lane < 16) reinterpret_cast<unsigned*>(lds)[L_CK + lane] = o;
        if (w == 6 && lane == 0) reinterpret_cast<unsigned*>(lds)[L_TE + 4] = o;
      }
      if ((PM >> r) & 1) {
        const unsigned org = __builtin_amdgcn_readfirstlane(reinterpret_cast<const unsigned*>(lds)[L_CK + r]);
        tag = __builtin_amdgcn_readfirstlane(reinterpret_cast<const unsigned*>(lds)[L_SY + 3]);
        THRN(3);
        const unsigned now = NOWT() - 9;
        const int l = (int)((tl ? tl : now) - org) - (t_round(r) + ph);
        __builtin_amdgcn_raw_buffer_store_b64(u32x2{(unsigned)(l < -64 ? 0 : l + 64), tag}, rl, w == MW && l < ULIM ? lw_ : NOWHERE,
                                              64 * LSP * r, SC1);
      }
      SCHED_FENCE();
    }
    if (r == C0) {
      lds_wait(lds, L_SY + 2, 1u);
      STAMP(3);
#pragma unroll
      for (int j = 0; j < 3; ++j) qf[j] = *reinterpret_cast<const u32x4*>(lds + L_QS + (c + 8 * j) * 4);
#pragma unroll
      for (int k = 0; k < 4; ++k) score(k);
    } else if (r == C0 + 1) {
#pragma unroll
      for (int k = 4; k < 7; ++k) score(k);
    } else if (r == C0 + 2) {
      score(7);
      mw = sc[0];
#pragma unroll
      for (int k = 1; k < 8; ++k) mw = fmaxf(mw, sc[k]);
      mw = xg_max(mw);
      lw = 0.f;
#pragma unroll
      for (int k = 0; k < 8; ++k) {
        const float p = ex2((sc[k] - mw) * LOG2E);
        lw += p;
        pb[k] = bfbits(p);
      }
      lw = xg_sum(lw);
      if (lane == 0) {
        lds[L_ML + 2 * w] = mw;
        lds[L_ML + 2 * w + 1] = lw;
      }
      if (early) {
        float z[8];
#pragma unroll
        for (int n = 0; n < 8; ++n) z[n] = 0.f;
#pragma unroll
        for (int r = 0; r < 4; ++r) {
          const unsigned chi = fbits(lds[L_H + 16 * w + 4 * r + r4]), clo = chi >> 16;
#pragma unroll
          for (int n = 0; n < 4; ++n) {
            z[2 * n] = dot2(W2[r][n], clo, z[2 * n]);
            z[2 * n + 1] = dot2(W2[r][n], chi, z[2 * n + 1]);
          }
        }
        const float2 zr = rows_reduce(z);
        if (c16 < 10) *reinterpret_cast<float2*>(lds + L_Z + 80 * w + 8 * c16 + 2 * r4) = zr;
      }
      LDS_DRAIN();
      if (lane == 0) lds_add(lds, L_SY + 4);
      STAMP(4);
    } else if (r == C0 + 3) {
#pragma unroll
      for (int k = 0; k < t + 1 - PVL; ++k) pv(k);
    } else if (r > C0 + 3 && r < 15) {
      pv(t - PVL);
    }
    if constexpr (!early && ((PM >> (r - 1)) & 1)) {
      SCHED_FENCE();
      constexpr int p = r - 1;
      const float m = wave_sum((float)(lat[1] == tag && lat[0] < UMISS + 64 ? lat[0] : UMISS + 64)) * (1.f / 64) - 64.f;
      int d = 0;
      if (p <= 8) {
        kslow = m >= ATHR;
        if (kslow) d = (int)(m * (ASH / 4.f)) + AGX * (lane <= p ? 0 : lane < 8 ? lane - p : 8 - p);
      } else if (!vslow && m >= (p <= 10 ? (kslow ? UTA : UTHR) : UTH2)) {
        vslow = true;
        d = (int)(fmaxf(m, 0.f) * ((p <= 10 ? USH : USH2) / 4.f)) + (lane > p ? UGX * (lane - p) : 0);
      } else if (vslow && m < CT) {
        d = lane > p + 1 ? -UGX * (lane - p - 1) : 0;
      }
      if (w == MW && lane < 16) reinterpret_cast<unsigned*>(lds)[L_CK + lane] += d;
      if (w == MW && lane == 0) reinterpret_cast<int*>(lds)[L_TE + (p <= 8 ? 1 : p <= 10 ? 2 : 3)] = (int)m;
    }
  });
  asm volatile("" ::"v"(tch));
  if (PRI && !early) __builtin_amdgcn_s_setprio(3);
  SCHED_FENCE();
  const __amdgpu_buffer_rsrc_t rwo = rsrc(wo + (size_t)h * DH * D, DH * D * 2);
  lds_wait(lds, L_SY + 4, 8u);
  float f, M, L;
  wg_ml(lds, M, L);
  f = ex2((mw - M) * LOG2E) / L;
  const unsigned cw = fbits(fmaf(M, LOG2E, __builtin_amdgcn_logf(L)));
  if (!early) tag = __builtin_amdgcn_readfirstlane(reinterpret_cast<const unsigned*>(lds)[L_SY + 3]);
  PIN(f);
  SCHED_FENCE();
  const int l40 = lane < 40 ? lane : 39;
  u64* rdst = gw + G2 + (size_t)rec * RS + 32 * r4 + 2 * c16;
  u64* zdst = gw + GZ + (size_t)(fr * 5 + (l40 >> 3)) * YB + 32 * gI + 16 * (gI >> 4) + 2 * (l40 & 7);
  unsigned pdst = lds_addr(lds + L_PART + w * DH + 8 * c + g), psrc = lds_addr(lds + L_PART + 48 * r4 + 4 * c16);
  unsigned cnt6 = lds_addr(lds + L_SY + 6), one = 1u;
  u32x4 rq = {cw, tag, cw, tag};
  u64 m48 = 0x0fff0fff0fff0fffull, m52 = 0x1fff1fff1fff1fffull;
  asm volatile("" : "+s"(m48), "+s"(m52), "+{v[156:159]}"(rq));
  PIN(rdst);
  PIN(zdst);
  PIN(pdst);
  PIN(psrc);
  PIN(cnt6);
  PIN(one);
#pragma unroll
  for (int k = 7 - PVL; k < 7; ++k) pv(k);
  auto wo1 = [&]() {
    if (WT_) pace<T_LAST + WT_, 15>(clk, ph);
    SCHED_FENCE();
#pragma unroll
    for (int i = 0; i < W1N; ++i) WO[i] = __builtin_amdgcn_raw_buffer_load_b128(rwo, wooff, i * (4 * D * 2), 2);
    SCHED_FENCE();
  };
  {
    unsigned plo = pb[7], phi = plo << 16;
    PIN(plo);
    PIN(phi);
    sfor<0, 3>([&](auto J) {
      constexpr int j = J.v;
      SCHED_FENCE();
      float a[8];
#pragma unroll
      for (int i = 0; i < 4; ++i) {
        a[2 * i] = dot2(V[7][j][i], plo, acc[8 * j + 2 * i]);
        a[2 * i + 1] = dot2(V[7][j][i], phi, acc[8 * j + 2 * i + 1]);
      }
      xg_store<256 * j>(a, f, pdst);
    });
    if (early) wo1();
  }
  STAMP(5);
  unsigned arr;
  asm volatile(
      "s_mov_b64 exec, 1\n\tds_add_rtn_u32 v112, %[cnt], %[one]\n\ts_mov_b64 exec, %[m48]\n\t" SUM8("%[src]")
      "v_readfirstlane_b32 %[arr], v112\n\t" SUM8B
      "s_cmp_eq_u32 %[arr], 7\n\t" SUM8C
      "v_cvt_pk_bf16_f32 v156, v116, v117\n\tv_cvt_pk_bf16_f32 v158, v118, v119\n\t"
      "s_cselect_b64 exec, %[m52], 0\n\tglobal_store_dwordx4 %[dst], v[156:159], off sc1\n\ts_mov_b64 exec, -1\n\ts_nop 0"
      : [arr] "=&s"(arr), "+{v[156:159]}"(rq)
      : [cnt] "v"(cnt6), [one] "v"(one), [m48] "s"(m48), [m52] "s"(m52), [src] "v"(psrc), [dst] "v"(rdst)
      : CLOB_T, "scc", "memory");
  if (arr == 0 && isF) {
    const float2* zp = reinterpret_cast<const float2*>(lds + L_Z) + l40;
    const float2 z0 = zp[0], z1 = zp[40], z2 = zp[80], z3 = zp[120];
    gst16(zdst, fbits((z0.x + z1.x) + (z2.x + z3.x)), fbits((z0.y + z1.y) + (z2.y + z3.y)), tag);
  }
  if constexpr (!early) {
    if (PRI) __builtin_amdgcn_s_setprio(0);
    if (w == 7 && h < 5) {
      const int col = 80 * sp + 16 * h + 2 * c + (lane >> 5);
      const float xv = bf_lo(xs[col]);
      const u64* py = gw + GZ + (size_t)(sp * 5 + h) * YB + 32 * g + 2 * c + 392;
      bf16* po = out + col;
      float gt = 0.f;
      u64 m3 = 0ull, ms = 0ull;
      PIN(po);
#pragma unroll 1
      for (int pass = 0; pass < 2; ++pass) {
        lds_wait(lds, L_SY + (pass ? 1 : 6), pass ? 4u : 8u);
        STAMP(8);
        asm volatile("" : "+s"(m3), "+s"(ms));
        float t;
        unsigned n;
        asm volatile(
            "v_mov_b32 v124, 0\n\tv_mov_b32 v125, %[tag]\n\tv_mov_b32 v126, 0\n\tv_mov_b32 v127, %[tag]\n\t"
            "s_mov_b64 vcc, -1\n\ts_mov_b32 %[n], 0\n"
            ".Lfp%=:\n\t"
            "s_add_u32 %[n], %[n], 1\n\ts_cmp_gt_u32 %[n], %[max]\n\ts_cbranch_scc1 .Lfd%=\n\t"
            "s_mov_b64 exec, vcc\n\t"
            "global_load_dwordx4 v[112:115], %[p], off offset:-3136 sc1\n\t"
            "global_load_dwordx4 v[116:119], %[p], off offset:-1088 sc1\n\t"
            "global_load_dwordx4 v[120:123], %[p], off offset:1088 sc1\n\t"
            "s_and_b64 exec, vcc, %[m3]\n\t"
            "global_load_dwordx4 v[124:127], %[p], off offset:3136 sc1\n\t"
            "s_mov_b64 exec, -1\n\ts_waitcnt vmcnt(0)\n\t"
            "v_bitop3_b32 v128, v113, v115, %[tag] bitop3:0x7e\n\tv_bitop3_b32 v129, v117, v119, %[tag] bitop3:0x7e\n\t"
            "v_bitop3_b32 v130, v121, v123, %[tag] bitop3:0x7e\n\tv_bitop3_b32 v131, v125, v127, %[tag] bitop3:0x7e\n\t"
            "v_or3_b32 v128, v128, v129, v130\n\tv_add_f32 v132, v112, v116\n\tv_or_b32 v128, v128, v131\n\t"
            "v_add_f32 v134, v120, v124\n\tv_cmp_ne_u32 vcc, 0, v128\n\tv_add_f32 v133, v114, v118\n\t"
            "s_cbranch_vccnz .Lfp%=\n"
            ".Lfd%=:\n\t"
            "v_add_f32 v135, v122, v126\n\tv_add_f32 v132, v132, v134\n\tv_add_f32 v133, v133, v135\n\ts_nop 1\n\t"
            "v_permlane32_swap_b32 v132, v133\n\tv_add_f32 v134, v132, v133\n\tv_add_f32 v135, v132, v133\n\ts_nop 1\n\t"
            "v_permlane16_swap_b32 v134, v135\n\tv_add_f32 v134, v134, v135\n\ts_nop 1\n\t"
            "v_add_f32_dpp v134, v134, v134 row_ror:8 row_mask:0xf bank_mask:0xf\n\t"
            "v_cvt_pk_bf16_f32 v134, v134, v134\n\tv_lshlrev_b32 %[t], 16, v134\n\t"
            "v_mul_f32 v134, %[t], %[gt]\n\tv_cvt_pk_bf16_f32 v134, v134, v134\n\tv_lshlrev_b32 v134, 16, v134\n\t"
            "v_add_f32 v134, %[xv], v134\n\tv_cvt_pk_bf16_f32 v134, v134, v134\n\t"
            "s_mov_b64 exec, %[ms]\n\tglobal_store_short %[po], v134, off sc1\n\ts_mov_b64 exec, -1"
            : [t] "=&v"(t), [n] "=&s"(n)
            : [tag] "s"(tag), [max] "n"(SPIN_MAX), [p] "v"(py), [m3] "s"(m3), [ms] "s"(ms), [gt] "v"(gt), [xv] "v"(xv), [po] "v"(po)
            : CLOB_T, "vcc", "scc", "memory");
        if (pass) break;
        gt = bfr(silu(t));
        py -= GZ - GY;
        m3 = ~0ull;
        ms = 0x000000ff000000ffull;
      }
      if (b == 0 && lane == 0)
        __hip_atomic_store(reinterpret_cast<unsigned*>(gw + GEP), tag, __ATOMIC_RELAXED, __HIP_MEMORY_SCOPE_AGENT);
      if (b == 0) {
        const volatile unsigned* lu = reinterpret_cast<const volatile unsigned*>(lds);
        const unsigned now = NOWT(), tk0 = lu[L_TE];
        int code = 0;
        if (TEL == 1) code = (int)(lu[L_TE + 4] - tk0) / 5 + 20;
        if (TEL == 2) code = (int)(lu[L_CK + 15] - lu[L_TE + 4]) / 5;
        if (TEL == 3) code = (int)(now - lu[L_CK + 15] - T_LAST - 300) / 5;
        if (TEL >= 4) code = (int)lu[L_TE + TEL - 3] + 20;
        code = code < 0 ? 0 : code > 200 ? 200 : code;
        while (NOWT() - tk0 < 10000u + 400u * code) __builtin_amdgcn_s_sleep(4);
      }
      STAMP(10);
    }
    STAMP_FLUSH();
    asm volatile(".Lcode_lastb:");
    return;
  }
  pace<T_LAST + W2T, 15>(clk, ph);
#pragma unroll
  for (int i = W1N; i < 6; ++i) WO[i] = __builtin_amdgcn_raw_buffer_load_b128(rwo, wooff, i * (4 * D * 2), 2);
  unsigned dm0 = __builtin_amdgcn_raw_buffer_load_b32(rwo, NOWHERE, 0, 2), dm1 = __builtin_amdgcn_raw_buffer_load_b32(rwo, NOWHERE, 4, 2);
  STAMP(3);
  SCHED_FENCE();
  const int ly = isF ? lane & 31 : lane;
  u64* ydst = gw + GY + (size_t)(4 * sp + (ly >> 3) + 4 * (ly >> 5)) * YB + 32 * h + 16 * (h >> 4) + 2 * (ly & 7);
  static_assert(RS * 8 * 4 == 4608, "record r4 + 4 is 4608 bytes behind record r4");
  const u64* pr = gw + G2 + (size_t)(h * NSP + r4 + 2) * RS + 32 * w + 2 * c16;
  const u64 ownm = 0xffffull << (16 * (sp & 3));
  u64 own0 = sp < 4 ? ownm : 0ull, own1 = sp < 4 ? 0ull : ownm, my = isF ? 0xffffffffull : ~0ull;
  u64 for0 = m52 & ~own0, for1 = m52 & ~own1;
  own0 &= m48;
  own1 &= m48;
  unsigned osrc = lds_addr(lds + L_PART + 48 * w + 4 * c16), ysrc = lds_addr(lds + L_Y + 2 * ly), cnt1 = lds_addr(lds + L_SY + 1);
  unsigned ydl = lds_addr(lds + L_Y + 128 * w + (isF ? 8 * c + g : 8 * c16 + 2 * r4));
  asm volatile("" : "+s"(own0), "+s"(own1), "+s"(for0), "+s"(for1), "+s"(my));
  PIN(ydst);
  PIN(pr);
  PIN(osrc);
  PIN(ysrc);
  PIN(cnt1);
  PIN(ydl);
  u32x8 WP[3], XP[3];
#pragma unroll
  for (int p = 0; p < 3; ++p)
#pragma unroll
    for (int k = 0; k < 4; ++k) {
      XP[p][2 * k] = __builtin_amdgcn_perm(X[2 * p + 1][k], X[2 * p][k], 0x05040100u);
      XP[p][2 * k + 1] = __builtin_amdgcn_perm(X[2 * p + 1][k], X[2 * p][k], 0x07060302u);
    }
  asm volatile("" : "+{v[64:71]}"(XP[0]), "+{v[72:79]}"(XP[1]), "+{v[80:87]}"(XP[2]));
  SCHED_FENCE();
  auto perm_wo = [&]() {
#pragma unroll
    for (int p = 0; p < 3; ++p)
#pragma unroll
      for (int k = 0; k < 4; ++k) {
        WP[p][2 * k] = __builtin_amdgcn_perm(WO[2 * p + 1][k], WO[2 * p][k], 0x05040100u);
        WP[p][2 * k + 1] = __builtin_amdgcn_perm(WO[2 * p + 1][k], WO[2 * p][k], 0x07060302u);
      }
    asm volatile("" : "+{v[88:95]}"(WP[0]), "+{v[96:103]}"(WP[1]), "+{v[104:111]}"(WP[2]));
    SCHED_FENCE();
    STAMP(1);
  };
  lds_wait(lds, L_SY + 6, 8u);
  if (PT_) pace<T_LAST + PT_, 15>(clk, 0);
  STAMP(4);
  if (PRI) __builtin_amdgcn_s_setprio(3);
  u32x4 ra0, ra1;
  asm volatile(
      "v_mov_b32 v148, %[cw]\n\tv_mov_b32 v149, %[tag]\n\tv_mov_b32 v151, %[tag]\n\t"
      "v_mov_b32 v152, %[cw]\n\tv_mov_b32 v153, %[tag]\n\tv_mov_b32 v155, %[tag]\n\t"
      "s_mov_b64 exec, %[for0]\n\tglobal_load_dwordx4 v[148:151], %[p], off offset:-2304 sc1\n\t"
      "s_mov_b64 exec, %[for1]\n\tglobal_load_dwordx4 v[152:155], %[p], off offset:2304 sc1\n\t"
      "s_mov_b64 exec, %[m48]\n\t" SUM8("%[src]") SUM8B SUM8C
      "s_mov_b64 exec, %[own0]\n\tv_cvt_pk_bf16_f32 v148, v116, v117\n\tv_cvt_pk_bf16_f32 v150, v118, v119\n\t"
      "s_mov_b64 exec, %[own1]\n\tv_cvt_pk_bf16_f32 v152, v116, v117\n\tv_cvt_pk_bf16_f32 v154, v118, v119\n\t"
      "s_mov_b64 exec, -1"
      : "=&{v[148:151]}"(ra0), "=&{v[152:155]}"(ra1)
      : [cw] "v"(cw), [tag] "s"(tag), [for0] "s"(for0), [for1] "s"(for1), [p] "v"(pr), [own0] "s"(own0), [own1] "s"(own1),
        [m48] "s"(m48), [src] "v"(osrc)
      : CLOB_T, "memory");
  SCHED_FENCE();
  perm_wo();
  unsigned np;
  asm volatile(
      "s_mov_b32 %[n], 0\n\t"
      "s_mov_b64 exec, -1\n\tv_mov_b64 v[160:161], 0\n\tv_mov_b64 v[162:163], 0\n\tv_mov_b64 v[164:165], 0\n\t"
      "v_mov_b64 v[166:167], 0\n\ts_branch .Lrc%=\n"
      ".Lrp%=:\n\t"
      "s_add_u32 %[n], %[n], 1\n\ts_cmp_gt_u32 %[n], %[max]\n\ts_cbranch_scc1 .Lrd%=\n\t"
      "v_cmp_ne_u32 vcc, 0, v112\n\tv_cmp_ne_u32 s[98:99], 0, v113\n\ts_sleep " STR(RSLEEP) "\n\t"
      "s_mov_b64 exec, vcc\n\tglobal_load_dwordx4 v[148:151], %[p], off offset:-2304 sc1\n\t"
      "s_mov_b64 exec, s[98:99]\n\tglobal_load_dwordx4 v[152:155], %[p], off offset:2304 sc1\n\t"
      "s_mov_b64 exec, -1\n"
      ".Lrc%=:\n\t"
      "s_waitcnt vmcnt(0)\n\t"
      "v_bitop3_b32 v112, v149, v151, %[tag] bitop3:0x7e\n\tv_bitop3_b32 v113, v153, v155, %[tag] bitop3:0x7e\n\t"
      "v_max_f32 v115, v148, v152\n\tv_or_b32 v114, v112, v113\n\tv_max_f32 v116, v148, v152\n\t"
      "v_cmp_ne_u32 vcc, 0, v114\n\tv_lshlrev_b32 v122, 16, v148\n\ts_cbranch_vccnz .Lrp%=\n"
      ".Lrd%=:\n\t"
      "v_permlane16_swap_b32 v115, v116\n\tv_max_f32 v117, v115, v116\n\tv_max_f32 v118, v115, v116\n\t"
      "v_and_b32 v123, 0xffff0000, v148\n\tv_lshlrev_b32 v124, 16, v150\n\t"
      "v_permlane32_swap_b32 v117, v118\n\tv_max_f32 v117, v117, v118\n\t"
      "v_sub_f32 v115, v148, v117\n\tv_sub_f32 v116, v152, v117\n\tv_exp_f32 v115, v115\n\tv_exp_f32 v116, v116\n\t"
      "v_and_b32 v125, 0xffff0000, v150\n\tv_lshlrev_b32 v126, 16, v152\n\t"
      "v_add_f32 v117, v115, v116\n\tv_add_f32 v118, v115, v116\n\t"
      "v_mul_f32_dpp v122, v115, v122" BC12 "v_mul_f32_dpp v123, v115, v123" BC12
      "v_permlane16_swap_b32 v117, v118\n\tv_add_f32 v119, v117, v118\n\tv_add_f32 v120, v117, v118\n\t"
      "v_mul_f32_dpp v124, v115, v124" BC12 "v_mul_f32_dpp v125, v115, v125" BC12
      "v_permlane32_swap_b32 v119, v120\n\tv_add_f32 v119, v119, v120\n\tv_lshlrev_b32 v128, 16, v154\n\t"
      "v_rcp_f32 v119, v119\n\tv_and_b32 v127, 0xffff0000, v152\n\tv_and_b32 v129, 0xffff0000, v154\n\t"
      "v_fmac_f32_dpp v122, v116, v126" BC12 "v_fmac_f32_dpp v124, v116, v128" BC12
      "v_fmac_f32_dpp v123, v116, v127" BC12 "v_fmac_f32_dpp v125, v116, v129" BC12
      "v_permlane32_swap_b32 v122, v124\n\tv_add_f32 v122, v122, v124\n\t"
      "v_permlane32_swap_b32 v123, v125\n\tv_add_f32 v123, v123, v125\n\t"
      "v_mov_b32_dpp v120, v119" BC12 "s_nop 0\n\t"
      "v_permlane16_swap_b32 v122, v123\n\tv_add_f32 v122, v122, v123\n\t"
      "v_mul_f32 v123, v122, v120\n\ts_cmp_lt_u32 %[sp], 6\n\t"
      "v_mul_f32_dpp v124, v122, v120 quad_perm:[1,0,3,2] row_mask:0xf bank_mask:0xf\n\t"
      "v_cvt_pk_bf16_f32 v124, v123, v124\n\t"
      "s_cbranch_scc1 .Lyf%=\n\ts_nop 0\n\t"
      DOT8(64, 65, 66, 67, 68, 69, 70, 71, 0) DOT8(72, 73, 74, 75, 76, 77, 78, 79, 2) DOT8(80, 81, 82, 83, 84, 85, 86, 87, 4)
      DOT8(88, 89, 90, 91, 92, 93, 94, 95, 6) DOT8(96, 97, 98, 99, 100, 101, 102, 103, 8)
      DOT8(104, 105, 106, 107, 108, 109, 110, 111, 10) YRED
      "v_pk_add_f32 v[160:161], v[160:161], v[162:163]\n\tds_write_b64 %[ydl], v[160:161]\n"
      ".Lys%=:\n\t"
      "s_mov_b64 exec, 1\n\tds_add_rtn_u32 v112, %[cnt], %[one]\n\ts_mov_b64 exec, -1\n\t"
      "ds_read2st64_b64 v[116:119], %[ysrc] offset0:0 offset1:1\n\tds_read2st64_b64 v[120:123], %[ysrc] offset0:2 offset1:3\n\t"
      "s_waitcnt lgkmcnt(2)\n\tv_readfirstlane_b32 s98, v112\n\ts_waitcnt lgkmcnt(1)\n\t"
      "v_pk_add_f32 v[116:117], v[116:117], v[118:119]\n\ts_cmp_eq_u32 s98, 3\n\ts_cselect_b64 exec, %[my], 0\n\t"
      "s_waitcnt lgkmcnt(0)\n\tv_pk_add_f32 v[120:121], v[120:121], v[122:123]\n\ts_nop 0\n\t"
      "v_add_f32 v156, v116, v120\n\tv_add_f32 v158, v117, v121\n\t"
      "global_store_dwordx4 %[dst], v[156:159], off sc1\n\ts_mov_b64 exec, -1\n\ts_branch .Lye%=\n"
      ".Lyf%=:\n\ts_nop 0\n\t"
      "v_mov_b32_dpp v125, v124 row_newbcast:0 row_mask:0xf bank_mask:0x3\n\t"
      "v_mov_b32_dpp v126, v124 row_newbcast:2 row_mask:0xf bank_mask:0x3\n\t"
      "v_mov_b32_dpp v127, v124 row_newbcast:4 row_mask:0xf bank_mask:0x3\n\t"
      "v_mov_b32_dpp v125, v124 row_newbcast:6 row_mask:0xf bank_mask:0xc\n\t"
      "v_mov_b32_dpp v126, v124 row_newbcast:8 row_mask:0xf bank_mask:0xc\n\t"
      "v_mov_b32_dpp v127, v124 row_newbcast:10 row_mask:0xf bank_mask:0xc\n\t"
      DOT8F(88, 89, 90, 91, 92, 93, 94, 95, 125) DOT8F(96, 97, 98, 99, 100, 101, 102, 103, 126)
      DOT8F(104, 105, 106, 107, 108, 109, 110, 111, 127) YRED
      "v_add_f32 v160, v160, v162\n\tv_add_f32 v161, v161, v163\n\ts_nop 0\n\t"
      "v_add_f32_dpp v160, v160, v160 row_ror:8 row_mask:0xf bank_mask:0x3\n\t"
      "v_add_f32_dpp v160, v161, v161 row_ror:8 row_mask:0xf bank_mask:0xc\n\t"
      "ds_write_b32 %[ydl], v160\n\ts_branch .Lys%=\n"
      ".Lye%=:"
      : [n] "=&s"(np), "+{v[156:159]}"(rq), "+{v[148:151]}"(ra0), "+{v[152:155]}"(ra1)
      : [tag] "s"(tag), [p] "v"(pr), [max] "n"(SPIN_MAX), [sp] "s"(sp), [ydl] "v"(ydl), [cnt] "v"(cnt1), [one] "v"(one),
        [ysrc] "v"(ysrc), [my] "s"(my), [dst] "v"(ydst), "{v[64:71]}"(XP[0]), "{v[72:79]}"(XP[1]), "{v[80:87]}"(XP[2]),
        "{v[88:95]}"(WP[0]), "{v[96:103]}"(WP[1]), "{v[104:111]}"(WP[2])
      : CLOB_T, "v160", "v161", "v162", "v163", "v164", "v165", "v166", "v167", "s98", "s99", "vcc", "scc", "memory");
  STAMP(9);
  asm volatile("" ::"v"(dm0), "v"(dm1));
  STAMP_FLUSH();
  asm volatile(".Lcode_last:");
}

__global__ void __launch_bounds__(NT, 2)
fused_decode(const bf16* __restrict__ wq, const bf16* __restrict__ w1, const bf16* __restrict__ w3, u64* __restrict__ gw,
             const bf16* __restrict__ x, const bf16* __restrict__ kc, const bf16* __restrict__ vc,
             const bf16* __restrict__ wo, const bf16* __restrict__ w2, bf16* __restrict__ out) {
  __shared__ __attribute__((aligned(16))) float lds[L_END];
  const int b = blockIdx.x;
  const int xcd = b & 7, jj = b >> 3;
  const int h = xcd * 4 + (jj >> 3), sp = jj & 7;
  const int w = __builtin_amdgcn_readfirstlane(threadIdx.x >> 6);
  unsigned cpf = threadIdx.x;
  {
    u64 pc;
    unsigned len;
    asm volatile(
        "s_cmp_lt_u32 %[w], 4\n\ts_cbranch_scc1 .Lnocpf\n\t"
        "s_getpc_b64 %[pc]\n.Lcode_here:\n\tv_xor_b32 %[c], %[hi], %[c]\n\tv_lshlrev_b32 %[c], 6, %[c]\n\t"
        "s_mov_b32 %[len], .Lcode_last-.Lcode_here-4\n\ts_max_i32 %[len], %[len], .Lcode_lastb-.Lcode_here-4\n\t"
        "v_min_u32 %[c], %[len], %[c]\n\tglobal_load_dword %[c], %[c], %[pc]\n.Lnocpf:"
        : [c] "+v"(cpf), [pc] "=&s"(pc), [len] "=&s"(len) : [w] "s"(w), [hi] "s"((b & 24) << 5) : "scc", "memory");
  }
  SCHED_FENCE();
  if (w >= 4)
    body<false>(lds, h, sp, w, x, kc, vc, wq, wo, w1, w3, w2, gw, out, cpf);
  else
    body<true>(lds, h, sp, w, x, kc, vc, wq, wo, w1, w3, w2, gw, out, cpf);
}

}

extern "C" void kernel_launch(void* const* d_in, const int* in_sizes, int n_in, void* d_out, int out_size, void* d_ws,
                              size_t ws_size, hipStream_t stream) {
  const bf16* x = (const bf16*)d_in[0];
  const bf16* kc = (const bf16*)d_in[1];
  const bf16* vc = (const bf16*)d_in[2];
  const bf16* wq = (const bf16*)d_in[3];
  const bf16* wo = (const bf16*)d_in[4];
  const bf16* w1 = (const bf16*)d_in[6];
  const bf16* w3 = (const bf16*)d_in[7];
  const bf16* w2 = (const bf16*)d_in[8];
  u64* gw = (u64*)d_ws;
  fused_decode<<<NWG, NT, 0, stream>>>(wq, w1, w3, gw, x, kc, vc, wo, w2, (bf16*)d_out);
}
```

```cpp
#define TEL 7
#include <hip/hip_runtime.h>
#include <hip/hip_bf16.h>
#include <stdint.h>

typedef __hip_bfloat16 bf16;
typedef unsigned long long u64;
typedef unsigned u32x4 __attribute__((ext_vector_type(4)));
typedef unsigned u32x8 __attribute__((ext_vector_type(8)));
typedef float f32x4 __attribute__((ext_vector_type(4)));
typedef float f32x2 __attribute__((ext_vector_type(2)));
typedef __bf16 bf16x2_t __attribute__((ext_vector_type(2)));

namespace {

constexpr int D = 640, HQ = 32, DH = 192, SK = 32768, WIN = 4096, FF = 1536;
constexpr int NSP = 8;
constexpr int NWG = HQ * NSP;
constexpr int NT = 512;
constexpr float SCALE = 0.07216878364870322f;
constexpr float LOG2E = 1.4426950408889634f;
constexpr unsigned SPIN_MAX = 1u << 16;
constexpr int SC1 = 16;
constexpr int NOWHERE = 1 << 30;

#define PV_ 81
#define PK_ 82
#define PX_ PK_
#define TS_ 358
#define SB_ 246
#define LOC 30
#define WT_ 98
#define W2T 132
#define W1N 3
#define PT_ 215
#define NFL 7
#define CE 7
#define CL 8
#define AR 3
#define GR 5
#define GRW 2
#define RSLEEP 1
#define MW 4
#define PM 0x1440
#define MEAS 1
#define ATHR 20
#define ASH 4
#define AGX 7
#define UTHR 8
#define UTH2 40
#define USH 4
#define UGX 9
#define ULIM 20
#define UMISS 60
#define PVL 1
#define PB0 0
#define LSP 16
#define GLP 1
#define E1P 1
#define UTA 3
#define PRI 0
#define USH2 3
#define CT 10
#define HROT 16
constexpr int t_round(int r) { return r <= 2 ? TS_ : t_round(r - 1) + (r > 8 ? PV_ : r == 8 ? PX_ : PK_); }
constexpr int T_LAST = t_round(15);
constexpr int t_late(int i) { return t_round(i); }
constexpr int t_early(int i) { return t_round(i); }

constexpr int QS = 144;
constexpr int YB = 2 * (16 * 32 + 16);
constexpr int RS = 144;
constexpr size_t GQ = 0;
constexpr size_t G2 = GQ + (size_t)NWG * QS;
constexpr size_t GY = G2 + (size_t)NWG * RS;
constexpr size_t GZ = GY + 16;
constexpr size_t GEP = GY + (size_t)NSP * 5 * YB;
constexpr size_t GL = GEP + 16;

__device__ __forceinline__ unsigned fbits(float x) { return __builtin_bit_cast(unsigned, x); }
__device__ __forceinline__ u64 gld(const u64* p) {
  return __hip_atomic_load(p, __ATOMIC_RELAXED, __HIP_MEMORY_SCOPE_AGENT);
}
__device__ __forceinline__ float bf_lo(unsigned u) { return __builtin_bit_cast(float, u << 16); }
__device__ __forceinline__ float bf_hi(unsigned u) { return __builtin_bit_cast(float, u & 0xffff0000u); }
__device__ __forceinline__ float bfr(float x) { return __bfloat162float(__float2bfloat16(x)); }
__device__ __forceinline__ unsigned short bfbits(float x) {
  return __builtin_bit_cast(unsigned short, __float2bfloat16(x));
}
__device__ __forceinline__ float dot2(unsigned a, unsigned b, float c) {
  return __builtin_amdgcn_fdot2_f32_bf16(__builtin_bit_cast(bf16x2_t, a), __builtin_bit_cast(bf16x2_t, b), c, false);
}
template <int CTRL>
__device__ __forceinline__ float dpp(float x) {
  return __builtin_bit_cast(float, __builtin_amdgcn_mov_dpp(__builtin_bit_cast(int, x), CTRL, 0xf, 0xf, true));
}
__device__ __forceinline__ float grp8_sum(float x) {
  x += dpp<0xB1>(x);
  x += dpp<0x4E>(x);
  x += dpp<0x141>(x);
  return x;
}
__device__ __forceinline__ float sw16(float a, float b) {
  auto s = __builtin_amdgcn_permlane16_swap(fbits(a), fbits(b), false, false);
  return __builtin_bit_cast(float, (unsigned)s[0]) + __builtin_bit_cast(float, (unsigned)s[1]);
}
__device__ __forceinline__ float sw32(float a, float b) {
  auto s = __builtin_amdgcn_permlane32_swap(fbits(a), fbits(b), false, false);
  return __builtin_bit_cast(float, (unsigned)s[0]) + __builtin_bit_cast(float, (unsigned)s[1]);
}
__device__ __forceinline__ float xg_sum(float x) {
  x += dpp<0x128>(x);
  x = sw16(x, x);
  return sw32(x, x);
}
__device__ __forceinline__ float xg_max(float x) {
  x = fmaxf(x, dpp<0x128>(x));
  auto s = __builtin_amdgcn_permlane16_swap(fbits(x), fbits(x), false, false);
  x = fmaxf(__builtin_bit_cast(float, (unsigned)s[0]), __builtin_bit_cast(float, (unsigned)s[1]));
  auto t = __builtin_amdgcn_permlane32_swap(fbits(x), fbits(x), false, false);
  return fmaxf(__builtin_bit_cast(float, (unsigned)t[0]), __builtin_bit_cast(float, (unsigned)t[1]));
}
__device__ __forceinline__ float wave_sum(float x) { return xg_sum(grp8_sum(x)); }
__device__ __forceinline__ float ex2(float x) { return __builtin_amdgcn_exp2f(x); }
__device__ __forceinline__ float silu(float x) { return x / (1.0f + __expf(-x)); }
__device__ __forceinline__ float xg_reduce8(const float (&a)[8], bool g0) {
  float r[4];
#pragma unroll
  for (int i = 0; i < 4; ++i) r[i] = sw32(a[i], a[i + 4]);
  const float s0 = sw16(r[0], r[2]), s1 = sw16(r[1], r[3]);
  const float keep = g0 ? s1 : s0, send = g0 ? s0 : s1;
  return keep + dpp<0x128>(send);
}
__device__ __forceinline__ __amdgpu_buffer_rsrc_t rsrc(const void* p, unsigned bytes) {
  return __builtin_amdgcn_make_buffer_rsrc(const_cast<void*>(p), 0, bytes, 0x00020000);
}
__device__ __forceinline__ void gst16(u64* p, unsigned v0, unsigned v1, unsigned tag) {
  const u32x4 o = {v0, tag, v1, tag};
  asm volatile("global_store_dwordx4 %0, %1, off sc1\n\ts_nop 1" ::"v"(p), "v"(o) : "memory");
}
__device__ __forceinline__ unsigned bfpack(float a, float b) {
  return __builtin_bit_cast(unsigned, __builtin_convertvector(f32x2{a, b}, bf16x2_t));
}
__device__ __forceinline__ float2 rows_reduce(const float (&v)[8]) {
  float r[4];
#pragma unroll
  for (int i = 0; i < 4; ++i) r[i] = sw32(v[i], v[i + 4]);
  return float2{sw16(r[0], r[2]), sw16(r[1], r[3])};
}
template <int OFF>
__device__ __forceinline__ void xg_store(const float (&v)[8], float f, unsigned a) {
  float p[4], q[4], t, u;
#pragma unroll
  for (int i = 0; i < 4; ++i) {
    auto s = __builtin_amdgcn_permlane32_swap(fbits(v[i]), fbits(v[i + 4]), false, false);
    p[i] = __builtin_bit_cast(float, (unsigned)s[0]);
    q[i] = __builtin_bit_cast(float, (unsigned)s[1]);
  }
  const f32x2 r01 = f32x2{p[0], p[1]} + f32x2{q[0], q[1]}, r23 = f32x2{p[2], p[3]} + f32x2{q[2], q[3]};
  auto s0 = __builtin_amdgcn_permlane16_swap(fbits(r01[0]), fbits(r23[0]), false, false);
  auto s1 = __builtin_amdgcn_permlane16_swap(fbits(r01[1]), fbits(r23[1]), false, false);
  asm volatile("v_add_f32 %0, %2, %3\n\tv_add_f32 %1, %4, %5\n\ts_nop 0\n\t"
               "v_add_f32_dpp %0, %0, %0 row_ror:8 row_mask:0xf bank_mask:0x3\n\t"
               "v_add_f32_dpp %0, %1, %1 row_ror:8 row_mask:0xf bank_mask:0xc\n\t"
               "v_mul_f32 %0, %6, %0\n\tds_write_b32 %7, %0 offset:%8"
               : "=&v"(t), "=&v"(u)
               : "v"((unsigned)s0[0]), "v"((unsigned)s0[1]), "v"((unsigned)s1[0]), "v"((unsigned)s1[1]), "v"(f), "v"(a), "n"(OFF)
               : "memory");
}
#define SUM8(a)                                                                                                          \
  "ds_read_b128 v[116:119], " a "\n\tds_read_b128 v[120:123], " a " offset:768\n\t"                                      \
  "ds_read_b128 v[124:127], " a " offset:1536\n\tds_read_b128 v[128:131], " a " offset:2304\n\t"                         \
  "ds_read_b128 v[132:135], " a " offset:3072\n\tds_read_b128 v[136:139], " a " offset:3840\n\t"                         \
  "ds_read_b128 v[140:143], " a " offset:4608\n\tds_read_b128 v[144:147], " a " offset:5376\n\ts_waitcnt lgkmcnt(6)\n\t"
#define SUM8B                                                                                                            \
  "v_pk_add_f32 v[116:117], v[116:117], v[120:121]\n\tv_pk_add_f32 v[118:119], v[118:119], v[122:123]\n\t"               \
  "s_waitcnt lgkmcnt(4)\n\t"                                                                                             \
  "v_pk_add_f32 v[124:125], v[124:125], v[128:129]\n\tv_pk_add_f32 v[126:127], v[126:127], v[130:131]\n\t"               \
  "v_pk_add_f32 v[116:117], v[116:117], v[124:125]\n\tv_pk_add_f32 v[118:119], v[118:119], v[126:127]\n\t"
#define SUM8C                                                                                                            \
  "s_waitcnt lgkmcnt(2)\n\t"                                                                                             \
  "v_pk_add_f32 v[132:133], v[132:133], v[136:137]\n\tv_pk_add_f32 v[134:135], v[134:135], v[138:139]\n\t"               \
  "s_waitcnt lgkmcnt(0)\n\t"                                                                                             \
  "v_pk_add_f32 v[140:141], v[140:141], v[144:145]\n\tv_pk_add_f32 v[142:143], v[142:143], v[146:147]\n\t"               \
  "v_pk_add_f32 v[132:133], v[132:133], v[140:141]\n\tv_pk_add_f32 v[134:135], v[134:135], v[142:143]\n\t"               \
  "v_pk_add_f32 v[116:117], v[116:117], v[132:133]\n\tv_pk_add_f32 v[118:119], v[118:119], v[134:135]\n\t"
#define BC12 " row_newbcast:12 row_mask:0xf bank_mask:0xf\n\t"
#define DOTB(y, a, n) "v_dot2c_f32_bf16_dpp v" #y ", v124, v" #a " row_newbcast:" #n " row_mask:0xf bank_mask:0xf\n\t"
#define DOT8(a, b, c, d, e, f, g, h, n) \
  DOTB(160, a, n) DOTB(161, b, n) DOTB(162, c, n) DOTB(163, d, n) DOTB(164, e, n) DOTB(165, f, n) DOTB(166, g, n) DOTB(167, h, n)
#define DOTF(y, a, n) "v_dot2c_f32_bf16 v" #y ", v" #n ", v" #a "\n\t"
#define DOT8F(a, b, c, d, e, f, g, h, n) \
  DOTF(160, a, n) DOTF(161, b, n) DOTF(162, c, n) DOTF(163, d, n) DOTF(164, e, n) DOTF(165, f, n) DOTF(166, g, n) DOTF(167, h, n)
#define YRED                                                                                                             \
  "v_permlane32_swap_b32 v160, v164\n\tv_permlane32_swap_b32 v161, v165\n\tv_permlane32_swap_b32 v162, v166\n\t"         \
  "v_permlane32_swap_b32 v163, v167\n\tv_pk_add_f32 v[160:161], v[160:161], v[164:165]\n\t"                              \
  "v_pk_add_f32 v[162:163], v[162:163], v[166:167]\n\ts_nop 1\n\t"                                                       \
  "v_permlane16_swap_b32 v160, v162\n\tv_permlane16_swap_b32 v161, v163\n\t"
#define CLOB_T                                                                                                     \
  "v112", "v113", "v114", "v115", "v116", "v117", "v118", "v119", "v120", "v121", "v122", "v123", "v124", "v125",        \
      "v126", "v127", "v128", "v129", "v130", "v131", "v132", "v133", "v134", "v135", "v136", "v137", "v138", "v139",    \
      "v140", "v141", "v142", "v143", "v144", "v145", "v146", "v147"
#define SCHED_FENCE() __builtin_amdgcn_sched_barrier(0)
#define STR2(x) #x
#define STR(x) STR2(x)
#define THRN(n) do { SCHED_FENCE(); asm volatile("s_waitcnt vmcnt(%0)" ::"n"(n) : "memory"); SCHED_FENCE(); } while (0)
#define NOWT() ((unsigned)__builtin_amdgcn_s_memrealtime())
template <int N>
struct IC {
  static constexpr int v = N;
};
template <int I, int E, class F>
__device__ __forceinline__ void sfor(F&& f) {
  if constexpr (I < E) {
    f(IC<I>{});
    sfor<I + 1, E>(f);
  }
}
#define LDS_DRAIN() asm volatile("s_waitcnt lgkmcnt(0)" ::: "memory")
__device__ __forceinline__ unsigned lds_add(float* lds, int idx) {
  return __hip_atomic_fetch_add(reinterpret_cast<unsigned*>(lds + idx), 1u, __ATOMIC_RELAXED, __HIP_MEMORY_SCOPE_WORKGROUP);
}
__device__ __forceinline__ void lds_wait(float* lds, int idx, unsigned n) {
  SCHED_FENCE();
#pragma unroll 1
  for (unsigned spin = 0; spin < (SPIN_MAX << 4); ++spin) {
    if (__hip_atomic_load(reinterpret_cast<unsigned*>(lds + idx), __ATOMIC_RELAXED, __HIP_MEMORY_SCOPE_WORKGROUP) >= n) break;
    __builtin_amdgcn_s_sleep(1);
  }
  asm volatile("" ::: "memory");
  SCHED_FENCE();
}
template <int T, int R>
__device__ __forceinline__ void pace(unsigned clk, int ph) {
  unsigned o;
  SCHED_FENCE();
  asm volatile(
      "s_branch .Lpb%=\n"
      ".Lpa%=:\n\ts_sleep 1\n"
      ".Lpb%=:\n\tds_read_b32 %0, %1 offset:%4\n\ts_memrealtime s[98:99]\n\ts_waitcnt lgkmcnt(0)\n\tv_readfirstlane_b32 s99, %0\n\t"
      "s_sub_u32 s98, s98, %2\n\ts_sub_u32 s98, s98, s99\n\ts_cmp_lt_i32 s98, %3\n\ts_cbranch_scc1 .Lpa%="
      : "=&v"(o) : "v"(clk), "s"(ph), "n"(T), "n"(4 * R) : "s98", "s99", "scc", "memory");
  SCHED_FENCE();
}

template <int T, int R>
__device__ __forceinline__ unsigned pace_m(unsigned clk, int ph) {
  unsigned o, tl;
  SCHED_FENCE();
  asm volatile(
      "s_mov_b32 %1, 0\n\ts_branch .Lpb%=\n"
      ".Lpa%=:\n\ts_sleep 1\n"
      ".Lpb%=:\n\tds_read_b32 %0, %2 offset:%5\n\ts_getreg_b32 s97, hwreg(HW_REG_IB_STS, 0, 4)\n\ts_memrealtime s[98:99]\n\t"
      "s_or_b32 s97, s97, %1\n\ts_waitcnt lgkmcnt(0)\n\ts_cselect_b32 %1, %1, s98\n\tv_readfirstlane_b32 s99, %0\n\t"
      "s_sub_u32 s98, s98, %3\n\ts_sub_u32 s98, s98, s99\n\ts_cmp_lt_i32 s98, %4\n\ts_cbranch_scc1 .Lpa%="
      : "=&v"(o), "=&s"(tl) : "v"(clk), "s"(ph), "n"(T), "n"(4 * R) : "s97", "s98", "s99", "scc", "memory");
  SCHED_FENCE();
  return tl;
}

#define STAMP(i)
#define STAMP_FLUSH()
#define TT(i)
constexpr int L_QP = 0;
constexpr int L_QS = L_QP + 4 * 128;
constexpr int L_PART = L_QS + 96;
constexpr int L_ML = L_PART + 8 * DH;
constexpr int L_Z = L_ML + 16;
constexpr int L_Y = L_Z + 320;
constexpr int L_H = L_Y + 512;
constexpr int L_SY = L_H + 64;
constexpr int L_CK = L_SY + 8;
constexpr int L_TE = L_CK + 16;
constexpr int L_END = L_TE + 8;

#define PIN(x) asm volatile("" : "+v"(x))

__device__ __forceinline__ void wg_ml(const float* lds, float& M, float& L) {
  float mm[8], ll[8];
#pragma unroll
  for (int i = 0; i < 4; ++i) {
    const f32x4 t = *reinterpret_cast<const f32x4*>(lds + L_ML + 4 * i);
    mm[2 * i] = t[0];
    ll[2 * i] = t[1];
    mm[2 * i + 1] = t[2];
    ll[2 * i + 1] = t[3];
  }
  M = mm[0];
#pragma unroll
  for (int ww = 1; ww < 8; ++ww) M = fmaxf(M, mm[ww]);
  L = 0.f;
#pragma unroll
  for (int ww = 0; ww < 8; ++ww) L = fmaf(ex2((mm[ww] - M) * LOG2E), ll[ww], L);
}
__device__ __forceinline__ unsigned lds_addr(const float* p) {
  return (unsigned)(size_t)(const __attribute__((address_space(3))) float*)p;
}

template <bool early>
__device__ __forceinline__ void body(float* lds, const int h, const int sp, const int w, const bf16* __restrict__ x,
                                     const bf16* __restrict__ kc, const bf16* __restrict__ vc,
                                     const bf16* __restrict__ wq, const bf16* __restrict__ wo,
                                     const bf16* __restrict__ w1, const bf16* __restrict__ w3,
                                     const bf16* __restrict__ w2, u64* __restrict__ gw, bf16* __restrict__ out,
                                     unsigned cpf, unsigned tz0) {
  const int b = blockIdx.x;
  const int tid = threadIdx.x, lane = tid & 63;
  const int g = lane >> 3, c = lane & 7;
  const int rec = h * NSP + sp;

  STAMP(0);
  const bool isF = sp < 6;
  u32x4 PR[8], xa, xb, xq;
  unsigned epoch = 0, tk = 0;
  int ra = 0, RA0 = 0;
  bool va = false;
  if constexpr (early) {
    int s0_ = 240, s1_ = 80, s2_ = 240 * (sp & 1), s3_ = 80 * sp, s4_ = 64 * (sp >> 1), s5_ = 64 * w, s6_ = 24 * w;
    asm("" : "+s"(s0_), "+s"(s1_), "+s"(s2_), "+s"(s3_), "+s"(s4_), "+s"(s5_), "+s"(s6_));
    const int rw0 = isF ? s5_ : s6_;
    int lim = (isF ? s0_ : s1_) - rw0, lim2 = isF ? 64 : 24;
    asm("" : "+s"(lim), "+s"(lim2));
    lim = lim < lim2 ? lim : lim2;
    ra = rw0 + 8 * g;
    va = 8 * g < lim;
    RA0 = isF ? s2_ : s3_;
    int aoff = (int)__umul24(g, 8 * HQ * DH * 2) + 16 * c;
    PIN(aoff);
    aoff = va ? aoff : NOWHERE;
    int sa = ((RA0 + rw0) * (HQ * DH) + h * DH + (isF ? s4_ : 64)) * 2;
    PIN(aoff);
    asm volatile("" : "+s"(sa));
    SCHED_FENCE();
    const __amdgpu_buffer_rsrc_t rw = rsrc(wq, NOWHERE);
    PR[0] = __builtin_amdgcn_raw_buffer_load_b128(rw, aoff, sa, 2);
    SCHED_FENCE();
    tk = NOWT();
    SCHED_FENCE();
    asm volatile("s_load_dword %0, %1, %2" : "=&s"(epoch) : "s"(gw), "s"(E1P && w == 1 ? (int)GL * 8 + (sp * 8 + (b & 7)) * LSP : (int)GEP * 8) : "memory");
    SCHED_FENCE();
#pragma unroll
    for (int i = 1; i < 8; ++i) PR[i] = __builtin_amdgcn_raw_buffer_load_b128(rw, aoff, sa + i * (HQ * DH * 2), 2);
    SCHED_FENCE();
  }
  const unsigned short* xs = reinterpret_cast<const unsigned short*>(x);
  const size_t wgb = ((size_t)h * SK + (SK - WIN)) * DH;
  const __amdgpu_buffer_rsrc_t rk = rsrc(kc + wgb, WIN * DH * 2);
  const __amdgpu_buffer_rsrc_t rv = rsrc(vc + wgb, WIN * DH * 2);
  const int voff = (g * DH + 8 * c) * 2;
  auto tile = [&](int t) { return (t * 64 + h * HROT + w * 8 + sp) & 511; };
  u32x4 K[8][3], V[8][3];
  auto kld = [&](int t, int j) { K[t][j] = __builtin_amdgcn_raw_buffer_load_b128(rk, voff + 128 * j, tile(t) * 3072, 2); };
  auto vld = [&](int t, int j) { V[t][j] = __builtin_amdgcn_raw_buffer_load_b128(rv, voff + 128 * j, tile(t) * 3072, 2); };
  unsigned tag = 0;
  const unsigned clk = lds_addr(lds + L_CK);
  const int ph = (w * PV_) >> 3;
  const int r4 = lane >> 4, c16 = lane & 15;
  const int ch = c16 < 10 ? c16 : 9;
  const int fi = (h & 3) * 6 + (isF ? sp : 5), gl = fi >> 3, gI = (h >> 2) * 3 + gl, fr = fi & 7;
  u32x4 WO[6], X[6], W2[4];
  unsigned tch = 0;
  const int wooff = ((48 * w + r4 + (isF ? 24 * (g & 1) : 24)) * D + 64 * sp + 8 * c + (isF ? 0 : 128 * (g & 1))) * 2;
  typedef unsigned u32x2 __attribute__((ext_vector_type(2)));
  const __amdgpu_buffer_rsrc_t rl = rsrc(gw + GL, 16 * 64 * LSP);
  const bool pub = !((sp + (b >> 6)) & 3);
  const int lw_ = lane == 0 && pub ? (sp * 8 + (b & 7)) * LSP : NOWHERE;
  u32x2 lat = {0u, 0u};
  u64 own = 0, pre = 0;
  unsigned pro = 0, st1 = 0, old = 0;
  bool vslow = false, kslow = false;
  if constexpr (early) {
    u32x4 PF[5];
    const int F0 = (isF ? 160 * (fr >> 1) : 480) + 40 * w;
    const int fs = isF ? FF : HQ * DH;
    int s6_ = 64 * gI, s7_ = h * DH + 128 * (sp & 1);
    asm("" : "+s"(s6_), "+s"(s7_));
    const int boff = ((int)__umul24(g, fs) + 8 * c) * 2;
    const int sb = (F0 * fs + (isF ? s6_ : s7_)) * 2;
    const bf16* pf_ = (fr & 1) ? w3 : w1;
    asm("" : "+s"(pf_));
    const __amdgpu_buffer_rsrc_t rf = rsrc(isF ? pf_ : wq, NOWHERE);
    unsigned short xr[5];
#pragma unroll
    for (int i = 0; i < 5; ++i) PF[i] = __builtin_amdgcn_raw_buffer_load_b128(rf, boff, sb + i * 16 * fs, 2);
    SCHED_FENCE();
    if (!isF) {
#pragma unroll
      for (int i = 0; i < 6; ++i)
        X[i] = __builtin_amdgcn_raw_buffer_load_b128(rsrc(wo + (size_t)h * DH * D, DH * D * 2), wooff - 24 * D * 2 + i * (4 * D * 2), 0, 2);
    } else {
#pragma unroll
      for (int i = 0; i < 6; ++i) X[i] = u32x4{0u, 0u, 0u, 0u};
    }
    SCHED_FENCE();
    xa = *reinterpret_cast<const u32x4*>(x + 8 * lane);
    xb = *reinterpret_cast<const u32x4*>(x + 512 + 8 * (lane & 15));
    xq = *reinterpret_cast<const u32x4*>(x + RA0 + (va ? ra : 0));
#pragma unroll
    for (int i = 0; i < 5; ++i) xr[i] = xs[F0 + 8 * i + g];
    SCHED_FENCE();
    const unsigned epv = __hip_atomic_load(reinterpret_cast<unsigned*>(gw + GEP), __ATOMIC_RELAXED, __HIP_MEMORY_SCOPE_AGENT);
    SCHED_FENCE();
    if (tid < 17) reinterpret_cast<unsigned*>(lds)[L_CK + tid] = tk;
    if (tid == 0) reinterpret_cast<unsigned*>(lds)[L_TE + 5] = tz0;
    asm volatile("s_waitcnt lgkmcnt(0)\n\ts_barrier" : "+s"(epoch)::"memory");
    STAMP(6);
    SCHED_FENCE();
#pragma unroll
    for (int t = 0; t < 3; ++t)
#pragma unroll
      for (int j = 0; j < 3; ++j) kld(t, j);
    SCHED_FENCE();
    float mb, den;
    {
      float xf[8], xg[8];
#pragma unroll
      for (int i = 0; i < 4; ++i) {
        xf[2 * i] = bf_lo(xa[i]);
        xf[2 * i + 1] = bf_hi(xa[i]);
        xg[2 * i] = bf_lo(xb[i]);
        xg[2 * i + 1] = bf_hi(xb[i]);
      }
      const bool lo16 = lane < 16;
      float sum = 0.f;
#pragma unroll
      for (int i = 0; i < 8; ++i) sum += xf[i] + (lo16 ? xg[i] : 0.f);
      sum = wave_sum(sum);
      const float mean = sum * (1.0f / D);
      float sq = 0.f;
#pragma unroll
      for (int i = 0; i < 8; ++i) {
        const float d1 = xf[i] - mean, d2 = xg[i] - mean;
        sq += d1 * d1 + (lo16 ? d2 * d2 : 0.f);
      }
      sq = wave_sum(sq);
      mb = bfr(mean);
      const float vb = bfr(sq * (1.0f / D));
      den = bfr(sqrtf(bfr(vb + 1e-5f)));
    }
    {
      unsigned xn[13];
#pragma unroll
      for (int i = 0; i < 4; ++i) {
        xn[2 * i] = fbits(bfr(bfr(bf_lo(xq[i]) - mb) / den));
        xn[2 * i + 1] = fbits(bfr(bfr(bf_hi(xq[i]) - mb) / den));
      }
#pragma unroll
      for (int i = 0; i < 5; ++i) xn[8 + i] = fbits(bfr(bfr(bf_lo(xr[i]) - mb) / den));
#pragma unroll
      for (int i = 0; i < 13; ++i) PIN(xn[i]);
      SCHED_FENCE();
      pace<t_round(2), 2>(clk, ph);
#pragma unroll
      for (int j = 0; j < 3; ++j) kld(3, j);
      TT(2);
      SCHED_FENCE();
      float fa[8], fg[8];
#pragma unroll
      for (int k = 0; k < 8; ++k) fa[k] = fg[k] = 0.f;
#pragma unroll
      for (int i = 0; i < 8; ++i)
#pragma unroll
        for (int k = 0; k < 4; ++k) {
          fa[2 * k] = dot2(PR[i][k], xn[i] >> 16, fa[2 * k]);
          fa[2 * k + 1] = dot2(PR[i][k], xn[i], fa[2 * k + 1]);
        }
      lds[L_QP + w * 128 + 8 * c + g] = xg_reduce8(fa, g & 1);
#pragma unroll
      for (int i = 0; i < 5; ++i)
#pragma unroll
        for (int k = 0; k < 4; ++k) {
          fg[2 * k] = dot2(PF[i][k], xn[8 + i] >> 16, fg[2 * k]);
          fg[2 * k + 1] = dot2(PF[i][k], xn[8 + i], fg[2 * k + 1]);
        }
      lds[L_QP + w * 128 + 64 + 8 * c + g] = xg_reduce8(fg, g & 1);
    }
    SCHED_FENCE();
    tag = __builtin_amdgcn_readfirstlane(epv) + 1u;
    if (tid == 0) reinterpret_cast<unsigned*>(lds)[L_SY + 3] = tag;
    LDS_DRAIN();
    if (lane == 0) old = lds_add(lds, L_SY);
    old = __builtin_amdgcn_readfirstlane(old);
    if (old == 3) {
      const float2* qp = reinterpret_cast<const float2*>(lds + L_QP) + lane;
      const float2 p0_ = qp[0], p1_ = qp[64], p2_ = qp[128], p3_ = qp[192];
      gst16(gw + GQ + (size_t)rec * QS + 2 * lane, fbits((p0_.x + p1_.x) + (p2_.x + p3_.x)),
            fbits((p0_.y + p1_.y) + (p2_.y + p3_.y)), tag);
    }
    STAMP(11);
  } else {
    if (tid >= NT - 8) reinterpret_cast<unsigned*>(lds)[L_SY + tid - (NT - 8)] = 0u;
    unsigned st0 = 0;
    {
      const bf16* tp = w == 4 ? kc + wgb : w == 5 ? (!isF ? wo + (size_t)h * DH * D : (fr & 1) ? w3 : w1) : w == 6 ? ((sp & 1) ? x : wq) : vc + wgb;
      asm volatile("s_waitcnt lgkmcnt(0)\n\ts_load_dword %0, %1, 0x0" : "=&s"(st0) : "s"(tp) : "memory");
    }
    SCHED_FENCE();
#pragma unroll
    for (int j = 0; j < 3; ++j) kld(0, j);
    SCHED_FENCE();
    asm volatile("s_barrier" ::: "memory");
    STAMP(6);
    SCHED_FENCE();
    if (w < 6) {
      const int l = lane < 42 ? lane : 41, k = l & 15;
      const int yg = 4 * (k < 8 || isF ? sp : sp + 2) + ((k >> 1) & 3);
      const size_t yo = l < 32 || !isF ? GY + (size_t)yg * YB + 32 * h + 16 * (h >> 4) + 8 * (k & 1)
                                       : GZ + (size_t)(fr * 5 + (k >> 1)) * YB + 32 * gI + 16 * (gI >> 4) + 8 * (k & 1);
      unsigned o4 = (unsigned)(l < 16 ? G2 + (size_t)rec * RS + 8 * l : yo);
      unsigned o5 = (unsigned)(GQ + (size_t)rec * QS + 8 * (lane < 16 ? lane : 15));
      if (GLP && pub && lane >= 16 && lane < 32 && ((PM >> (lane - 16)) & 1))
        o5 = (unsigned)GL + (unsigned)(((lane - 16) * 64 + sp * 8 + (b & 7)) * (LSP / 8));
      PIN(o4);
      PIN(o5);
      pre = gld(gw + (w == 4 ? o4 : o5));
    }
    if (w == 7 && h < 5)
      pro = __hip_atomic_load(reinterpret_cast<const unsigned*>(out + 80 * sp + 16 * h) + (lane & 7), __ATOMIC_RELAXED,
                              __HIP_MEMORY_SCOPE_AGENT);
    SCHED_FENCE();
    asm volatile("s_waitcnt lgkmcnt(0)" ::"s"(st0) : "memory");
#pragma unroll
    for (int j = 0; j < 3; ++j) {
      kld(1, j);
      THRN(5);
    }
    THRN(3);
    TT(1);
    own = __builtin_amdgcn_s_memrealtime() - SB_;
    __builtin_amdgcn_raw_buffer_store_b64(u32x2{(unsigned)own, (unsigned)(own >> 32)}, rl, w == 6 ? lw_ : NOWHERE, 0, SC1);
    SCHED_FENCE();
    if (LOC && w == 6 && lane < 16) {
      const unsigned o = (unsigned)own - LOC;
      if ((int)(o - reinterpret_cast<unsigned*>(lds)[L_CK + lane]) > 0) reinterpret_cast<unsigned*>(lds)[L_CK + lane] = o;
    }
    SCHED_FENCE();
    {
      const bf16* tp = (w & 1) ? wo + (size_t)h * DH * D : w2 + (size_t)gI * 64 * D;
      asm volatile("s_load_dword %0, %1, 0x0" : "=&s"(st1) : "s"(tp) : "memory");
    }
    SCHED_FENCE();
  }
  u32x4 qf[3];
  float sc[8], mw, lw, acc[24];
  unsigned pb[8];
  auto score = [&](int t) {
    float d = 0.f;
#pragma unroll
    for (int j = 0; j < 3; ++j)
#pragma unroll
      for (int i = 0; i < 4; ++i) d = dot2(K[t][j][i], qf[j][i], d);
    d = grp8_sum(d);
    sc[t] = bfr(bfr(d) * SCALE);
  };
  auto pv = [&](int t) {
    const unsigned plo = pb[t], phi = plo << 16;
#pragma unroll
    for (int j = 0; j < 3; ++j)
#pragma unroll
      for (int i = 0; i < 4; ++i) {
        acc[8 * j + 2 * i] = dot2(V[t][j][i], plo, t ? acc[8 * j + 2 * i] : 0.f);
        acc[8 * j + 2 * i + 1] = dot2(V[t][j][i], phi, t ? acc[8 * j + 2 * i + 1] : 0.f);
        PIN(acc[8 * j + 2 * i]);
        PIN(acc[8 * j + 2 * i + 1]);
      }
  };
  const bool hf = lane >> 5;
  const __amdgpu_buffer_rsrc_t rq_ = rsrc(gw + GQ, NWG * QS * 8);
  int go[9];
  {
    const int l16 = (lane & 31) * 16;
    const int qa = l16 + (8 * h + (hf ? 4 : 0)) * (QS * 8);
    go[0] = qa;
    go[1] = qa + QS * 8;
    go[2] = l16 + (8 * h + (hf ? 7 : 6)) * (QS * 8) + 512;
    go[3] = qa + 2 * QS * 8;
    go[4] = qa + 3 * QS * 8;
#pragma unroll
    for (int k = 0; k < 4; ++k) {
      const int m = 8 * gl + 2 * k + (hf ? 1 : 0);
      go[5 + k] = isF ? l16 + (32 * (h >> 2) + m + (((m * 43) >> 7) & 6)) * (QS * 8) + 512 : NOWHERE;
    }
  }
  const bool gath = old == GRW;
  u32x4 pa[9];
  constexpr int C0 = early ? CE : CL;
  static_assert(C0 + 3 < 15 && C0 > GR + 1 && C0 >= 6, "schedule");
  sfor<early ? 3 : 2, 16>([&](auto R) {
    constexpr int r = R.v, t = r >= 8 ? r - 8 : r + early;
    unsigned tl = 0;
    if constexpr (!early && MEAS && ((PM >> r) & 1)) tl = pace_m<t_round(r), r>(clk, ph); else pace<t_round(r), r>(clk, ph);
    if constexpr (!early && ((PM >> (r - 1)) & 1)) {
      lat = __builtin_amdgcn_raw_buffer_load_b64(rl, w == MW ? lane * LSP : NOWHERE, 64 * LSP * (r - 1), SC1);
      SCHED_FENCE();
    }
    if constexpr (r < 8 && t == 8) {
      const __amdgpu_buffer_rsrc_t rx = rsrc(w2 + ((size_t)gI * 64 + 16 * w) * D + 80 * fr, isF ? 16 * D * 2 : 0);
#pragma unroll
      for (int i = 0; i < 4; ++i)
        W2[i] = __builtin_amdgcn_raw_buffer_load_b128(rx, (r4 * D + 8 * ch) * 2 + i * (4 * D * 2), 0, 2);
      tch = __builtin_amdgcn_raw_buffer_load_b32(rsrc(wo + (size_t)h * DH * D, DH * D * 2), tid ? NOWHERE : wooff, 0, 2);
    } else {
#pragma unroll
      for (int j = 0; j < 3; ++j) {
        if (r >= 8) vld(t, j); else kld(t, j);
        if (r == 15 && j == 2) break;
        if (early && r == GR + 1) continue;
        THRN(early && r == 8 ? NFL + 2 : NFL);
      }
    }
    TT(r);
    SCHED_FENCE();
    if constexpr (early) {
      if (r == GR && gath) {
#pragma unroll
        for (int s = 0; s < 9; ++s) pa[s] = __builtin_amdgcn_raw_buffer_load_b128(rq_, go[s], 0, SC1);
        SCHED_FENCE();
      }
      if (r == GR + 1) {
        if (gath) {
          const unsigned tf = isF ? tag : 0u;
          bool ok = true;
#pragma unroll
          for (int s = 0; s < 9; ++s) ok = ok & (pa[s][1] == (s < 5 ? tag : tf)) & (pa[s][3] == (s < 5 ? tag : tf));
          if (!__all(ok)) {
            for (unsigned spin = 0;; ++spin) {
              ok = true;
              asm volatile("" ::: "memory");
#pragma unroll
              for (int s = 0; s < 9; ++s) {
                pa[s] = __builtin_amdgcn_raw_buffer_load_b128(rq_, go[s], 0, SC1);
                ok = ok & (pa[s][1] == (s < 5 ? tag : tf)) & (pa[s][3] == (s < 5 ? tag : tf));
              }
              if (__all(ok) || spin >= SPIN_MAX) break;
              __builtin_amdgcn_s_sleep(1);
            }
          }
          float qo[2], qm[2], hu[2];
#pragma unroll
          for (int k = 0; k < 2; ++k) {
            auto val = [&](int s) { return __builtin_bit_cast(float, (unsigned)pa[s][2 * k]); };
            qo[k] = (val(0) + val(1)) + val(2);
            const float t_ = val(3) + val(4);
            qm[k] = sw32(t_, t_);
            const float u = bfr((val(5) + val(6)) + (val(7) + val(8)));
            auto sw = __builtin_amdgcn_permlane32_swap(fbits(u), fbits(u), false, false);
            hu[k] = bfr(bfr(silu(__builtin_bit_cast(float, (unsigned)sw[0]))) * __builtin_bit_cast(float, (unsigned)sw[1]));
          }
          reinterpret_cast<unsigned*>(lds + L_QS)[lane + (hf ? 32 : 0)] = bfpack(qo[0], qo[1]);
          if (hf) {
            reinterpret_cast<unsigned*>(lds + L_QS)[lane] = bfpack(qm[0], qm[1]);
          } else {
            *reinterpret_cast<float2*>(lds + L_H + 2 * lane) = float2{hu[0], hu[1]};
          }
          LDS_DRAIN();
          if (lane == 0) lds_add(lds, L_SY + 2);
        }
        STAMP(2);
      }
    } else {
      if (r == 2) asm volatile("" ::"s"(st1), "v"(cpf), "v"(pre), "v"(pro));
      if (r == AR) lat = __builtin_amdgcn_raw_buffer_load_b64(rl, w == 6 ? lane * LSP : NOWHERE, 0, SC1);
      if (r == AR + 1) {
        const u64 d = (((u64)lat[1] << 32) | lat[0]) - own + 200u;
        const bool ok = d <= 400u;
        const float s_ = wave_sum(ok ? (float)(unsigned)d : 0.f), n = (float)__builtin_popcountll(__ballot(ok));
        const unsigned o = (unsigned)own + (n > 0.f ? (unsigned)(s_ * __builtin_amdgcn_rcpf(n)) - 200u : 0u);
        if (w == 6 && lane < 16) reinterpret_cast<unsigned*>(lds)[L_CK + lane] = o;
        if (w == 6 && lane == 0) reinterpret_cast<unsigned*>(lds)[L_TE + 4] = o;
      }
      if ((PM >> r) & 1) {
        const unsigned org = __builtin_amdgcn_readfirstlane(reinterpret_cast<const unsigned*>(lds)[L_CK + r]);
        tag = __builtin_amdgcn_readfirstlane(reinterpret_cast<const unsigned*>(lds)[L_SY + 3]);
        THRN(3);
        const unsigned now = NOWT() - 9;
        const int l = (int)((tl ? tl : now) - org) - (t_round(r) + ph);
        __builtin_amdgcn_raw_buffer_store_b64(u32x2{(unsigned)(l < -64 ? 0 : l + 64), tag}, rl, w == MW && l < ULIM ? lw_ : NOWHERE,
                                              64 * LSP * r, SC1);
      }
      SCHED_FENCE();
    }
    if (r == C0) {
      lds_wait(lds, L_SY + 2, 1u);
      STAMP(3);
#pragma unroll
      for (int j = 0; j < 3; ++j) qf[j] = *reinterpret_cast<const u32x4*>(lds + L_QS + (c + 8 * j) * 4);
#pragma unroll
      for (int k = 0; k < 4; ++k) score(k);
    } else if (r == C0 + 1) {
#pragma unroll
      for (int k = 4; k < 7; ++k) score(k);
    } else if (r == C0 + 2) {
      score(7);
      mw = sc[0];
#pragma unroll
      for (int k = 1; k < 8; ++k) mw = fmaxf(mw, sc[k]);
      mw = xg_max(mw);
      lw = 0.f;
#pragma unroll
      for (int k = 0; k < 8; ++k) {
        const float p = ex2((sc[k] - mw) * LOG2E);
        lw += p;
        pb[k] = bfbits(p);
      }
      lw = xg_sum(lw);
      if (lane == 0) {
        lds[L_ML + 2 * w] = mw;
        lds[L_ML + 2 * w + 1] = lw;
      }
      if (early) {
        float z[8];
#pragma unroll
        for (int n = 0; n < 8; ++n) z[n] = 0.f;
#pragma unroll
        for (int r = 0; r < 4; ++r) {
          const unsigned chi = fbits(lds[L_H + 16 * w + 4 * r + r4]), clo = chi >> 16;
#pragma unroll
          for (int n = 0; n < 4; ++n) {
            z[2 * n] = dot2(W2[r][n], clo, z[2 * n]);
            z[2 * n + 1] = dot2(W2[r][n], chi, z[2 * n + 1]);
          }
        }
        const float2 zr = rows_reduce(z);
        if (c16 < 10) *reinterpret_cast<float2*>(lds + L_Z + 80 * w + 8 * c16 + 2 * r4) = zr;
      }
      LDS_DRAIN();
      if (lane == 0) lds_add(lds, L_SY + 4);
      STAMP(4);
    } else if (r == C0 + 3) {
#pragma unroll
      for (int k = 0; k < t + 1 - PVL; ++k) pv(k);
    } else if (r > C0 + 3 && r < 15) {
      pv(t - PVL);
    }
    if constexpr (!early && ((PM >> (r - 1)) & 1)) {
      SCHED_FENCE();
      constexpr int p = r - 1;
      const float m = wave_sum((float)(lat[1] == tag && lat[0] < UMISS + 64 ? lat[0] : UMISS + 64)) * (1.f / 64) - 64.f;
      int d = 0;
      if (p <= 8) {
        kslow = m >= ATHR;
        if (kslow) d = (int)(m * (ASH / 4.f)) + AGX * (lane <= p ? 0 : lane < 8 ? lane - p : 8 - p);
      } else if (!vslow && m >= (p <= 10 ? (kslow ? UTA : UTHR) : UTH2)) {
        vslow = true;
        d = (int)(fmaxf(m, 0.f) * ((p <= 10 ? USH : USH2) / 4.f)) + (lane > p ? UGX * (lane - p) : 0);
      } else if (vslow && m < CT) {
        d = lane > p + 1 ? -UGX * (lane - p - 1) : 0;
      }
      if (w == MW && lane < 16) reinterpret_cast<unsigned*>(lds)[L_CK + lane] += d;
      if (w == MW && lane == 0) reinterpret_cast<int*>(lds)[L_TE + (p <= 8 ? 1 : p <= 10 ? 2 : 3)] = (int)m;
    }
  });
  asm volatile("" ::"v"(tch));
  if (PRI && !early) __builtin_amdgcn_s_setprio(3);
  SCHED_FENCE();
  const __amdgpu_buffer_rsrc_t rwo = rsrc(wo + (size_t)h * DH * D, DH * D * 2);
  lds_wait(lds, L_SY + 4, 8u);
  float f, M, L;
  wg_ml(lds, M, L);
  f = ex2((mw - M) * LOG2E) / L;
  const unsigned cw = fbits(fmaf(M, LOG2E, __builtin_amdgcn_logf(L)));
  if (!early) tag = __builtin_amdgcn_readfirstlane(reinterpret_cast<const unsigned*>(lds)[L_SY + 3]);
  PIN(f);
  SCHED_FENCE();
  const int l40 = lane < 40 ? lane : 39;
  u64* rdst = gw + G2 + (size_t)rec * RS + 32 * r4 + 2 * c16;
  u64* zdst = gw + GZ + (size_t)(fr * 5 + (l40 >> 3)) * YB + 32 * gI + 16 * (gI >> 4) + 2 * (l40 & 7);
  unsigned pdst = lds_addr(lds + L_PART + w * DH + 8 * c + g), psrc = lds_addr(lds + L_PART + 48 * r4 + 4 * c16);
  unsigned cnt6 = lds_addr(lds + L_SY + 6), one = 1u;
  u32x4 rq = {cw, tag, cw, tag};
  u64 m48 = 0x0fff0fff0fff0fffull, m52 = 0x1fff1fff1fff1fffull;
  asm volatile("" : "+s"(m48), "+s"(m52), "+{v[156:159]}"(rq));
  PIN(rdst);
  PIN(zdst);
  PIN(pdst);
  PIN(psrc);
  PIN(cnt6);
  PIN(one);
#pragma unroll
  for (int k = 7 - PVL; k < 7; ++k) pv(k);
  auto wo1 = [&]() {
    if (WT_) pace<T_LAST + WT_, 15>(clk, ph);
    SCHED_FENCE();
#pragma unroll
    for (int i = 0; i < W1N; ++i) WO[i] = __builtin_amdgcn_raw_buffer_load_b128(rwo, wooff, i * (4 * D * 2), 2);
    SCHED_FENCE();
  };
  {
    unsigned plo = pb[7], phi = plo << 16;
    PIN(plo);
    PIN(phi);
    sfor<0, 3>([&](auto J) {
      constexpr int j = J.v;
      SCHED_FENCE();
      float a[8];
#pragma unroll
      for (int i = 0; i < 4; ++i) {
        a[2 * i] = dot2(V[7][j][i], plo, acc[8 * j + 2 * i]);
        a[2 * i + 1] = dot2(V[7][j][i], phi, acc[8 * j + 2 * i + 1]);
      }
      xg_store<256 * j>(a, f, pdst);
    });
    if (early) wo1();
  }
  STAMP(5);
  unsigned arr;
  asm volatile(
      "s_mov_b64 exec, 1\n\tds_add_rtn_u32 v112, %[cnt], %[one]\n\ts_mov_b64 exec, %[m48]\n\t" SUM8("%[src]")
      "v_readfirstlane_b32 %[arr], v112\n\t" SUM8B
      "s_cmp_eq_u32 %[arr], 7\n\t" SUM8C
      "v_cvt_pk_bf16_f32 v156, v116, v117\n\tv_cvt_pk_bf16_f32 v158, v118, v119\n\t"
      "s_cselect_b64 exec, %[m52], 0\n\tglobal_store_dwordx4 %[dst], v[156:159], off sc1\n\ts_mov_b64 exec, -1\n\ts_nop 0"
      : [arr] "=&s"(arr), "+{v[156:159]}"(rq)
      : [cnt] "v"(cnt6), [one] "v"(one), [m48] "s"(m48), [m52] "s"(m52), [src] "v"(psrc), [dst] "v"(rdst)
      : CLOB_T, "scc", "memory");
  if (arr == 0 && isF) {
    const float2* zp = reinterpret_cast<const float2*>(lds + L_Z) + l40;
    const float2 z0 = zp[0], z1 = zp[40], z2 = zp[80], z3 = zp[120];
    gst16(zdst, fbits((z0.x + z1.x) + (z2.x + z3.x)), fbits((z0.y + z1.y) + (z2.y + z3.y)), tag);
  }
  if constexpr (!early) {
    if (PRI) __builtin_amdgcn_s_setprio(0);
    if (w == 7 && h < 5) {
      const int col = 80 * sp + 16 * h + 2 * c + (lane >> 5);
      const float xv = bf_lo(xs[col]);
      const u64* py = gw + GZ + (size_t)(sp * 5 + h) * YB + 32 * g + 2 * c + 392;
      bf16* po = out + col;
      float gt = 0.f;
      u64 m3 = 0ull, ms = 0ull;
      PIN(po);
#pragma unroll 1
      for (int pass = 0; pass < 2; ++pass) {
        lds_wait(lds, L_SY + (pass ? 1 : 6), pass ? 4u : 8u);
        STAMP(8);
        asm volatile("" : "+s"(m3), "+s"(ms));
        float t;
        unsigned n;
        asm volatile(
            "v_mov_b32 v124, 0\n\tv_mov_b32 v125, %[tag]\n\tv_mov_b32 v126, 0\n\tv_mov_b32 v127, %[tag]\n\t"
            "s_mov_b64 vcc, -1\n\ts_mov_b32 %[n], 0\n"
            ".Lfp%=:\n\t"
            "s_add_u32 %[n], %[n], 1\n\ts_cmp_gt_u32 %[n], %[max]\n\ts_cbranch_scc1 .Lfd%=\n\t"
            "s_mov_b64 exec, vcc\n\t"
            "global_load_dwordx4 v[112:115], %[p], off offset:-3136 sc1\n\t"
            "global_load_dwordx4 v[116:119], %[p], off offset:-1088 sc1\n\t"
            "global_load_dwordx4 v[120:123], %[p], off offset:1088 sc1\n\t"
            "s_and_b64 exec, vcc, %[m3]\n\t"
            "global_load_dwordx4 v[124:127], %[p], off offset:3136 sc1\n\t"
            "s_mov_b64 exec, -1\n\ts_waitcnt vmcnt(0)\n\t"
            "v_bitop3_b32 v128, v113, v115, %[tag] bitop3:0x7e\n\tv_bitop3_b32 v129, v117, v119, %[tag] bitop3:0x7e\n\t"
            "v_bitop3_b32 v130, v121, v123, %[tag] bitop3:0x7e\n\tv_bitop3_b32 v131, v125, v127, %[tag] bitop3:0x7e\n\t"
            "v_or3_b32 v128, v128, v129, v130\n\tv_add_f32 v132, v112, v116\n\tv_or_b32 v128, v128, v131\n\t"
            "v_add_f32 v134, v120, v124\n\tv_cmp_ne_u32 vcc, 0, v128\n\tv_add_f32 v133, v114, v118\n\t"
            "s_cbranch_vccnz .Lfp%=\n"
            ".Lfd%=:\n\t"
            "v_add_f32 v135, v122, v126\n\tv_add_f32 v132, v132, v134\n\tv_add_f32 v133, v133, v135\n\ts_nop 1\n\t"
            "v_permlane32_swap_b32 v132, v133\n\tv_add_f32 v134, v132, v133\n\tv_add_f32 v135, v132, v133\n\ts_nop 1\n\t"
            "v_permlane16_swap_b32 v134, v135\n\tv_add_f32 v134, v134, v135\n\ts_nop 1\n\t"
            "v_add_f32_dpp v134, v134, v134 row_ror:8 row_mask:0xf bank_mask:0xf\n\t"
            "v_cvt_pk_bf16_f32 v134, v134, v134\n\tv_lshlrev_b32 %[t], 16, v134\n\t"
            "v_mul_f32 v134, %[t], %[gt]\n\tv_cvt_pk_bf16_f32 v134, v134, v134\n\tv_lshlrev_b32 v134, 16, v134\n\t"
            "v_add_f32 v134, %[xv], v134\n\tv_cvt_pk_bf16_f32 v134, v134, v134\n\t"
            "s_mov_b64 exec, %[ms]\n\tglobal_store_short %[po], v134, off sc1\n\ts_mov_b64 exec, -1"
            : [t] "=&v"(t), [n] "=&s"(n)
            : [tag] "s"(tag), [max] "n"(SPIN_MAX), [p] "v"(py), [m3] "s"(m3), [ms] "s"(ms), [gt] "v"(gt), [xv] "v"(xv), [po] "v"(po)
            : CLOB_T, "vcc", "scc", "memory");
        if (pass) break;
        gt = bfr(silu(t));
        py -= GZ - GY;
        m3 = ~0ull;
        ms = 0x000000ff000000ffull;
      }
      if (b == 0 && lane == 0)
        __hip_atomic_store(reinterpret_cast<unsigned*>(gw + GEP), tag, __ATOMIC_RELAXED, __HIP_MEMORY_SCOPE_AGENT);
      if (b == 0) {
        const volatile unsigned* lu = reinterpret_cast<const volatile unsigned*>(lds);
        const unsigned now = NOWT(), tk0 = lu[L_TE];
        int code = 0;
        if (TEL == 1) code = (int)(lu[L_TE + 4] - tk0) / 5 + 20;
        if (TEL == 2) code = (int)(lu[L_CK + 15] - lu[L_TE + 4]) / 5;
        if (TEL == 3) code = (int)(now - lu[L_CK + 15] - T_LAST - 300) / 5;
        if (TEL == 7) code = (int)(tk0 - lu[L_TE + 5]) / 5;
        if (TEL >= 4 && TEL < 7) code = (int)lu[L_TE + TEL - 3] + 20;
        code = code < 0 ? 0 : code > 200 ? 200 : code;
        while (NOWT() - tk0 < 10000u + 400u * code) __builtin_amdgcn_s_sleep(4);
      }
      STAMP(10);
    }
    STAMP_FLUSH();
    asm volatile(".Lcode_lastb:");
    return;
  }
  pace<T_LAST + W2T, 15>(clk, ph);
#pragma unroll
  for (int i = W1N; i < 6; ++i) WO[i] = __builtin_amdgcn_raw_buffer_load_b128(rwo, wooff, i * (4 * D * 2), 2);
  unsigned dm0 = __builtin_amdgcn_raw_buffer_load_b32(rwo, NOWHERE, 0, 2), dm1 = __builtin_amdgcn_raw_buffer_load_b32(rwo, NOWHERE, 4, 2);
  STAMP(3);
  SCHED_FENCE();
  const int ly = isF ? lane & 31 : lane;
  u64* ydst = gw + GY + (size_t)(4 * sp + (ly >> 3) + 4 * (ly >> 5)) * YB + 32 * h + 16 * (h >> 4) + 2 * (ly & 7);
  static_assert(RS * 8 * 4 == 4608, "record r4 + 4 is 4608 bytes behind record r4");
  const u64* pr = gw + G2 + (size_t)(h * NSP + r4 + 2) * RS + 32 * w + 2 * c16;
  const u64 ownm = 0xffffull << (16 * (sp & 3));
  u64 own0 = sp < 4 ? ownm : 0ull, own1 = sp < 4 ? 0ull : ownm, my = isF ? 0xffffffffull : ~0ull;
  u64 for0 = m52 & ~own0, for1 = m52 & ~own1;
  own0 &= m48;
  own1 &= m48;
  unsigned osrc = lds_addr(lds + L_PART + 48 * w + 4 * c16), ysrc = lds_addr(lds + L_Y + 2 * ly), cnt1 = lds_addr(lds + L_SY + 1);
  unsigned ydl = lds_addr(lds + L_Y + 128 * w + (isF ? 8 * c + g : 8 * c16 + 2 * r4));
  asm volatile("" : "+s"(own0), "+s"(own1), "+s"(for0), "+s"(for1), "+s"(my));
  PIN(ydst);
  PIN(pr);
  PIN(osrc);
  PIN(ysrc);
  PIN(cnt1);
  PIN(ydl);
  u32x8 WP[3], XP[3];
#pragma unroll
  for (int p = 0; p < 3; ++p)
#pragma unroll
    for (int k = 0; k < 4; ++k) {
      XP[p][2 * k] = __builtin_amdgcn_perm(X[2 * p + 1][k], X[2 * p][k], 0x05040100u);
      XP[p][2 * k + 1] = __builtin_amdgcn_perm(X[2 * p + 1][k], X[2 * p][k], 0x07060302u);
    }
  asm volatile("" : "+{v[64:71]}"(XP[0]), "+{v[72:79]}"(XP[1]), "+{v[80:87]}"(XP[2]));
  SCHED_FENCE();
  auto perm_wo = [&]() {
#pragma unroll
    for (int p = 0; p < 3; ++p)
#pragma unroll
      for (int k = 0; k < 4; ++k) {
        WP[p][2 * k] = __builtin_amdgcn_perm(WO[2 * p + 1][k], WO[2 * p][k], 0x05040100u);
        WP[p][2 * k + 1] = __builtin_amdgcn_perm(WO[2 * p + 1][k], WO[2 * p][k], 0x07060302u);
      }
    asm volatile("" : "+{v[88:95]}"(WP[0]), "+{v[96:103]}"(WP[1]), "+{v[104:111]}"(WP[2]));
    SCHED_FENCE();
    STAMP(1);
  };
  lds_wait(lds, L_SY + 6, 8u);
  if (PT_) pace<T_LAST + PT_, 15>(clk, 0);
  STAMP(4);
  if (PRI) __builtin_amdgcn_s_setprio(3);
  u32x4 ra0, ra1;
  asm volatile(
      "v_mov_b32 v148, %[cw]\n\tv_mov_b32 v149, %[tag]\n\tv_mov_b32 v151, %[tag]\n\t"
      "v_mov_b32 v152, %[cw]\n\tv_mov_b32 v153, %[tag]\n\tv_mov_b32 v155, %[tag]\n\t"
      "s_mov_b64 exec, %[for0]\n\tglobal_load_dwordx4 v[148:151], %[p], off offset:-2304 sc1\n\t"
      "s_mov_b64 exec, %[for1]\n\tglobal_load_dwordx4 v[152:155], %[p], off offset:2304 sc1\n\t"
      "s_mov_b64 exec, %[m48]\n\t" SUM8("%[src]") SUM8B SUM8C
      "s_mov_b64 exec, %[own0]\n\tv_cvt_pk_bf16_f32 v148, v116, v117\n\tv_cvt_pk_bf16_f32 v150, v118, v119\n\t"
      "s_mov_b64 exec, %[own1]\n\tv_cvt_pk_bf16_f32 v152, v116, v117\n\tv_cvt_pk_bf16_f32 v154, v118, v119\n\t"
      "s_mov_b64 exec, -1"
      : "=&{v[148:151]}"(ra0), "=&{v[152:155]}"(ra1)
      : [cw] "v"(cw), [tag] "s"(tag), [for0] "s"(for0), [for1] "s"(for1), [p] "v"(pr), [own0] "s"(own0), [own1] "s"(own1),
        [m48] "s"(m48), [src] "v"(osrc)
      : CLOB_T, "memory");
  SCHED_FENCE();
  perm_wo();
  unsigned np;
  asm volatile(
      "s_mov_b32 %[n], 0\n\t"
      "s_mov_b64 exec, -1\n\tv_mov_b64 v[160:161], 0\n\tv_mov_b64 v[162:163], 0\n\tv_mov_b64 v[164:165], 0\n\t"
      "v_mov_b64 v[166:167], 0\n\ts_branch .Lrc%=\n"
      ".Lrp%=:\n\t"
      "s_add_u32 %[n], %[n], 1\n\ts_cmp_gt_u32 %[n], %[max]\n\ts_cbranch_scc1 .Lrd%=\n\t"
      "v_cmp_ne_u32 vcc, 0, v112\n\tv_cmp_ne_u32 s[98:99], 0, v113\n\ts_sleep " STR(RSLEEP) "\n\t"
      "s_mov_b64 exec, vcc\n\tglobal_load_dwordx4 v[148:151], %[p], off offset:-2304 sc1\n\t"
      "s_mov_b64 exec, s[98:99]\n\tglobal_load_dwordx4 v[152:155], %[p], off offset:2304 sc1\n\t"
      "s_mov_b64 exec, -1\n"
      ".Lrc%=:\n\t"
      "s_waitcnt vmcnt(0)\n\t"
      "v_bitop3_b32 v112, v149, v151, %[tag] bitop3:0x7e\n\tv_bitop3_b32 v113, v153, v155, %[tag] bitop3:0x7e\n\t"
      "v_max_f32 v115, v148, v152\n\tv_or_b32 v114, v112, v113\n\tv_max_f32 v116, v148, v152\n\t"
      "v_cmp_ne_u32 vcc, 0, v114\n\tv_lshlrev_b32 v122, 16, v148\n\ts_cbranch_vccnz .Lrp%=\n"
      ".Lrd%=:\n\t"
      "v_permlane16_swap_b32 v115, v116\n\tv_max_f32 v117, v115, v116\n\tv_max_f32 v118, v115, v116\n\t"
      "v_and_b32 v123, 0xffff0000, v148\n\tv_lshlrev_b32 v124, 16, v150\n\t"
      "v_permlane32_swap_b32 v117, v118\n\tv_max_f32 v117, v117, v118\n\t"
      "v_sub_f32 v115, v148, v117\n\tv_sub_f32 v116, v152, v117\n\tv_exp_f32 v115, v115\n\tv_exp_f32 v116, v116\n\t"
      "v_and_b32 v125, 0xffff0000, v150\n\tv_lshlrev_b32 v126, 16, v152\n\t"
      "v_add_f32 v117, v115, v116\n\tv_add_f32 v118, v115, v116\n\t"
      "v_mul_f32_dpp v122, v115, v122" BC12 "v_mul_f32_dpp v123, v115, v123" BC12
      "v_permlane16_swap_b32 v117, v118\n\tv_add_f32 v119, v117, v118\n\tv_add_f32 v120, v117, v118\n\t"
      "v_mul_f32_dpp v124, v115, v124" BC12 "v_mul_f32_dpp v125, v115, v125" BC12
      "v_permlane32_swap_b32 v119, v120\n\tv_add_f32 v119, v119, v120\n\tv_lshlrev_b32 v128, 16, v154\n\t"
      "v_rcp_f32 v119, v119\n\tv_and_b32 v127, 0xffff0000, v152\n\tv_and_b32 v129, 0xffff0000, v154\n\t"
      "v_fmac_f32_dpp v122, v116, v126" BC12 "v_fmac_f32_dpp v124, v116, v128" BC12
      "v_fmac_f32_dpp v123, v116, v127" BC12 "v_fmac_f32_dpp v125, v116, v129" BC12
      "v_permlane32_swap_b32 v122, v124\n\tv_add_f32 v122, v122, v124\n\t"
      "v_permlane32_swap_b32 v123, v125\n\tv_add_f32 v123, v123, v125\n\t"
      "v_mov_b32_dpp v120, v119" BC12 "s_nop 0\n\t"
      "v_permlane16_swap_b32 v122, v123\n\tv_add_f32 v122, v122, v123\n\t"
      "v_mul_f32 v123, v122, v120\n\ts_cmp_lt_u32 %[sp], 6\n\t"
      "v_mul_f32_dpp v124, v122, v120 quad_perm:[1,0,3,2] row_mask:0xf bank_mask:0xf\n\t"
      "v_cvt_pk_bf16_f32 v124, v123, v124\n\t"
      "s_cbranch_scc1 .Lyf%=\n\ts_nop 0\n\t"
      DOT8(64, 65, 66, 67, 68, 69, 70, 71, 0) DOT8(72, 73, 74, 75, 76, 77, 78, 79, 2) DOT8(80, 81, 82, 83, 84, 85, 86, 87, 4)
      DOT8(88, 89, 90, 91, 92, 93, 94, 95, 6) DOT8(96, 97, 98, 99, 100, 101, 102, 103, 8)
      DOT8(104, 105, 106, 107, 108, 109, 110, 111, 10) YRED
      "v_pk_add_f32 v[160:161], v[160:161], v[162:163]\n\tds_write_b64 %[ydl], v[160:161]\n"
      ".Lys%=:\n\t"
      "s_mov_b64 exec, 1\n\tds_add_rtn_u32 v112, %[cnt], %[one]\n\ts_mov_b64 exec, -1\n\t"
      "ds_read2st64_b64 v[116:119], %[ysrc] offset0:0 offset1:1\n\tds_read2st64_b64 v[120:123], %[ysrc] offset0:2 offset1:3\n\t"
      "s_waitcnt lgkmcnt(2)\n\tv_readfirstlane_b32 s98, v112\n\ts_waitcnt lgkmcnt(1)\n\t"
      "v_pk_add_f32 v[116:117], v[116:117], v[118:119]\n\ts_cmp_eq_u32 s98, 3\n\ts_cselect_b64 exec, %[my], 0\n\t"
      "s_waitcnt lgkmcnt(0)\n\tv_pk_add_f32 v[120:121], v[120:121], v[122:123]\n\ts_nop 0\n\t"
      "v_add_f32 v156, v116, v120\n\tv_add_f32 v158, v117, v121\n\t"
      "global_store_dwordx4 %[dst], v[156:159], off sc1\n\ts_mov_b64 exec, -1\n\ts_branch .Lye%=\n"
      ".Lyf%=:\n\ts_nop 0\n\t"
      "v_mov_b32_dpp v125, v124 row_newbcast:0 row_mask:0xf bank_mask:0x3\n\t"
      "v_mov_b32_dpp v126, v124 row_newbcast:2 row_mask:0xf bank_mask:0x3\n\t"
      "v_mov_b32_dpp v127, v124 row_newbcast:4 row_mask:0xf bank_mask:0x3\n\t"
      "v_mov_b32_dpp v125, v124 row_newbcast:6 row_mask:0xf bank_mask:0xc\n\t"
      "v_mov_b32_dpp v126, v124 row_newbcast:8 row_mask:0xf bank_mask:0xc\n\t"
      "v_mov_b32_dpp v127, v124 row_newbcast:10 row_mask:0xf bank_mask:0xc\n\t"
      DOT8F(88, 89, 90, 91, 92, 93, 94, 95, 125) DOT8F(96, 97, 98, 99, 100, 101, 102, 103, 126)
      DOT8F(104, 105, 106, 107, 108, 109, 110, 111, 127) YRED
      "v_add_f32 v160, v160, v162\n\tv_add_f32 v161, v161, v163\n\ts_nop 0\n\t"
      "v_add_f32_dpp v160, v160, v160 row_ror:8 row_mask:0xf bank_mask:0x3\n\t"
      "v_add_f32_dpp v160, v161, v161 row_ror:8 row_mask:0xf bank_mask:0xc\n\t"
      "ds_write_b32 %[ydl], v160\n\ts_branch .Lys%=\n"
      ".Lye%=:"
      : [n] "=&s"(np), "+{v[156:159]}"(rq), "+{v[148:151]}"(ra0), "+{v[152:155]}"(ra1)
      : [tag] "s"(tag), [p] "v"(pr), [max] "n"(SPIN_MAX), [sp] "s"(sp), [ydl] "v"(ydl), [cnt] "v"(cnt1), [one] "v"(one),
        [ysrc] "v"(ysrc), [my] "s"(my), [dst] "v"(ydst), "{v[64:71]}"(XP[0]), "{v[72:79]}"(XP[1]), "{v[80:87]}"(XP[2]),
        "{v[88:95]}"(WP[0]), "{v[96:103]}"(WP[1]), "{v[104:111]}"(WP[2])
      : CLOB_T, "v160", "v161", "v162", "v163", "v164", "v165", "v166", "v167", "s98", "s99", "vcc", "scc", "memory");
  STAMP(9);
  asm volatile("" ::"v"(dm0), "v"(dm1));
  STAMP_FLUSH();
  asm volatile(".Lcode_last:");
}

__global__ void __launch_bounds__(NT, 2)
fused_decode(const bf16* __restrict__ wq, const bf16* __restrict__ w1, const bf16* __restrict__ w3, u64* __restrict__ gw,
             const bf16* __restrict__ x, const bf16* __restrict__ kc, const bf16* __restrict__ vc,
             const bf16* __restrict__ wo, const bf16* __restrict__ w2, bf16* __restrict__ out) {
  __shared__ __attribute__((aligned(16))) float lds[L_END];
  const unsigned tz0 = NOWT();
  const int b = blockIdx.x;
  const int xcd = b & 7, jj = b >> 3;
  const int h = xcd * 4 + (jj >> 3), sp = jj & 7;
  const int w = __builtin_amdgcn_readfirstlane(threadIdx.x >> 6);
  unsigned cpf = threadIdx.x;
  {
    u64 pc;
    unsigned len;
    asm volatile(
        "s_cmp_lt_u32 %[w], 4\n\ts_cbranch_scc1 .Lnocpf\n\t"
        "s_getpc_b64 %[pc]\n.Lcode_here:\n\tv_xor_b32 %[c], %[hi], %[c]\n\tv_lshlrev_b32 %[c], 6, %[c]\n\t"
        "s_mov_b32 %[len], .Lcode_last-.Lcode_here-4\n\ts_max_i32 %[len], %[len], .Lcode_lastb-.Lcode_here-4\n\t"
        "v_min_u32 %[c], %[len], %[c]\n\tglobal_load_dword %[c], %[c], %[pc]\n.Lnocpf:"
        : [c] "+v"(cpf), [pc] "=&s"(pc), [len] "=&s"(len) : [w] "s"(w), [hi] "s"((b & 24) << 5) : "scc", "memory");
  }
  SCHED_FENCE();
  if (w >= 4)
    body<false>(lds, h, sp, w, x, kc, vc, wq, wo, w1, w3, w2, gw, out, cpf, tz0);
  else
    body<true>(lds, h, sp, w, x, kc, vc, wq, wo, w1, w3, w2, gw, out, cpf, tz0);
}

}

extern "C" void kernel_launch(void* const* d_in, const int* in_sizes, int n_in, void* d_out, int out_size, void* d_ws,
                              size_t ws_size, hipStream_t stream) {
  const bf16* x = (const bf16*)d_in[0];
  const bf16* kc = (const bf16*)d_in[1];
  const bf16* vc = (const bf16*)d_in[2];
  const bf16* wq = (const bf16*)d_in[3];
  const bf16* wo = (const bf16*)d_in[4];
  const bf16* w1 = (const bf16*)d_in[6];
  const bf16* w3 = (const bf16*)d_in[7];
  const bf16* w2 = (const bf16*)d_in[8];
  u64* gw = (u64*)d_ws;
  fused_decode<<<NWG, NT, 0, stream>>>(wq, w1, w3, gw, x, kc, vc, wo, w2, (bf16*)d_out);
}
```

```cpp
#include <hip/hip_runtime.h>
#include <hip/hip_bf16.h>
#include <stdint.h>

typedef __hip_bfloat16 bf16;
typedef unsigned long long u64;
typedef unsigned u32x4 __attribute__((ext_vector_type(4)));
typedef unsigned u32x8 __attribute__((ext_vector_type(8)));
typedef float f32x4 __attribute__((ext_vector_type(4)));
typedef float f32x2 __attribute__((ext_vector_type(2)));
typedef __bf16 bf16x2_t __attribute__((ext_vector_type(2)));

namespace {

constexpr int D = 640, HQ = 32, DH = 192, SK = 32768, WIN = 4096, FF = 1536;
constexpr int NSP = 8;
constexpr int NWG = HQ * NSP;
constexpr int NT = 512;
constexpr float SCALE = 0.07216878364870322f;
constexpr float LOG2E = 1.4426950408889634f;
constexpr unsigned SPIN_MAX = 1u << 16;
constexpr int SC1 = 16;
constexpr int NOWHERE = 1 << 30;

#define PV_ 81
#define PK_ 82
#define PX_ PK_
#define TS_ 358
#define SB_ 246
#define LOC 30
#define WT_ 98
#define W2T 132
#define W1N 3
#define PT_ 215
#define NFL 7
#define CE 7
#define CL 8
#define AR 3
#define GR 5
#define GRW 2
#define RSLEEP 1
#define MW 4
#define PM 0x1440
#define MEAS 1
#define ATHR 20
#define ASH 4
#define AGX 7
#define UTHR 8
#define UTH2 40
#define USH 4
#define UGX 9
#define ULIM 20
#define UMISS 60
#define PVL 1
#define PB0 0
#define LSP 16
#define GLP 1
#define E1P 1
#define UTA 3
#define PRI 0
#define USH2 3
#define CT 10
#define KF 0
#define HROT 16
constexpr int t_round(int r) { return r <= 2 ? TS_ : t_round(r - 1) + (r > 8 ? PV_ : r == 8 ? PX_ : PK_); }
constexpr int T_LAST = t_round(15);
constexpr int t_late(int i) { return t_round(i); }
constexpr int t_early(int i) { return t_round(i); }

constexpr int QS = 144;
constexpr int YB = 2 * (16 * 32 + 16);
constexpr int RS = 144;
constexpr size_t GQ = 0;
constexpr size_t G2 = GQ + (size_t)NWG * QS;
constexpr size_t GY = G2 + (size_t)NWG * RS;
constexpr size_t GZ = GY + 16;
constexpr size_t GEP = GY + (size_t)NSP * 5 * YB;
constexpr size_t GL = GEP + 16;

__device__ __forceinline__ unsigned fbits(float x) { return __builtin_bit_cast(unsigned, x); }
__device__ __forceinline__ u64 gld(const u64* p) {
  return __hip_atomic_load(p, __ATOMIC_RELAXED, __HIP_MEMORY_SCOPE_AGENT);
}
__device__ __forceinline__ float bf_lo(unsigned u) { return __builtin_bit_cast(float, u << 16); }
__device__ __forceinline__ float bf_hi(unsigned u) { return __builtin_bit_cast(float, u & 0xffff0000u); }
__device__ __forceinline__ float bfr(float x) { return __bfloat162float(__float2bfloat16(x)); }
__device__ __forceinline__ unsigned short bfbits(float x) {
  return __builtin_bit_cast(unsigned short, __float2bfloat16(x));
}
__device__ __forceinline__ float dot2(unsigned a, unsigned b, float c) {
  return __builtin_amdgcn_fdot2_f32_bf16(__builtin_bit_cast(bf16x2_t, a), __builtin_bit_cast(bf16x2_t, b), c, false);
}
template <int CTRL>
__device__ __forceinline__ float dpp(float x) {
  return __builtin_bit_cast(float, __builtin_amdgcn_mov_dpp(__builtin_bit_cast(int, x), CTRL, 0xf, 0xf, true));
}
__device__ __forceinline__ float grp8_sum(float x) {
  x += dpp<0xB1>(x);
  x += dpp<0x4E>(x);
  x += dpp<0x141>(x);
  return x;
}
__device__ __forceinline__ float sw16(float a, float b) {
  auto s = __builtin_amdgcn_permlane16_swap(fbits(a), fbits(b), false, false);
  return __builtin_bit_cast(float, (unsigned)s[0]) + __builtin_bit_cast(float, (unsigned)s[1]);
}
__device__ __forceinline__ float sw32(float a, float b) {
  auto s = __builtin_amdgcn_permlane32_swap(fbits(a), fbits(b), false, false);
  return __builtin_bit_cast(float, (unsigned)s[0]) + __builtin_bit_cast(float, (unsigned)s[1]);
}
__device__ __forceinline__ float xg_sum(float x) {
  x += dpp<0x128>(x);
  x = sw16(x, x);
  return sw32(x, x);
}
__device__ __forceinline__ float xg_max(float x) {
  x = fmaxf(x, dpp<0x128>(x));
  auto s = __builtin_amdgcn_permlane16_swap(fbits(x), fbits(x), false, false);
  x = fmaxf(__builtin_bit_cast(float, (unsigned)s[0]), __builtin_bit_cast(float, (unsigned)s[1]));
  auto t = __builtin_amdgcn_permlane32_swap(fbits(x), fbits(x), false, false);
  return fmaxf(__builtin_bit_cast(float, (unsigned)t[0]), __builtin_bit_cast(float, (unsigned)t[1]));
}
__device__ __forceinline__ float wave_sum(float x) { return xg_sum(grp8_sum(x)); }
__device__ __forceinline__ float ex2(float x) { return __builtin_amdgcn_exp2f(x); }
__device__ __forceinline__ float silu(float x) { return x / (1.0f + __expf(-x)); }
__device__ __forceinline__ float xg_reduce8(const float (&a)[8], bool g0) {
  float r[4];
#pragma unroll
  for (int i = 0; i < 4; ++i) r[i] = sw32(a[i], a[i + 4]);
  const float s0 = sw16(r[0], r[2]), s1 = sw16(r[1], r[3]);
  const float keep = g0 ? s1 : s0, send = g0 ? s0 : s1;
  return keep + dpp<0x128>(send);
}
__device__ __forceinline__ __amdgpu_buffer_rsrc_t rsrc(const void* p, unsigned bytes) {
  return __builtin_amdgcn_make_buffer_rsrc(const_cast<void*>(p), 0, bytes, 0x00020000);
}
__device__ __forceinline__ void gst16(u64* p, unsigned v0, unsigned v1, unsigned tag) {
  const u32x4 o = {v0, tag, v1, tag};
  asm volatile("global_store_dwordx4 %0, %1, off sc1\n\ts_nop 1" ::"v"(p), "v"(o) : "memory");
}
__device__ __forceinline__ unsigned bfpack(float a, float b) {
  return __builtin_bit_cast(unsigned, __builtin_convertvector(f32x2{a, b}, bf16x2_t));
}
__device__ __forceinline__ float2 rows_reduce(const float (&v)[8]) {
  float r[4];
#pragma unroll
  for (int i = 0; i < 4; ++i) r[i] = sw32(v[i], v[i + 4]);
  return float2{sw16(r[0], r[2]), sw16(r[1], r[3])};
}
template <int OFF>
__device__ __forceinline__ void xg_store(const float (&v)[8], float f, unsigned a) {
  float p[4], q[4], t, u;
#pragma unroll
  for (int i = 0; i < 4; ++i) {
    auto s = __builtin_amdgcn_permlane32_swap(fbits(v[i]), fbits(v[i + 4]), false, false);
    p[i] = __builtin_bit_cast(float, (unsigned)s[0]);
    q[i] = __builtin_bit_cast(float, (unsigned)s[1]);
  }
  const f32x2 r01 = f32x2{p[0], p[1]} + f32x2{q[0], q[1]}, r23 = f32x2{p[2], p[3]} + f32x2{q[2], q[3]};
  auto s0 = __builtin_amdgcn_permlane16_swap(fbits(r01[0]), fbits(r23[0]), false, false);
  auto s1 = __builtin_amdgcn_permlane16_swap(fbits(r01[1]), fbits(r23[1]), false, false);
  asm volatile("v_add_f32 %0, %2, %3\n\tv_add_f32 %1, %4, %5\n\ts_nop 0\n\t"
               "v_add_f32_dpp %0, %0, %0 row_ror:8 row_mask:0xf bank_mask:0x3\n\t"
               "v_add_f32_dpp %0, %1, %1 row_ror:8 row_mask:0xf bank_mask:0xc\n\t"
               "v_mul_f32 %0, %6, %0\n\tds_write_b32 %7, %0 offset:%8"
               : "=&v"(t), "=&v"(u)
               : "v"((unsigned)s0[0]), "v"((unsigned)s0[1]), "v"((unsigned)s1[0]), "v"((unsigned)s1[1]), "v"(f), "v"(a), "n"(OFF)
               : "memory");
}
#define SUM8(a)                                                                                                          \
  "ds_read_b128 v[116:119], " a "\n\tds_read_b128 v[120:123], " a " offset:768\n\t"                                      \
  "ds_read_b128 v[124:127], " a " offset:1536\n\tds_read_b128 v[128:131], " a " offset:2304\n\t"                         \
  "ds_read_b128 v[132:135], " a " offset:3072\n\tds_read_b128 v[136:139], " a " offset:3840\n\t"                         \
  "ds_read_b128 v[140:143], " a " offset:4608\n\tds_read_b128 v[144:147], " a " offset:5376\n\ts_waitcnt lgkmcnt(6)\n\t"
#define SUM8B                                                                                                            \
  "v_pk_add_f32 v[116:117], v[116:117], v[120:121]\n\tv_pk_add_f32 v[118:119], v[118:119], v[122:123]\n\t"               \
  "s_waitcnt lgkmcnt(4)\n\t"                                                                                             \
  "v_pk_add_f32 v[124:125], v[124:125], v[128:129]\n\tv_pk_add_f32 v[126:127], v[126:127], v[130:131]\n\t"               \
  "v_pk_add_f32 v[116:117], v[116:117], v[124:125]\n\tv_pk_add_f32 v[118:119], v[118:119], v[126:127]\n\t"
#define SUM8C                                                                                                            \
  "s_waitcnt lgkmcnt(2)\n\t"                                                                                             \
  "v_pk_add_f32 v[132:133], v[132:133], v[136:137]\n\tv_pk_add_f32 v[134:135], v[134:135], v[138:139]\n\t"               \
  "s_waitcnt lgkmcnt(0)\n\t"                                                                                             \
  "v_pk_add_f32 v[140:141], v[140:141], v[144:145]\n\tv_pk_add_f32 v[142:143], v[142:143], v[146:147]\n\t"               \
  "v_pk_add_f32 v[132:133], v[132:133], v[140:141]\n\tv_pk_add_f32 v[134:135], v[134:135], v[142:143]\n\t"               \
  "v_pk_add_f32 v[116:117], v[116:117], v[132:133]\n\tv_pk_add_f32 v[118:119], v[118:119], v[134:135]\n\t"
#define BC12 " row_newbcast:12 row_mask:0xf bank_mask:0xf\n\t"
#define DOTB(y, a, n) "v_dot2c_f32_bf16_dpp v" #y ", v124, v" #a " row_newbcast:" #n " row_mask:0xf bank_mask:0xf\n\t"
#define DOT8(a, b, c, d, e, f, g, h, n) \
  DOTB(160, a, n) DOTB(161, b, n) DOTB(162, c, n) DOTB(163, d, n) DOTB(164, e, n) DOTB(165, f, n) DOTB(166, g, n) DOTB(167, h, n)
#define DOTF(y, a, n) "v_dot2c_f32_bf16 v" #y ", v" #n ", v" #a "\n\t"
#define DOT8F(a, b, c, d, e, f, g, h, n) \
  DOTF(160, a, n) DOTF(161, b, n) DOTF(162, c, n) DOTF(163, d, n) DOTF(164, e, n) DOTF(165, f, n) DOTF(166, g, n) DOTF(167, h, n)
#define YRED                                                                                                             \
  "v_permlane32_swap_b32 v160, v164\n\tv_permlane32_swap_b32 v161, v165\n\tv_permlane32_swap_b32 v162, v166\n\t"         \
  "v_permlane32_swap_b32 v163, v167\n\tv_pk_add_f32 v[160:161], v[160:161], v[164:165]\n\t"                              \
  "v_pk_add_f32 v[162:163], v[162:163], v[166:167]\n\ts_nop 1\n\t"                                                       \
  "v_permlane16_swap_b32 v160, v162\n\tv_permlane16_swap_b32 v161, v163\n\t"
#define CLOB_T                                                                                                     \
  "v112", "v113", "v114", "v115", "v116", "v117", "v118", "v119", "v120", "v121", "v122", "v123", "v124", "v125",        \
      "v126", "v127", "v128", "v129", "v130", "v131", "v132", "v133", "v134", "v135", "v136", "v137", "v138", "v139",    \
      "v140", "v141", "v142", "v143", "v144", "v145", "v146", "v147"
#define SCHED_FENCE() __builtin_amdgcn_sched_barrier(0)
#define STR2(x) #x
#define STR(x) STR2(x)
#define THRN(n) do { SCHED_FENCE(); asm volatile("s_waitcnt vmcnt(%0)" ::"n"(n) : "memory"); SCHED_FENCE(); } while (0)
#define NOWT() ((unsigned)__builtin_amdgcn_s_memrealtime())
template <int N>
struct IC {
  static constexpr int v = N;
};
template <int I, int E, class F>
__device__ __forceinline__ void sfor(F&& f) {
  if constexpr (I < E) {
    f(IC<I>{});
    sfor<I + 1, E>(f);
  }
}
#define LDS_DRAIN() asm volatile("s_waitcnt lgkmcnt(0)" ::: "memory")
__device__ __forceinline__ unsigned lds_add(float* lds, int idx) {
  return __hip_atomic_fetch_add(reinterpret_cast<unsigned*>(lds + idx), 1u, __ATOMIC_RELAXED, __HIP_MEMORY_SCOPE_WORKGROUP);
}
__device__ __forceinline__ void lds_wait(float* lds, int idx, unsigned n) {
  SCHED_FENCE();
#pragma unroll 1
  for (unsigned spin = 0; spin < (SPIN_MAX << 4); ++spin) {
    if (__hip_atomic_load(reinterpret_cast<unsigned*>(lds + idx), __ATOMIC_RELAXED, __HIP_MEMORY_SCOPE_WORKGROUP) >= n) break;
    __builtin_amdgcn_s_sleep(1);
  }
  asm volatile("" ::: "memory");
  SCHED_FENCE();
}
template <int T, int R>
__device__ __forceinline__ void pace(unsigned clk, int ph) {
  unsigned o;
  SCHED_FENCE();
  asm volatile(
      "s_branch .Lpb%=\n"
      ".Lpa%=:\n\ts_sleep 1\n"
      ".Lpb%=:\n\tds_read_b32 %0, %1 offset:%4\n\ts_memrealtime s[98:99]\n\ts_waitcnt lgkmcnt(0)\n\tv_readfirstlane_b32 s99, %0\n\t"
      "s_sub_u32 s98, s98, %2\n\ts_sub_u32 s98, s98, s99\n\ts_cmp_lt_i32 s98, %3\n\ts_cbranch_scc1 .Lpa%="
      : "=&v"(o) : "v"(clk), "s"(ph), "n"(T), "n"(4 * R) : "s98", "s99", "scc", "memory");
  SCHED_FENCE();
}

template <int T, int R>
__device__ __forceinline__ unsigned pace_m(unsigned clk, int ph) {
  unsigned o, tl;
  SCHED_FENCE();
  asm volatile(
      "s_mov_b32 %1, 0\n\ts_branch .Lpb%=\n"
      ".Lpa%=:\n\ts_sleep 1\n"
      ".Lpb%=:\n\tds_read_b32 %0, %2 offset:%5\n\ts_getreg_b32 s97, hwreg(HW_REG_IB_STS, 0, 4)\n\ts_memrealtime s[98:99]\n\t"
      "s_or_b32 s97, s97, %1\n\ts_waitcnt lgkmcnt(0)\n\ts_cselect_b32 %1, %1, s98\n\tv_readfirstlane_b32 s99, %0\n\t"
      "s_sub_u32 s98, s98, %3\n\ts_sub_u32 s98, s98, s99\n\ts_cmp_lt_i32 s98, %4\n\ts_cbranch_scc1 .Lpa%="
      : "=&v"(o), "=&s"(tl) : "v"(clk), "s"(ph), "n"(T), "n"(4 * R) : "s97", "s98", "s99", "scc", "memory");
  SCHED_FENCE();
  return tl;
}

#define STAMP(i)
#define STAMP_FLUSH()
#define TT(i)
constexpr int L_QP = 0;
constexpr int L_QS = L_QP + 4 * 128;
constexpr int L_PART = L_QS + 96;
constexpr int L_ML = L_PART + 8 * DH;
constexpr int L_Z = L_ML + 16;
constexpr int L_Y = L_Z + 320;
constexpr int L_H = L_Y + 512;
constexpr int L_SY = L_H + 64;
constexpr int L_CK = L_SY + 8;
constexpr int L_END = L_CK + 16;

#define PIN(x) asm volatile("" : "+v"(x))

__device__ __forceinline__ void wg_ml(const float* lds, float& M, float& L) {
  float mm[8], ll[8];
#pragma unroll
  for (int i = 0; i < 4; ++i) {
    const f32x4 t = *reinterpret_cast<const f32x4*>(lds + L_ML + 4 * i);
    mm[2 * i] = t[0];
    ll[2 * i] = t[1];
    mm[2 * i + 1] = t[2];
    ll[2 * i + 1] = t[3];
  }
  M = mm[0];
#pragma unroll
  for (int ww = 1; ww < 8; ++ww) M = fmaxf(M, mm[ww]);
  L = 0.f;
#pragma unroll
  for (int ww = 0; ww < 8; ++ww) L = fmaf(ex2((mm[ww] - M) * LOG2E), ll[ww], L);
}
__device__ __forceinline__ unsigned lds_addr(const float* p) {
  return (unsigned)(size_t)(const __attribute__((address_space(3))) float*)p;
}

template <bool early>
__device__ __forceinline__ void body(float* lds, const int h, const int sp, const int w, const bf16* __restrict__ x,
                                     const bf16* __restrict__ kc, const bf16* __restrict__ vc,
                                     const bf16* __restrict__ wq, const bf16* __restrict__ wo,
                                     const bf16* __restrict__ w1, const bf16* __restrict__ w3,
                                     const bf16* __restrict__ w2, u64* __restrict__ gw, bf16* __restrict__ out,
                                     unsigned cpf) {
  const int b = blockIdx.x;
  const int tid = threadIdx.x, lane = tid & 63;
  const int g = lane >> 3, c = lane & 7;
  const int rec = h * NSP + sp;

  STAMP(0);
  const bool isF = sp < 6;
  u32x4 PR[8], xa, xb, xq;
  unsigned epoch = 0;
  u64 tk = 0;
  int ra = 0, RA0 = 0;
  bool va = false;
  if constexpr (early) {
    int s0_ = 240, s1_ = 80, s2_ = 240 * (sp & 1), s3_ = 80 * sp, s4_ = 64 * (sp >> 1), s5_ = 64 * w, s6_ = 24 * w;
    asm("" : "+s"(s0_), "+s"(s1_), "+s"(s2_), "+s"(s3_), "+s"(s4_), "+s"(s5_), "+s"(s6_));
    const int rw0 = isF ? s5_ : s6_;
    int lim = (isF ? s0_ : s1_) - rw0, lim2 = isF ? 64 : 24;
    asm("" : "+s"(lim), "+s"(lim2));
    lim = lim < lim2 ? lim : lim2;
    ra = rw0 + 8 * g;
    va = 8 * g < lim;
    RA0 = isF ? s2_ : s3_;
    int aoff = (int)__umul24(g, 8 * HQ * DH * 2) + 16 * c;
    PIN(aoff);
    aoff = va ? aoff : NOWHERE;
    int sa = ((RA0 + rw0) * (HQ * DH) + h * DH + (isF ? s4_ : 64)) * 2;
    PIN(aoff);
    asm volatile("" : "+s"(sa));
    SCHED_FENCE();
    const __amdgpu_buffer_rsrc_t rw = rsrc(wq, NOWHERE);
    PR[0] = __builtin_amdgcn_raw_buffer_load_b128(rw, aoff, sa, 2);
    SCHED_FENCE();
    tk = __builtin_amdgcn_s_memrealtime();
    SCHED_FENCE();
    if (!KF) {
    asm volatile("s_load_dword %0, %1, %2" : "=&s"(epoch) : "s"(gw), "s"(E1P && w == 1 ? (int)GL * 8 + (sp * 8 + (b & 7)) * LSP : (int)GEP * 8) : "memory");
    SCHED_FENCE();
    }
#pragma unroll
    for (int i = 1; i < 8; ++i) PR[i] = __builtin_amdgcn_raw_buffer_load_b128(rw, aoff, sa + i * (HQ * DH * 2), 2);
    SCHED_FENCE();
    if (KF) {
    asm volatile("s_load_dword %0, %1, %2" : "=&s"(epoch) : "s"(gw), "s"(E1P && w == 1 ? (int)GL * 8 + (sp * 8 + (b & 7)) * LSP : (int)GEP * 8) : "memory");
    SCHED_FENCE();
    }
    SCHED_FENCE();
  }
  const unsigned short* xs = reinterpret_cast<const unsigned short*>(x);
  const size_t wgb = ((size_t)h * SK + (SK - WIN)) * DH;
  const __amdgpu_buffer_rsrc_t rk = rsrc(kc + wgb, WIN * DH * 2);
  const __amdgpu_buffer_rsrc_t rv = rsrc(vc + wgb, WIN * DH * 2);
  const int voff = (g * DH + 8 * c) * 2;
  auto tile = [&](int t) { return (t * 64 + h * HROT + w * 8 + sp) & 511; };
  u32x4 K[8][3], V[8][3];
  auto kld = [&](int t, int j) { K[t][j] = __builtin_amdgcn_raw_buffer_load_b128(rk, voff + 128 * j, tile(t) * 3072, 2); };
  auto vld = [&](int t, int j) { V[t][j] = __builtin_amdgcn_raw_buffer_load_b128(rv, voff + 128 * j, tile(t) * 3072, 2); };
  unsigned tag = 0;
  const unsigned clk = lds_addr(lds + L_CK);
  const int ph = (w * PV_) >> 3;
  const int r4 = lane >> 4, c16 = lane & 15;
  const int ch = c16 < 10 ? c16 : 9;
  const int fi = (h & 3) * 6 + (isF ? sp : 5), gl = fi >> 3, gI = (h >> 2) * 3 + gl, fr = fi & 7;
  u32x4 WO[6], X[6], W2[4];
  unsigned tch = 0;
  const int wooff = ((48 * w + r4 + (isF ? 24 * (g & 1) : 24)) * D + 64 * sp + 8 * c + (isF ? 0 : 128 * (g & 1))) * 2;
  typedef unsigned u32x2 __attribute__((ext_vector_type(2)));
  const __amdgpu_buffer_rsrc_t rl = rsrc(gw + GL, 16 * 64 * LSP);
  const bool pub = !((sp + (b >> 6)) & 3);
  const int lw_ = lane == 0 && pub ? (sp * 8 + (b & 7)) * LSP : NOWHERE;
  u32x2 lat = {0u, 0u};
  u64 own = 0, pre = 0;
  unsigned pro = 0, st1 = 0, old = 0;
  bool vslow = false, kslow = false;
  if constexpr (early) {
    u32x4 PF[5];
    const int F0 = (isF ? 160 * (fr >> 1) : 480) + 40 * w;
    const int fs = isF ? FF : HQ * DH;
    int s6_ = 64 * gI, s7_ = h * DH + 128 * (sp & 1);
    asm("" : "+s"(s6_), "+s"(s7_));
    const int boff = ((int)__umul24(g, fs) + 8 * c) * 2;
    const int sb = (F0 * fs + (isF ? s6_ : s7_)) * 2;
    const bf16* pf_ = (fr & 1) ? w3 : w1;
    asm("" : "+s"(pf_));
    const __amdgpu_buffer_rsrc_t rf = rsrc(isF ? pf_ : wq, NOWHERE);
    unsigned short xr[5];
#pragma unroll
    for (int i = 0; i < 5; ++i) PF[i] = __builtin_amdgcn_raw_buffer_load_b128(rf, boff, sb + i * 16 * fs, 2);
    SCHED_FENCE();
    if (!isF) {
#pragma unroll
      for (int i = 0; i < 6; ++i)
        X[i] = __builtin_amdgcn_raw_buffer_load_b128(rsrc(wo + (size_t)h * DH * D, DH * D * 2), wooff - 24 * D * 2 + i * (4 * D * 2), 0, 2);
    } else {
#pragma unroll
      for (int i = 0; i < 6; ++i) X[i] = u32x4{0u, 0u, 0u, 0u};
    }
    SCHED_FENCE();
    xa = *reinterpret_cast<const u32x4*>(x + 8 * lane);
    xb = *reinterpret_cast<const u32x4*>(x + 512 + 8 * (lane & 15));
    xq = *reinterpret_cast<const u32x4*>(x + RA0 + (va ? ra : 0));
#pragma unroll
    for (int i = 0; i < 5; ++i) xr[i] = xs[F0 + 8 * i + g];
    SCHED_FENCE();
    const unsigned epv = __hip_atomic_load(reinterpret_cast<unsigned*>(gw + GEP), __ATOMIC_RELAXED, __HIP_MEMORY_SCOPE_AGENT);
    SCHED_FENCE();
    asm volatile("" : "+s"(tk));
    if (tid < 16) reinterpret_cast<unsigned*>(lds)[L_CK + tid] = (unsigned)tk;
    asm volatile("s_waitcnt lgkmcnt(0)\n\ts_barrier" : "+s"(epoch)::"memory");
    STAMP(6);
    SCHED_FENCE();
#pragma unroll
    for (int t = 0; t < 3; ++t)
#pragma unroll
      for (int j = 0; j < 3; ++j) kld(t, j);
    SCHED_FENCE();
    float mb, den;
    {
      float xf[8], xg[8];
#pragma unroll
      for (int i = 0; i < 4; ++i) {
        xf[2 * i] = bf_lo(xa[i]);
        xf[2 * i + 1] = bf_hi(xa[i]);
        xg[2 * i] = bf_lo(xb[i]);
        xg[2 * i + 1] = bf_hi(xb[i]);
      }
      const bool lo16 = lane < 16;
      float sum = 0.f;
#pragma unroll
      for (int i = 0; i < 8; ++i) sum += xf[i] + (lo16 ? xg[i] : 0.f);
      sum = wave_sum(sum);
      const float mean = sum * (1.0f / D);
      float sq = 0.f;
#pragma unroll
      for (int i = 0; i < 8; ++i) {
        const float d1 = xf[i] - mean, d2 = xg[i] - mean;
        sq += d1 * d1 + (lo16 ? d2 * d2 : 0.f);
      }
      sq = wave_sum(sq);
      mb = bfr(mean);
      const float vb = bfr(sq * (1.0f / D));
      den = bfr(sqrtf(bfr(vb + 1e-5f)));
    }
    {
      unsigned xn[13];
#pragma unroll
      for (int i = 0; i < 4; ++i) {
        xn[2 * i] = fbits(bfr(bfr(bf_lo(xq[i]) - mb) / den));
        xn[2 * i + 1] = fbits(bfr(bfr(bf_hi(xq[i]) - mb) / den));
      }
#pragma unroll
      for (int i = 0; i < 5; ++i) xn[8 + i] = fbits(bfr(bfr(bf_lo(xr[i]) - mb) / den));
#pragma unroll
      for (int i = 0; i < 13; ++i) PIN(xn[i]);
      SCHED_FENCE();
      pace<t_round(2), 2>(clk, ph);
#pragma unroll
      for (int j = 0; j < 3; ++j) kld(3, j);
      TT(2);
      SCHED_FENCE();
      float fa[8], fg[8];
#pragma unroll
      for (int k = 0; k < 8; ++k) fa[k] = fg[k] = 0.f;
#pragma unroll
      for (int i = 0; i < 8; ++i)
#pragma unroll
        for (int k = 0; k < 4; ++k) {
          fa[2 * k] = dot2(PR[i][k], xn[i] >> 16, fa[2 * k]);
          fa[2 * k + 1] = dot2(PR[i][k], xn[i], fa[2 * k + 1]);
        }
      lds[L_QP + w * 128 + 8 * c + g] = xg_reduce8(fa, g & 1);
#pragma unroll
      for (int i = 0; i < 5; ++i)
#pragma unroll
        for (int k = 0; k < 4; ++k) {
          fg[2 * k] = dot2(PF[i][k], xn[8 + i] >> 16, fg[2 * k]);
          fg[2 * k + 1] = dot2(PF[i][k], xn[8 + i], fg[2 * k + 1]);
        }
      lds[L_QP + w * 128 + 64 + 8 * c + g] = xg_reduce8(fg, g & 1);
    }
    SCHED_FENCE();
    tag = __builtin_amdgcn_readfirstlane(epv) + 1u;
    if (tid == 0) reinterpret_cast<unsigned*>(lds)[L_SY + 3] = tag;
    LDS_DRAIN();
    if (lane == 0) old = lds_add(lds, L_SY);
    old = __builtin_amdgcn_readfirstlane(old);
    if (old == 3) {
      const float2* qp = reinterpret_cast<const float2*>(lds + L_QP) + lane;
      const float2 p0_ = qp[0], p1_ = qp[64], p2_ = qp[128], p3_ = qp[192];
      gst16(gw + GQ + (size_t)rec * QS + 2 * lane, fbits((p0_.x + p1_.x) + (p2_.x + p3_.x)),
            fbits((p0_.y + p1_.y) + (p2_.y + p3_.y)), tag);
    }
    STAMP(11);
  } else {
    if (tid >= NT - 8) reinterpret_cast<unsigned*>(lds)[L_SY + tid - (NT - 8)] = 0u;
    unsigned st0 = 0;
    if (KF) {
#pragma unroll
      for (int j = 0; j < 3; ++j) kld(0, j);
      SCHED_FENCE();
    }
    {
      const bf16* tp = w == 4 ? kc + wgb : w == 5 ? (!isF ? wo + (size_t)h * DH * D : (fr & 1) ? w3 : w1) : w == 6 ? ((sp & 1) ? x : wq) : vc + wgb;
      asm volatile("s_waitcnt lgkmcnt(0)\n\ts_load_dword %0, %1, 0x0" : "=&s"(st0) : "s"(tp) : "memory");
    }
    SCHED_FENCE();
    if (!KF) {
#pragma unroll
      for (int j = 0; j < 3; ++j) kld(0, j);
    }
    SCHED_FENCE();
    asm volatile("s_barrier" ::: "memory");
    STAMP(6);
    SCHED_FENCE();
    if (w < 6) {
      const int l = lane < 42 ? lane : 41, k = l & 15;
      const int yg = 4 * (k < 8 || isF ? sp : sp + 2) + ((k >> 1) & 3);
      const size_t yo = l < 32 || !isF ? GY + (size_t)yg * YB + 32 * h + 16 * (h >> 4) + 8 * (k & 1)
                                       : GZ + (size_t)(fr * 5 + (k >> 1)) * YB + 32 * gI + 16 * (gI >> 4) + 8 * (k & 1);
      unsigned o4 = (unsigned)(l < 16 ? G2 + (size_t)rec * RS + 8 * l : yo);
      unsigned o5 = (unsigned)(GQ + (size_t)rec * QS + 8 * (lane < 16 ? lane : 15));
      if (GLP && pub && lane >= 16 && lane < 32 && ((PM >> (lane - 16)) & 1))
        o5 = (unsigned)GL + (unsigned)(((lane - 16) * 64 + sp * 8 + (b & 7)) * (LSP / 8));
      PIN(o4);
      PIN(o5);
      pre = gld(gw + (w == 4 ? o4 : o5));
    }
    if (w == 7 && h < 5)
      pro = __hip_atomic_load(reinterpret_cast<const unsigned*>(out + 80 * sp + 16 * h) + (lane & 7), __ATOMIC_RELAXED,
                              __HIP_MEMORY_SCOPE_AGENT);
    SCHED_FENCE();
    asm volatile("s_waitcnt lgkmcnt(0)" ::"s"(st0) : "memory");
#pragma unroll
    for (int j = 0; j < 3; ++j) {
      kld(1, j);
      THRN(5);
    }
    THRN(3);
    TT(1);
    own = __builtin_amdgcn_s_memrealtime() - SB_;
    __builtin_amdgcn_raw_buffer_store_b64(u32x2{(unsigned)own, (unsigned)(own >> 32)}, rl, w == 6 ? lw_ : NOWHERE, 0, SC1);
    SCHED_FENCE();
    if (LOC && w == 6 && lane < 16) {
      const unsigned o = (unsigned)own - LOC;
      if ((int)(o - reinterpret_cast<unsigned*>(lds)[L_CK + lane]) > 0) reinterpret_cast<unsigned*>(lds)[L_CK + lane] = o;
    }
    SCHED_FENCE();
    {
      const bf16* tp = (w & 1) ? wo + (size_t)h * DH * D : w2 + (size_t)gI * 64 * D;
      asm volatile("s_load_dword %0, %1, 0x0" : "=&s"(st1) : "s"(tp) : "memory");
    }
    SCHED_FENCE();
  }
  u32x4 qf[3];
  float sc[8], mw, lw, acc[24];
  unsigned pb[8];
  auto score = [&](int t) {
    float d = 0.f;
#pragma unroll
    for (int j = 0; j < 3; ++j)
#pragma unroll
      for (int i = 0; i < 4; ++i) d = dot2(K[t][j][i], qf[j][i], d);
    d = grp8_sum(d);
    sc[t] = bfr(bfr(d) * SCALE);
  };
  auto pv = [&](int t) {
    const unsigned plo = pb[t], phi = plo << 16;
#pragma unroll
    for (int j = 0; j < 3; ++j)
#pragma unroll
      for (int i = 0; i < 4; ++i) {
        acc[8 * j + 2 * i] = dot2(V[t][j][i], plo, t ? acc[8 * j + 2 * i] : 0.f);
        acc[8 * j + 2 * i + 1] = dot2(V[t][j][i], phi, t ? acc[8 * j + 2 * i + 1] : 0.f);
        PIN(acc[8 * j + 2 * i]);
        PIN(acc[8 * j + 2 * i + 1]);
      }
  };
  const bool hf = lane >> 5;
  const __amdgpu_buffer_rsrc_t rq_ = rsrc(gw + GQ, NWG * QS * 8);
  int go[9];
  {
    const int l16 = (lane & 31) * 16;
    const int qa = l16 + (8 * h + (hf ? 4 : 0)) * (QS * 8);
    go[0] = qa;
    go[1] = qa + QS * 8;
    go[2] = l16 + (8 * h + (hf ? 7 : 6)) * (QS * 8) + 512;
    go[3] = qa + 2 * QS * 8;
    go[4] = qa + 3 * QS * 8;
#pragma unroll
    for (int k = 0; k < 4; ++k) {
      const int m = 8 * gl + 2 * k + (hf ? 1 : 0);
      go[5 + k] = isF ? l16 + (32 * (h >> 2) + m + (((m * 43) >> 7) & 6)) * (QS * 8) + 512 : NOWHERE;
    }
  }
  const bool gath = old == GRW;
  u32x4 pa[9];
  constexpr int C0 = early ? CE : CL;
  static_assert(C0 + 3 < 15 && C0 > GR + 1 && C0 >= 6, "schedule");
  sfor<early ? 3 : 2, 16>([&](auto R) {
    constexpr int r = R.v, t = r >= 8 ? r - 8 : r + early;
    unsigned tl = 0;
    if constexpr (!early && MEAS && ((PM >> r) & 1)) tl = pace_m<t_round(r), r>(clk, ph); else pace<t_round(r), r>(clk, ph);
    if constexpr (!early && ((PM >> (r - 1)) & 1)) {
      lat = __builtin_amdgcn_raw_buffer_load_b64(rl, w == MW ? lane * LSP : NOWHERE, 64 * LSP * (r - 1), SC1);
      SCHED_FENCE();
    }
    if constexpr (r < 8 && t == 8) {
      const __amdgpu_buffer_rsrc_t rx = rsrc(w2 + ((size_t)gI * 64 + 16 * w) * D + 80 * fr, isF ? 16 * D * 2 : 0);
#pragma unroll
      for (int i = 0; i < 4; ++i)
        W2[i] = __builtin_amdgcn_raw_buffer_load_b128(rx, (r4 * D + 8 * ch) * 2 + i * (4 * D * 2), 0, 2);
      tch = __builtin_amdgcn_raw_buffer_load_b32(rsrc(wo + (size_t)h * DH * D, DH * D * 2), tid ? NOWHERE : wooff, 0, 2);
    } else {
#pragma unroll
      for (int j = 0; j < 3; ++j) {
        if (r >= 8) vld(t, j); else kld(t, j);
        if (r == 15 && j == 2) break;
        if (early && r == GR + 1) continue;
        THRN(early && r == 8 ? NFL + 2 : NFL);
      }
    }
    TT(r);
    SCHED_FENCE();
    if constexpr (early) {
      if (r == GR && gath) {
#pragma unroll
        for (int s = 0; s < 9; ++s) pa[s] = __builtin_amdgcn_raw_buffer_load_b128(rq_, go[s], 0, SC1);
        SCHED_FENCE();
      }
      if (r == GR + 1) {
        if (gath) {
          const unsigned tf = isF ? tag : 0u;
          bool ok = true;
#pragma unroll
          for (int s = 0; s < 9; ++s) ok = ok & (pa[s][1] == (s < 5 ? tag : tf)) & (pa[s][3] == (s < 5 ? tag : tf));
          if (!__all(ok)) {
            for (unsigned spin = 0;; ++spin) {
              ok = true;
              asm volatile("" ::: "memory");
#pragma unroll
              for (int s = 0; s < 9; ++s) {
                pa[s] = __builtin_amdgcn_raw_buffer_load_b128(rq_, go[s], 0, SC1);
                ok = ok & (pa[s][1] == (s < 5 ? tag : tf)) & (pa[s][3] == (s < 5 ? tag : tf));
              }
              if (__all(ok) || spin >= SPIN_MAX) break;
              __builtin_amdgcn_s_sleep(1);
            }
          }
          float qo[2], qm[2], hu[2];
#pragma unroll
          for (int k = 0; k < 2; ++k) {
            auto val = [&](int s) { return __builtin_bit_cast(float, (unsigned)pa[s][2 * k]); };
            qo[k] = (val(0) + val(1)) + val(2);
            const float t_ = val(3) + val(4);
            qm[k] = sw32(t_, t_);
            const float u = bfr((val(5) + val(6)) + (val(7) + val(8)));
            auto sw = __builtin_amdgcn_permlane32_swap(fbits(u), fbits(u), false, false);
            hu[k] = bfr(bfr(silu(__builtin_bit_cast(float, (unsigned)sw[0]))) * __builtin_bit_cast(float, (unsigned)sw[1]));
          }
          reinterpret_cast<unsigned*>(lds + L_QS)[lane + (hf ? 32 : 0)] = bfpack(qo[0], qo[1]);
          if (hf) {
            reinterpret_cast<unsigned*>(lds + L_QS)[lane] = bfpack(qm[0], qm[1]);
          } else {
            *reinterpret_cast<float2*>(lds + L_H + 2 * lane) = float2{hu[0], hu[1]};
          }
          LDS_DRAIN();
          if (lane == 0) lds_add(lds, L_SY + 2);
        }
        STAMP(2);
      }
    } else {
      if (r == 2) asm volatile("" ::"s"(st1), "v"(cpf), "v"(pre), "v"(pro));
      if (r == AR) lat = __builtin_amdgcn_raw_buffer_load_b64(rl, w == 6 ? lane * LSP : NOWHERE, 0, SC1);
      if (r == AR + 1) {
        const u64 d = (((u64)lat[1] << 32) | lat[0]) - own + 200u;
        const bool ok = d <= 400u;
        const float s_ = wave_sum(ok ? (float)(unsigned)d : 0.f), n = (float)__builtin_popcountll(__ballot(ok));
        const unsigned o = (unsigned)own + (n > 0.f ? (unsigned)(s_ * __builtin_amdgcn_rcpf(n)) - 200u : 0u);
        if (w == 6 && lane < 16) reinterpret_cast<unsigned*>(lds)[L_CK + lane] = o;
      }
      if ((PM >> r) & 1) {
        const unsigned org = __builtin_amdgcn_readfirstlane(reinterpret_cast<const unsigned*>(lds)[L_CK + r]);
        tag = __builtin_amdgcn_readfirstlane(reinterpret_cast<const unsigned*>(lds)[L_SY + 3]);
        THRN(3);
        const unsigned now = NOWT() - 9;
        const int l = (int)((tl ? tl : now) - org) - (t_round(r) + ph);
        __builtin_amdgcn_raw_buffer_store_b64(u32x2{(unsigned)(l < -64 ? 0 : l + 64), tag}, rl, w == MW && l < ULIM ? lw_ : NOWHERE,
                                              64 * LSP * r, SC1);
      }
      SCHED_FENCE();
    }
    if (r == C0) {
      lds_wait(lds, L_SY + 2, 1u);
      STAMP(3);
#pragma unroll
      for (int j = 0; j < 3; ++j) qf[j] = *reinterpret_cast<const u32x4*>(lds + L_QS + (c + 8 * j) * 4);
#pragma unroll
      for (int k = 0; k < 4; ++k) score(k);
    } else if (r == C0 + 1) {
#pragma unroll
      for (int k = 4; k < 7; ++k) score(k);
    } else if (r == C0 + 2) {
      score(7);
      mw = sc[0];
#pragma unroll
      for (int k = 1; k < 8; ++k) mw = fmaxf(mw, sc[k]);
      mw = xg_max(mw);
      lw = 0.f;
#pragma unroll
      for (int k = 0; k < 8; ++k) {
        const float p = ex2((sc[k] - mw) * LOG2E);
        lw += p;
        pb[k] = bfbits(p);
      }
      lw = xg_sum(lw);
      if (lane == 0) {
        lds[L_ML + 2 * w] = mw;
        lds[L_ML + 2 * w + 1] = lw;
      }
      if (early) {
        float z[8];
#pragma unroll
        for (int n = 0; n < 8; ++n) z[n] = 0.f;
#pragma unroll
        for (int r = 0; r < 4; ++r) {
          const unsigned chi = fbits(lds[L_H + 16 * w + 4 * r + r4]), clo = chi >> 16;
#pragma unroll
          for (int n = 0; n < 4; ++n) {
            z[2 * n] = dot2(W2[r][n], clo, z[2 * n]);
            z[2 * n + 1] = dot2(W2[r][n], chi, z[2 * n + 1]);
          }
        }
        const float2 zr = rows_reduce(z);
        if (c16 < 10) *reinterpret_cast<float2*>(lds + L_Z + 80 * w + 8 * c16 + 2 * r4) = zr;
      }
      LDS_DRAIN();
      if (lane == 0) lds_add(lds, L_SY + 4);
      STAMP(4);
    } else if (r == C0 + 3) {
#pragma unroll
      for (int k = 0; k < t + 1 - PVL; ++k) pv(k);
    } else if (r > C0 + 3 && r < 15) {
      pv(t - PVL);
    }
    if constexpr (!early && ((PM >> (r - 1)) & 1)) {
      SCHED_FENCE();
      constexpr int p = r - 1;
      const float m = wave_sum((float)(lat[1] == tag && lat[0] < UMISS + 64 ? lat[0] : UMISS + 64)) * (1.f / 64) - 64.f;
      int d = 0;
      if (p <= 8) {
        kslow = m >= ATHR;
        if (kslow) d = (int)(m * (ASH / 4.f)) + AGX * (lane <= p ? 0 : lane < 8 ? lane - p : 8 - p);
      } else if (!vslow && m >= (p <= 10 ? (kslow ? UTA : UTHR) : UTH2)) {
        vslow = true;
        d = (int)(fmaxf(m, 0.f) * ((p <= 10 ? USH : USH2) / 4.f)) + (lane > p ? UGX * (lane - p) : 0);
      } else if (vslow && m < CT) {
        d = lane > p + 1 ? -UGX * (lane - p - 1) : 0;
      }
      if (w == MW && lane < 16) reinterpret_cast<unsigned*>(lds)[L_CK + lane] += d;
    }
  });
  asm volatile("" ::"v"(tch));
  if (PRI && !early) __builtin_amdgcn_s_setprio(3);
  SCHED_FENCE();
  const __amdgpu_buffer_rsrc_t rwo = rsrc(wo + (size_t)h * DH * D, DH * D * 2);
  lds_wait(lds, L_SY + 4, 8u);
  float f, M, L;
  wg_ml(lds, M, L);
  f = ex2((mw - M) * LOG2E) / L;
  const unsigned cw = fbits(fmaf(M, LOG2E, __builtin_amdgcn_logf(L)));
  if (!early) tag = __builtin_amdgcn_readfirstlane(reinterpret_cast<const unsigned*>(lds)[L_SY + 3]);
  PIN(f);
  SCHED_FENCE();
  const int l40 = lane < 40 ? lane : 39;
  u64* rdst = gw + G2 + (size_t)rec * RS + 32 * r4 + 2 * c16;
  u64* zdst = gw + GZ + (size_t)(fr * 5 + (l40 >> 3)) * YB + 32 * gI + 16 * (gI >> 4) + 2 * (l40 & 7);
  unsigned pdst = lds_addr(lds + L_PART + w * DH + 8 * c + g), psrc = lds_addr(lds + L_PART + 48 * r4 + 4 * c16);
  unsigned cnt6 = lds_addr(lds + L_SY + 6), one = 1u;
  u32x4 rq = {cw, tag, cw, tag};
  u64 m48 = 0x0fff0fff0fff0fffull, m52 = 0x1fff1fff1fff1fffull;
  asm volatile("" : "+s"(m48), "+s"(m52), "+{v[156:159]}"(rq));
  PIN(rdst);
  PIN(zdst);
  PIN(pdst);
  PIN(psrc);
  PIN(cnt6);
  PIN(one);
#pragma unroll
  for (int k = 7 - PVL; k < 7; ++k) pv(k);
  auto wo1 = [&]() {
    if (WT_) pace<T_LAST + WT_, 15>(clk, ph);
    SCHED_FENCE();
#pragma unroll
    for (int i = 0; i < W1N; ++i) WO[i] = __builtin_amdgcn_raw_buffer_load_b128(rwo, wooff, i * (4 * D * 2), 2);
    SCHED_FENCE();
  };
  {
    unsigned plo = pb[7], phi = plo << 16;
    PIN(plo);
    PIN(phi);
    sfor<0, 3>([&](auto J) {
      constexpr int j = J.v;
      SCHED_FENCE();
      float a[8];
#pragma unroll
      for (int i = 0; i < 4; ++i) {
        a[2 * i] = dot2(V[7][j][i], plo, acc[8 * j + 2 * i]);
        a[2 * i + 1] = dot2(V[7][j][i], phi, acc[8 * j + 2 * i + 1]);
      }
      xg_store<256 * j>(a, f, pdst);
    });
    if (early) wo1();
  }
  STAMP(5);
  unsigned arr;
  asm volatile(
      "s_mov_b64 exec, 1\n\tds_add_rtn_u32 v112, %[cnt], %[one]\n\ts_mov_b64 exec, %[m48]\n\t" SUM8("%[src]")
      "v_readfirstlane_b32 %[arr], v112\n\t" SUM8B
      "s_cmp_eq_u32 %[arr], 7\n\t" SUM8C
      "v_cvt_pk_bf16_f32 v156, v116, v117\n\tv_cvt_pk_bf16_f32 v158, v118, v119\n\t"
      "s_cselect_b64 exec, %[m52], 0\n\tglobal_store_dwordx4 %[dst], v[156:159], off sc1\n\ts_mov_b64 exec, -1\n\ts_nop 0"
      : [arr] "=&s"(arr), "+{v[156:159]}"(rq)
      : [cnt] "v"(cnt6), [one] "v"(one), [m48] "s"(m48), [m52] "s"(m52), [src] "v"(psrc), [dst] "v"(rdst)
      : CLOB_T, "scc", "memory");
  if (arr == 0 && isF) {
    const float2* zp = reinterpret_cast<const float2*>(lds + L_Z) + l40;
    const float2 z0 = zp[0], z1 = zp[40], z2 = zp[80], z3 = zp[120];
    gst16(zdst, fbits((z0.x + z1.x) + (z2.x + z3.x)), fbits((z0.y + z1.y) + (z2.y + z3.y)), tag);
  }
  if constexpr (!early) {
    if (PRI) __builtin_amdgcn_s_setprio(0);
    if (w == 7 && h < 5) {
      const int col = 80 * sp + 16 * h + 2 * c + (lane >> 5);
      const float xv = bf_lo(xs[col]);
      const u64* py = gw + GZ + (size_t)(sp * 5 + h) * YB + 32 * g + 2 * c + 392;
      bf16* po = out + col;
      float gt = 0.f;
      u64 m3 = 0ull, ms = 0ull;
      PIN(po);
#pragma unroll 1
      for (int pass = 0; pass < 2; ++pass) {
        lds_wait(lds, L_SY + (pass ? 1 : 6), pass ? 4u : 8u);
        STAMP(8);
        asm volatile("" : "+s"(m3), "+s"(ms));
        float t;
        unsigned n;
        asm volatile(
            "v_mov_b32 v124, 0\n\tv_mov_b32 v125, %[tag]\n\tv_mov_b32 v126, 0\n\tv_mov_b32 v127, %[tag]\n\t"
            "s_mov_b64 vcc, -1\n\ts_mov_b32 %[n], 0\n"
            ".Lfp%=:\n\t"
            "s_add_u32 %[n], %[n], 1\n\ts_cmp_gt_u32 %[n], %[max]\n\ts_cbranch_scc1 .Lfd%=\n\t"
            "s_mov_b64 exec, vcc\n\t"
            "global_load_dwordx4 v[112:115], %[p], off offset:-3136 sc1\n\t"
            "global_load_dwordx4 v[116:119], %[p], off offset:-1088 sc1\n\t"
            "global_load_dwordx4 v[120:123], %[p], off offset:1088 sc1\n\t"
            "s_and_b64 exec, vcc, %[m3]\n\t"
            "global_load_dwordx4 v[124:127], %[p], off offset:3136 sc1\n\t"
            "s_mov_b64 exec, -1\n\ts_waitcnt vmcnt(0)\n\t"
            "v_bitop3_b32 v128, v113, v115, %[tag] bitop3:0x7e\n\tv_bitop3_b32 v129, v117, v119, %[tag] bitop3:0x7e\n\t"
            "v_bitop3_b32 v130, v121, v123, %[tag] bitop3:0x7e\n\tv_bitop3_b32 v131, v125, v127, %[tag] bitop3:0x7e\n\t"
            "v_or3_b32 v128, v128, v129, v130\n\tv_add_f32 v132, v112, v116\n\tv_or_b32 v128, v128, v131\n\t"
            "v_add_f32 v134, v120, v124\n\tv_cmp_ne_u32 vcc, 0, v128\n\tv_add_f32 v133, v114, v118\n\t"
            "s_cbranch_vccnz .Lfp%=\n"
            ".Lfd%=:\n\t"
            "v_add_f32 v135, v122, v126\n\tv_add_f32 v132, v132, v134\n\tv_add_f32 v133, v133, v135\n\ts_nop 1\n\t"
            "v_permlane32_swap_b32 v132, v133\n\tv_add_f32 v134, v132, v133\n\tv_add_f32 v135, v132, v133\n\ts_nop 1\n\t"
            "v_permlane16_swap_b32 v134, v135\n\tv_add_f32 v134, v134, v135\n\ts_nop 1\n\t"
            "v_add_f32_dpp v134, v134, v134 row_ror:8 row_mask:0xf bank_mask:0xf\n\t"
            "v_cvt_pk_bf16_f32 v134, v134, v134\n\tv_lshlrev_b32 %[t], 16, v134\n\t"
            "v_mul_f32 v134, %[t], %[gt]\n\tv_cvt_pk_bf16_f32 v134, v134, v134\n\tv_lshlrev_b32 v134, 16, v134\n\t"
            "v_add_f32 v134, %[xv], v134\n\tv_cvt_pk_bf16_f32 v134, v134, v134\n\t"
            "s_mov_b64 exec, %[ms]\n\tglobal_store_short %[po], v134, off sc1\n\ts_mov_b64 exec, -1"
            : [t] "=&v"(t), [n] "=&s"(n)
            : [tag] "s"(tag), [max] "n"(SPIN_MAX), [p] "v"(py), [m3] "s"(m3), [ms] "s"(ms), [gt] "v"(gt), [xv] "v"(xv), [po] "v"(po)
            : CLOB_T, "vcc", "scc", "memory");
        if (pass) break;
        gt = bfr(silu(t));
        py -= GZ - GY;
        m3 = ~0ull;
        ms = 0x000000ff000000ffull;
      }
      if (b == 0 && lane == 0)
        __hip_atomic_store(reinterpret_cast<unsigned*>(gw + GEP), tag, __ATOMIC_RELAXED, __HIP_MEMORY_SCOPE_AGENT);
      STAMP(10);
    }
    STAMP_FLUSH();
    asm volatile(".Lcode_lastb:");
    return;
  }
  pace<T_LAST + W2T, 15>(clk, ph);
#pragma unroll
  for (int i = W1N; i < 6; ++i) WO[i] = __builtin_amdgcn_raw_buffer_load_b128(rwo, wooff, i * (4 * D * 2), 2);
  unsigned dm0 = __builtin_amdgcn_raw_buffer_load_b32(rwo, NOWHERE, 0, 2), dm1 = __builtin_amdgcn_raw_buffer_load_b32(rwo, NOWHERE, 4, 2);
  STAMP(3);
  SCHED_FENCE();
  const int ly = isF ? lane & 31 : lane;
  u64* ydst = gw + GY + (size_t)(4 * sp + (ly >> 3) + 4 * (ly >> 5)) * YB + 32 * h + 16 * (h >> 4) + 2 * (ly & 7);
  static_assert(RS * 8 * 4 == 4608, "record r4 + 4 is 4608 bytes behind record r4");
  const u64* pr = gw + G2 + (size_t)(h * NSP + r4 + 2) * RS + 32 * w + 2 * c16;
  const u64 ownm = 0xffffull << (16 * (sp & 3));
  u64 own0 = sp < 4 ? ownm : 0ull, own1 = sp < 4 ? 0ull : ownm, my = isF ? 0xffffffffull : ~0ull;
  u64 for0 = m52 & ~own0, for1 = m52 & ~own1;
  own0 &= m48;
  own1 &= m48;
  unsigned osrc = lds_addr(lds + L_PART + 48 * w + 4 * c16), ysrc = lds_addr(lds + L_Y + 2 * ly), cnt1 = lds_addr(lds + L_SY + 1);
  unsigned ydl = lds_addr(lds + L_Y + 128 * w + (isF ? 8 * c + g : 8 * c16 + 2 * r4));
  asm volatile("" : "+s"(own0), "+s"(own1), "+s"(for0), "+s"(for1), "+s"(my));
  PIN(ydst);
  PIN(pr);
  PIN(osrc);
  PIN(ysrc);
  PIN(cnt1);
  PIN(ydl);
  u32x8 WP[3], XP[3];
#pragma unroll
  for (int p = 0; p < 3; ++p)
#pragma unroll
    for (int k = 0; k < 4; ++k) {
      XP[p][2 * k] = __builtin_amdgcn_perm(X[2 * p + 1][k], X[2 * p][k], 0x05040100u);
      XP[p][2 * k + 1] = __builtin_amdgcn_perm(X[2 * p + 1][k], X[2 * p][k], 0x07060302u);
    }
  asm volatile("" : "+{v[64:71]}"(XP[0]), "+{v[72:79]}"(XP[1]), "+{v[80:87]}"(XP[2]));
  SCHED_FENCE();
  auto perm_wo = [&]() {
#pragma unroll
    for (int p = 0; p < 3; ++p)
#pragma unroll
      for (int k = 0; k < 4; ++k) {
        WP[p][2 * k] = __builtin_amdgcn_perm(WO[2 * p + 1][k], WO[2 * p][k], 0x05040100u);
        WP[p][2 * k + 1] = __builtin_amdgcn_perm(WO[2 * p + 1][k], WO[2 * p][k], 0x07060302u);
      }
    asm volatile("" : "+{v[88:95]}"(WP[0]), "+{v[96:103]}"(WP[1]), "+{v[104:111]}"(WP[2]));
    SCHED_FENCE();
    STAMP(1);
  };
  lds_wait(lds, L_SY + 6, 8u);
  if (PT_) pace<T_LAST + PT_, 15>(clk, 0);
  STAMP(4);
  if (PRI) __builtin_amdgcn_s_setprio(3);
  u32x4 ra0, ra1;
  asm volatile(
      "v_mov_b32 v148, %[cw]\n\tv_mov_b32 v149, %[tag]\n\tv_mov_b32 v151, %[tag]\n\t"
      "v_mov_b32 v152, %[cw]\n\tv_mov_b32 v153, %[tag]\n\tv_mov_b32 v155, %[tag]\n\t"
      "s_mov_b64 exec, %[for0]\n\tglobal_load_dwordx4 v[148:151], %[p], off offset:-2304 sc1\n\t"
      "s_mov_b64 exec, %[for1]\n\tglobal_load_dwordx4 v[152:155], %[p], off offset:2304 sc1\n\t"
      "s_mov_b64 exec, %[m48]\n\t" SUM8("%[src]") SUM8B SUM8C
      "s_mov_b64 exec, %[own0]\n\tv_cvt_pk_bf16_f32 v148, v116, v117\n\tv_cvt_pk_bf16_f32 v150, v118, v119\n\t"
      "s_mov_b64 exec, %[own1]\n\tv_cvt_pk_bf16_f32 v152, v116, v117\n\tv_cvt_pk_bf16_f32 v154, v118, v119\n\t"
      "s_mov_b64 exec, -1"
      : "=&{v[148:151]}"(ra0), "=&{v[152:155]}"(ra1)
      : [cw] "v"(cw), [tag] "s"(tag), [for0] "s"(for0), [for1] "s"(for1), [p] "v"(pr), [own0] "s"(own0), [own1] "s"(own1),
        [m48] "s"(m48), [src] "v"(osrc)
      : CLOB_T, "memory");
  SCHED_FENCE();
  perm_wo();
  unsigned np;
  asm volatile(
      "s_mov_b32 %[n], 0\n\t"
      "s_mov_b64 exec, -1\n\tv_mov_b64 v[160:161], 0\n\tv_mov_b64 v[162:163], 0\n\tv_mov_b64 v[164:165], 0\n\t"
      "v_mov_b64 v[166:167], 0\n\ts_branch .Lrc%=\n"
      ".Lrp%=:\n\t"
      "s_add_u32 %[n], %[n], 1\n\ts_cmp_gt_u32 %[n], %[max]\n\ts_cbranch_scc1 .Lrd%=\n\t"
      "v_cmp_ne_u32 vcc, 0, v112\n\tv_cmp_ne_u32 s[98:99], 0, v113\n\ts_sleep " STR(RSLEEP) "\n\t"
      "s_mov_b64 exec, vcc\n\tglobal_load_dwordx4 v[148:151], %[p], off offset:-2304 sc1\n\t"
      "s_mov_b64 exec, s[98:99]\n\tglobal_load_dwordx4 v[152:155], %[p], off offset:2304 sc1\n\t"
      "s_mov_b64 exec, -1\n"
      ".Lrc%=:\n\t"
      "s_waitcnt vmcnt(0)\n\t"
      "v_bitop3_b32 v112, v149, v151, %[tag] bitop3:0x7e\n\tv_bitop3_b32 v113, v153, v155, %[tag] bitop3:0x7e\n\t"
      "v_max_f32 v115, v148, v152\n\tv_or_b32 v114, v112, v113\n\tv_max_f32 v116, v148, v152\n\t"
      "v_cmp_ne_u32 vcc, 0, v114\n\tv_lshlrev_b32 v122, 16, v148\n\ts_cbranch_vccnz .Lrp%=\n"
      ".Lrd%=:\n\t"
      "v_permlane16_swap_b32 v115, v116\n\tv_max_f32 v117, v115, v116\n\tv_max_f32 v118, v115, v116\n\t"
      "v_and_b32 v123, 0xffff0000, v148\n\tv_lshlrev_b32 v124, 16, v150\n\t"
      "v_permlane32_swap_b32 v117, v118\n\tv_max_f32 v117, v117, v118\n\t"
      "v_sub_f32 v115, v148, v117\n\tv_sub_f32 v116, v152, v117\n\tv_exp_f32 v115, v115\n\tv_exp_f32 v116, v116\n\t"
      "v_and_b32 v125, 0xffff0000, v150\n\tv_lshlrev_b32 v126, 16, v152\n\t"
      "v_add_f32 v117, v115, v116\n\tv_add_f32 v118, v115, v116\n\t"
      "v_mul_f32_dpp v122, v115, v122" BC12 "v_mul_f32_dpp v123, v115, v123" BC12
      "v_permlane16_swap_b32 v117, v118\n\tv_add_f32 v119, v117, v118\n\tv_add_f32 v120, v117, v118\n\t"
      "v_mul_f32_dpp v124, v115, v124" BC12 "v_mul_f32_dpp v125, v115, v125" BC12
      "v_permlane32_swap_b32 v119, v120\n\tv_add_f32 v119, v119, v120\n\tv_lshlrev_b32 v128, 16, v154\n\t"
      "v_rcp_f32 v119, v119\n\tv_and_b32 v127, 0xffff0000, v152\n\tv_and_b32 v129, 0xffff0000, v154\n\t"
      "v_fmac_f32_dpp v122, v116, v126" BC12 "v_fmac_f32_dpp v124, v116, v128" BC12
      "v_fmac_f32_dpp v123, v116, v127" BC12 "v_fmac_f32_dpp v125, v116, v129" BC12
      "v_permlane32_swap_b32 v122, v124\n\tv_add_f32 v122, v122, v124\n\t"
      "v_permlane32_swap_b32 v123, v125\n\tv_add_f32 v123, v123, v125\n\t"
      "v_mov_b32_dpp v120, v119" BC12 "s_nop 0\n\t"
      "v_permlane16_swap_b32 v122, v123\n\tv_add_f32 v122, v122, v123\n\t"
      "v_mul_f32 v123, v122, v120\n\ts_cmp_lt_u32 %[sp], 6\n\t"
      "v_mul_f32_dpp v124, v122, v120 quad_perm:[1,0,3,2] row_mask:0xf bank_mask:0xf\n\t"
      "v_cvt_pk_bf16_f32 v124, v123, v124\n\t"
      "s_cbranch_scc1 .Lyf%=\n\ts_nop 0\n\t"
      DOT8(64, 65, 66, 67, 68, 69, 70, 71, 0) DOT8(72, 73, 74, 75, 76, 77, 78, 79, 2) DOT8(80, 81, 82, 83, 84, 85, 86, 87, 4)
      DOT8(88, 89, 90, 91, 92, 93, 94, 95, 6) DOT8(96, 97, 98, 99, 100, 101, 102, 103, 8)
      DOT8(104, 105, 106, 107, 108, 109, 110, 111, 10) YRED
      "v_pk_add_f32 v[160:161], v[160:161], v[162:163]\n\tds_write_b64 %[ydl], v[160:161]\n"
      ".Lys%=:\n\t"
      "s_mov_b64 exec, 1\n\tds_add_rtn_u32 v112, %[cnt], %[one]\n\ts_mov_b64 exec, -1\n\t"
      "ds_read2st64_b64 v[116:119], %[ysrc] offset0:0 offset1:1\n\tds_read2st64_b64 v[120:123], %[ysrc] offset0:2 offset1:3\n\t"
      "s_waitcnt lgkmcnt(2)\n\tv_readfirstlane_b32 s98, v112\n\ts_waitcnt lgkmcnt(1)\n\t"
      "v_pk_add_f32 v[116:117], v[116:117], v[118:119]\n\ts_cmp_eq_u32 s98, 3\n\ts_cselect_b64 exec, %[my], 0\n\t"
      "s_waitcnt lgkmcnt(0)\n\tv_pk_add_f32 v[120:121], v[120:121], v[122:123]\n\ts_nop 0\n\t"
      "v_add_f32 v156, v116, v120\n\tv_add_f32 v158, v117, v121\n\t"
      "global_store_dwordx4 %[dst], v[156:159], off sc1\n\ts_mov_b64 exec, -1\n\ts_branch .Lye%=\n"
      ".Lyf%=:\n\ts_nop 0\n\t"
      "v_mov_b32_dpp v125, v124 row_newbcast:0 row_mask:0xf bank_mask:0x3\n\t"
      "v_mov_b32_dpp v126, v124 row_newbcast:2 row_mask:0xf bank_mask:0x3\n\t"
      "v_mov_b32_dpp v127, v124 row_newbcast:4 row_mask:0xf bank_mask:0x3\n\t"
      "v_mov_b32_dpp v125, v124 row_newbcast:6 row_mask:0xf bank_mask:0xc\n\t"
      "v_mov_b32_dpp v126, v124 row_newbcast:8 row_mask:0xf bank_mask:0xc\n\t"
      "v_mov_b32_dpp v127, v124 row_newbcast:10 row_mask:0xf bank_mask:0xc\n\t"
      DOT8F(88, 89, 90, 91, 92, 93, 94, 95, 125) DOT8F(96, 97, 98, 99, 100, 101, 102, 103, 126)
      DOT8F(104, 105, 106, 107, 108, 109, 110, 111, 127) YRED
      "v_add_f32 v160, v160, v162\n\tv_add_f32 v161, v161, v163\n\ts_nop 0\n\t"
      "v_add_f32_dpp v160, v160, v160 row_ror:8 row_mask:0xf bank_mask:0x3\n\t"
      "v_add_f32_dpp v160, v161, v161 row_ror:8 row_mask:0xf bank_mask:0xc\n\t"
      "ds_write_b32 %[ydl], v160\n\ts_branch .Lys%=\n"
      ".Lye%=:"
      : [n] "=&s"(np), "+{v[156:159]}"(rq), "+{v[148:151]}"(ra0), "+{v[152:155]}"(ra1)
      : [tag] "s"(tag), [p] "v"(pr), [max] "n"(SPIN_MAX), [sp] "s"(sp), [ydl] "v"(ydl), [cnt] "v"(cnt1), [one] "v"(one),
        [ysrc] "v"(ysrc), [my] "s"(my), [dst] "v"(ydst), "{v[64:71]}"(XP[0]), "{v[72:79]}"(XP[1]), "{v[80:87]}"(XP[2]),
        "{v[88:95]}"(WP[0]), "{v[96:103]}"(WP[1]), "{v[104:111]}"(WP[2])
      : CLOB_T, "v160", "v161", "v162", "v163", "v164", "v165", "v166", "v167", "s98", "s99", "vcc", "scc", "memory");
  STAMP(9);
  asm volatile("" ::"v"(dm0), "v"(dm1));
  STAMP_FLUSH();
  asm volatile(".Lcode_last:");
}

__global__ void __launch_bounds__(NT, 2)
fused_decode(const bf16* __restrict__ wq, const bf16* __restrict__ kc, const bf16* __restrict__ w1, const bf16* __restrict__ w3,
             u64* __restrict__ gw, const bf16* __restrict__ x, const bf16* __restrict__ vc,
             const bf16* __restrict__ wo, const bf16* __restrict__ w2, bf16* __restrict__ out) {
  __shared__ __attribute__((aligned(16))) float lds[L_END];
  const int b = blockIdx.x;
  const int xcd = b & 7, jj = b >> 3;
  const int h = xcd * 4 + (jj >> 3), sp = jj & 7;
  const int w = __builtin_amdgcn_readfirstlane(threadIdx.x >> 6);
  unsigned cpf = threadIdx.x;
  {
    u64 pc;
    unsigned len;
    asm volatile(
        "s_cmp_lt_u32 %[w], 4\n\ts_cbranch_scc1 .Lnocpf\n\t"
        "s_getpc_b64 %[pc]\n.Lcode_here:\n\tv_xor_b32 %[c], %[hi], %[c]\n\tv_lshlrev_b32 %[c], 6, %[c]\n\t"
        "s_mov_b32 %[len], .Lcode_last-.Lcode_here-4\n\ts_max_i32 %[len], %[len], .Lcode_lastb-.Lcode_here-4\n\t"
        "v_min_u32 %[c], %[len], %[c]\n\tglobal_load_dword %[c], %[c], %[pc]\n.Lnocpf:"
        : [c] "+v"(cpf), [pc] "=&s"(pc), [len] "=&s"(len) : [w] "s"(w), [hi] "s"((b & 24) << 5) : "scc", "memory");
  }
  SCHED_FENCE();
  if (w >= 4)
    body<false>(lds, h, sp, w, x, kc, vc, wq, wo, w1, w3, w2, gw, out, cpf);
  else
    body<true>(lds, h, sp, w, x, kc, vc, wq, wo, w1, w3, w2, gw, out, cpf);
}

}

extern "C" void kernel_launch(void* const* d_in, const int* in_sizes, int n_in, void* d_out, int out_size, void* d_ws,
                              size_t ws_size, hipStream_t stream) {
  const bf16* x = (const bf16*)d_in[0];
  const bf16* kc = (const bf16*)d_in[1];
  const bf16* vc = (const bf16*)d_in[2];
  const bf16* wq = (const bf16*)d_in[3];
  const bf16* wo = (const bf16*)d_in[4];
  const bf16* w1 = (const bf16*)d_in[6];
  const bf16* w3 = (const bf16*)d_in[7];
  const bf16* w2 = (const bf16*)d_in[8];
  u64* gw = (u64*)d_ws;
  fused_decode<<<NWG, NT, 0, stream>>>(wq, kc, w1, w3, gw, x, vc, wo, w2, (bf16*)d_out);
}
```

```cpp
#include <hip/hip_runtime.h>
#include <hip/hip_bf16.h>
#include <stdint.h>

typedef __hip_bfloat16 bf16;
typedef unsigned long long u64;
typedef unsigned u32x4 __attribute__((ext_vector_type(4)));
typedef unsigned u32x8 __attribute__((ext_vector_type(8)));
typedef float f32x4 __attribute__((ext_vector_type(4)));
typedef float f32x2 __attribute__((ext_vector_type(2)));
typedef __bf16 bf16x2_t __attribute__((ext_vector_type(2)));

namespace {

constexpr int D = 640, HQ = 32, DH = 192, SK = 32768, WIN = 4096, FF = 1536;
constexpr int NSP = 8;
constexpr int NWG = HQ * NSP;
constexpr int NT = 512;
constexpr float SCALE = 0.07216878364870322f;
constexpr float LOG2E = 1.4426950408889634f;
constexpr unsigned SPIN_MAX = 1u << 16;
constexpr int SC1 = 16;
constexpr int NOWHERE = 1 << 30;

#define PV_ 81
#define PK_ 82
#define PX_ PK_
#define TS_ 358
#define SB_ 246
#define LOC 30
#define WT_ 98
#define W2T 132
#define W1N 3
#define PT_ 215
#define NFL 7
#define CE 7
#define CL 8
#define AR 3
#define GR 5
#define GRW 2
#define RSLEEP 1
#define MW 4
#define PM 0x1440
#define MEAS 1
#define ATHR 20
#define ASH 4
#define AGX 7
#define UTHR 8
#define UTH2 40
#define USH 4
#define UGX 9
#define ULIM 20
#define UMISS 60
#define PVL 1
#define LSP 16
#define GLP 1
#define E1P 1
#define UTA 3
#define USH2 3
#define CT 10
#define CPM 8
#define XS 6
#define LOCO 60
#define HROT 16
constexpr int t_round(int r) { return r <= 2 ? TS_ : t_round(r - 1) + (r > 8 ? PV_ : r == 8 ? PX_ : PK_); }
constexpr int T_LAST = t_round(15);
constexpr int t_late(int i) { return t_round(i); }
constexpr int t_early(int i) { return t_round(i); }

constexpr int QS = 144;
constexpr int YB = 2 * (16 * 32 + 16);
constexpr int RS = 144;
constexpr size_t GQ = 0;
constexpr size_t G2 = GQ + (size_t)NWG * QS;
constexpr size_t GY = G2 + (size_t)NWG * RS;
constexpr size_t GZ = GY + 16;
constexpr size_t GEP = GY + (size_t)NSP * 5 * YB;
constexpr size_t GL = GEP + 16;

__device__ __forceinline__ unsigned fbits(float x) { return __builtin_bit_cast(unsigned, x); }
__device__ __forceinline__ u64 gld(const u64* p) {
  return __hip_atomic_load(p, __ATOMIC_RELAXED, __HIP_MEMORY_SCOPE_AGENT);
}
__device__ __forceinline__ float bf_lo(unsigned u) { return __builtin_bit_cast(float, u << 16); }
__device__ __forceinline__ float bf_hi(unsigned u) { return __builtin_bit_cast(float, u & 0xffff0000u); }
__device__ __forceinline__ float bfr(float x) { return __bfloat162float(__float2bfloat16(x)); }
__device__ __forceinline__ unsigned short bfbits(float x) {
  return __builtin_bit_cast(unsigned short, __float2bfloat16(x));
}
__device__ __forceinline__ float dot2(unsigned a, unsigned b, float c) {
  return __builtin_amdgcn_fdot2_f32_bf16(__builtin_bit_cast(bf16x2_t, a), __builtin_bit_cast(bf16x2_t, b), c, false);
}
template <int CTRL>
__device__ __forceinline__ float dpp(float x) {
  return __builtin_bit_cast(float, __builtin_amdgcn_mov_dpp(__builtin_bit_cast(int, x), CTRL, 0xf, 0xf, true));
}
__device__ __forceinline__ float grp8_sum(float x) {
  x += dpp<0xB1>(x);
  x += dpp<0x4E>(x);
  x += dpp<0x141>(x);
  return x;
}
__device__ __forceinline__ float sw16(float a, float b) {
  auto s = __builtin_amdgcn_permlane16_swap(fbits(a), fbits(b), false, false);
  return __builtin_bit_cast(float, (unsigned)s[0]) + __builtin_bit_cast(float, (unsigned)s[1]);
}
__device__ __forceinline__ float sw32(float a, float b) {
  auto s = __builtin_amdgcn_permlane32_swap(fbits(a), fbits(b), false, false);
  return __builtin_bit_cast(float, (unsigned)s[0]) + __builtin_bit_cast(float, (unsigned)s[1]);
}
__device__ __forceinline__ float xg_sum(float x) {
  x += dpp<0x128>(x);
  x = sw16(x, x);
  return sw32(x, x);
}
__device__ __forceinline__ float xg_max(float x) {
  x = fmaxf(x, dpp<0x128>(x));
  auto s = __builtin_amdgcn_permlane16_swap(fbits(x), fbits(x), false, false);
  x = fmaxf(__builtin_bit_cast(float, (unsigned)s[0]), __builtin_bit_cast(float, (unsigned)s[1]));
  auto t = __builtin_amdgcn_permlane32_swap(fbits(x), fbits(x), false, false);
  return fmaxf(__builtin_bit_cast(float, (unsigned)t[0]), __builtin_bit_cast(float, (unsigned)t[1]));
}
__device__ __forceinline__ float wave_sum(float x) { return xg_sum(grp8_sum(x)); }
__device__ __forceinline__ float ex2(float x) { return __builtin_amdgcn_exp2f(x); }
__device__ __forceinline__ float silu(float x) { return x / (1.0f + __expf(-x)); }
__device__ __forceinline__ float xg_reduce8(const float (&a)[8], bool g0) {
  float r[4];
#pragma unroll
  for (int i = 0; i < 4; ++i) r[i] = sw32(a[i], a[i + 4]);
  const float s0 = sw16(r[0], r[2]), s1 = sw16(r[1], r[3]);
  const float keep = g0 ? s1 : s0, send = g0 ? s0 : s1;
  return keep + dpp<0x128>(send);
}
__device__ __forceinline__ __amdgpu_buffer_rsrc_t rsrc(const void* p, unsigned bytes) {
  return __builtin_amdgcn_make_buffer_rsrc(const_cast<void*>(p), 0, bytes, 0x00020000);
}
__device__ __forceinline__ void gst16(u64* p, unsigned v0, unsigned v1, unsigned tag) {
  const u32x4 o = {v0, tag, v1, tag};
  asm volatile("global_store_dwordx4 %0, %1, off sc1\n\ts_nop 1" ::"v"(p), "v"(o) : "memory");
}
__device__ __forceinline__ unsigned bfpack(float a, float b) {
  return __builtin_bit_cast(unsigned, __builtin_convertvector(f32x2{a, b}, bf16x2_t));
}
__device__ __forceinline__ float2 rows_reduce(const float (&v)[8]) {
  float r[4];
#pragma unroll
  for (int i = 0; i < 4; ++i) r[i] = sw32(v[i], v[i + 4]);
  return float2{sw16(r[0], r[2]), sw16(r[1], r[3])};
}
template <int OFF>
__device__ __forceinline__ void xg_store(const float (&v)[8], float f, unsigned a) {
  float p[4], q[4], t, u;
#pragma unroll
  for (int i = 0; i < 4; ++i) {
    auto s = __builtin_amdgcn_permlane32_swap(fbits(v[i]), fbits(v[i + 4]), false, false);
    p[i] = __builtin_bit_cast(float, (unsigned)s[0]);
    q[i] = __builtin_bit_cast(float, (unsigned)s[1]);
  }
  const f32x2 r01 = f32x2{p[0], p[1]} + f32x2{q[0], q[1]}, r23 = f32x2{p[2], p[3]} + f32x2{q[2], q[3]};
  auto s0 = __builtin_amdgcn_permlane16_swap(fbits(r01[0]), fbits(r23[0]), false, false);
  auto s1 = __builtin_amdgcn_permlane16_swap(fbits(r01[1]), fbits(r23[1]), false, false);
  asm volatile("v_add_f32 %0, %2, %3\n\tv_add_f32 %1, %4, %5\n\ts_nop 0\n\t"
               "v_add_f32_dpp %0, %0, %0 row_ror:8 row_mask:0xf bank_mask:0x3\n\t"
               "v_add_f32_dpp %0, %1, %1 row_ror:8 row_mask:0xf bank_mask:0xc\n\t"
               "v_mul_f32 %0, %6, %0\n\tds_write_b32 %7, %0 offset:%8"
               : "=&v"(t), "=&v"(u)
               : "v"((unsigned)s0[0]), "v"((unsigned)s0[1]), "v"((unsigned)s1[0]), "v"((unsigned)s1[1]), "v"(f), "v"(a), "n"(OFF)
               : "memory");
}
#define SUM8(a)                                                                                                          \
  "ds_read_b128 v[116:119], " a "\n\tds_read_b128 v[120:123], " a " offset:768\n\t"                                      \
  "ds_read_b128 v[124:127], " a " offset:1536\n\tds_read_b128 v[128:131], " a " offset:2304\n\t"                         \
  "ds_read_b128 v[132:135], " a " offset:3072\n\tds_read_b128 v[136:139], " a " offset:3840\n\t"                         \
  "ds_read_b128 v[140:143], " a " offset:4608\n\tds_read_b128 v[144:147], " a " offset:5376\n\ts_waitcnt lgkmcnt(6)\n\t"
#define SUM8B                                                                                                            \
  "v_pk_add_f32 v[116:117], v[116:117], v[120:121]\n\tv_pk_add_f32 v[118:119], v[118:119], v[122:123]\n\t"               \
  "s_waitcnt lgkmcnt(4)\n\t"                                                                                             \
  "v_pk_add_f32 v[124:125], v[124:125], v[128:129]\n\tv_pk_add_f32 v[126:127], v[126:127], v[130:131]\n\t"               \
  "v_pk_add_f32 v[116:117], v[116:117], v[124:125]\n\tv_pk_add_f32 v[118:119], v[118:119], v[126:127]\n\t"
#define SUM8C                                                                                                            \
  "s_waitcnt lgkmcnt(2)\n\t"                                                                                             \
  "v_pk_add_f32 v[132:133], v[132:133], v[136:137]\n\tv_pk_add_f32 v[134:135], v[134:135], v[138:139]\n\t"               \
  "s_waitcnt lgkmcnt(0)\n\t"                                                                                             \
  "v_pk_add_f32 v[140:141], v[140:141], v[144:145]\n\tv_pk_add_f32 v[142:143], v[142:143], v[146:147]\n\t"               \
  "v_pk_add_f32 v[132:133], v[132:133], v[140:141]\n\tv_pk_add_f32 v[134:135], v[134:135], v[142:143]\n\t"               \
  "v_pk_add_f32 v[116:117], v[116:117], v[132:133]\n\tv_pk_add_f32 v[118:119], v[118:119], v[134:135]\n\t"
#define BC12 " row_newbcast:12 row_mask:0xf bank_mask:0xf\n\t"
#define DOTB(y, a, n) "v_dot2c_f32_bf16_dpp v" #y ", v124, v" #a " row_newbcast:" #n " row_mask:0xf bank_mask:0xf\n\t"
#define DOT8(a, b, c, d, e, f, g, h, n) \
  DOTB(160, a, n) DOTB(161, b, n) DOTB(162, c, n) DOTB(163, d, n) DOTB(164, e, n) DOTB(165, f, n) DOTB(166, g, n) DOTB(167, h, n)
#define DOTF(y, a, n) "v_dot2c_f32_bf16 v" #y ", v" #n ", v" #a "\n\t"
#define DOT8F(a, b, c, d, e, f, g, h, n) \
  DOTF(160, a, n) DOTF(161, b, n) DOTF(162, c, n) DOTF(163, d, n) DOTF(164, e, n) DOTF(165, f, n) DOTF(166, g, n) DOTF(167, h, n)
#define YRED                                                                                                             \
  "v_permlane32_swap_b32 v160, v164\n\tv_permlane32_swap_b32 v161, v165\n\tv_permlane32_swap_b32 v162, v166\n\t"         \
  "v_permlane32_swap_b32 v163, v167\n\tv_pk_add_f32 v[160:161], v[160:161], v[164:165]\n\t"                              \
  "v_pk_add_f32 v[162:163], v[162:163], v[166:167]\n\ts_nop 1\n\t"                                                       \
  "v_permlane16_swap_b32 v160, v162\n\tv_permlane16_swap_b32 v161, v163\n\t"
#define CLOB_T                                                                                                     \
  "v112", "v113", "v114", "v115", "v116", "v117", "v118", "v119", "v120", "v121", "v122", "v123", "v124", "v125",        \
      "v126", "v127", "v128", "v129", "v130", "v131", "v132", "v133", "v134", "v135", "v136", "v137", "v138", "v139",    \
      "v140", "v141", "v142", "v143", "v144", "v145", "v146", "v147"
#define SCHED_FENCE() __builtin_amdgcn_sched_barrier(0)
#define STR2(x) #x
#define STR(x) STR2(x)
#define THRN(n) do { SCHED_FENCE(); asm volatile("s_waitcnt vmcnt(%0)" ::"n"(n) : "memory"); SCHED_FENCE(); } while (0)
#define NOWT() ((unsigned)__builtin_amdgcn_s_memrealtime())
template <int N>
struct IC {
  static constexpr int v = N;
};
template <int I, int E, class F>
__device__ __forceinline__ void sfor(F&& f) {
  if constexpr (I < E) {
    f(IC<I>{});
    sfor<I + 1, E>(f);
  }
}
#define LDS_DRAIN() asm volatile("s_waitcnt lgkmcnt(0)" ::: "memory")
__device__ __forceinline__ unsigned lds_add(float* lds, int idx) {
  return __hip_atomic_fetch_add(reinterpret_cast<unsigned*>(lds + idx), 1u, __ATOMIC_RELAXED, __HIP_MEMORY_SCOPE_WORKGROUP);
}
__device__ __forceinline__ void lds_wait(float* lds, int idx, unsigned n) {
  SCHED_FENCE();
#pragma unroll 1
  for (unsigned spin = 0; spin < (SPIN_MAX << 4); ++spin) {
    if (__hip_atomic_load(reinterpret_cast<unsigned*>(lds + idx), __ATOMIC_RELAXED, __HIP_MEMORY_SCOPE_WORKGROUP) >= n) break;
    __builtin_amdgcn_s_sleep(1);
  }
  asm volatile("" ::: "memory");
  SCHED_FENCE();
}
template <int T, int R>
__device__ __forceinline__ void pace(unsigned clk, int ph) {
  unsigned o;
  SCHED_FENCE();
  asm volatile(
      "s_branch .Lpb%=\n"
      ".Lpa%=:\n\ts_sleep 1\n"
      ".Lpb%=:\n\tds_read_b32 %0, %1 offset:%4\n\ts_memrealtime s[98:99]\n\ts_waitcnt lgkmcnt(0)\n\tv_readfirstlane_b32 s99, %0\n\t"
      "s_sub_u32 s98, s98, %2\n\ts_sub_u32 s98, s98, s99\n\ts_cmp_lt_i32 s98, %3\n\ts_cbranch_scc1 .Lpa%="
      : "=&v"(o) : "v"(clk), "s"(ph), "n"(T), "n"(4 * R) : "s98", "s99", "scc", "memory");
  SCHED_FENCE();
}

template <int T, int R>
__device__ __forceinline__ unsigned pace_m(unsigned clk, int ph) {
  unsigned o, tl;
  SCHED_FENCE();
  asm volatile(
      "s_mov_b32 %1, 0\n\ts_branch .Lpb%=\n"
      ".Lpa%=:\n\ts_sleep 1\n"
      ".Lpb%=:\n\tds_read_b32 %0, %2 offset:%5\n\ts_getreg_b32 s97, hwreg(HW_REG_IB_STS, 0, 4)\n\ts_memrealtime s[98:99]\n\t"
      "s_or_b32 s97, s97, %1\n\ts_waitcnt lgkmcnt(0)\n\ts_cselect_b32 %1, %1, s98\n\tv_readfirstlane_b32 s99, %0\n\t"
      "s_sub_u32 s98, s98, %3\n\ts_sub_u32 s98, s98, s99\n\ts_cmp_lt_i32 s98, %4\n\ts_cbranch_scc1 .Lpa%="
      : "=&v"(o), "=&s"(tl) : "v"(clk), "s"(ph), "n"(T), "n"(4 * R) : "s97", "s98", "s99", "scc", "memory");
  SCHED_FENCE();
  return tl;
}

#define STAMP(i)
#define STAMP_FLUSH()
#define TT(i)
constexpr int L_QP = 0;
constexpr int L_QS = L_QP + 4 * 128;
constexpr int L_PART = L_QS + 96;
constexpr int L_ML = L_PART + 8 * DH;
constexpr int L_Z = L_ML + 16;
constexpr int L_Y = L_Z + 320;
constexpr int L_H = L_Y + 512;
constexpr int L_SY = L_H + 64;
constexpr int L_CK = L_SY + 8;
constexpr int L_END = L_CK + 16;

#define PIN(x) asm volatile("" : "+v"(x))

__device__ __forceinline__ void wg_ml(const float* lds, float& M, float& L) {
  float mm[8], ll[8];
#pragma unroll
  for (int i = 0; i < 4; ++i) {
    const f32x4 t = *reinterpret_cast<const f32x4*>(lds + L_ML + 4 * i);
    mm[2 * i] = t[0];
    ll[2 * i] = t[1];
    mm[2 * i + 1] = t[2];
    ll[2 * i + 1] = t[3];
  }
  M = mm[0];
#pragma unroll
  for (int ww = 1; ww < 8; ++ww) M = fmaxf(M, mm[ww]);
  L = 0.f;
#pragma unroll
  for (int ww = 0; ww < 8; ++ww) L = fmaf(ex2((mm[ww] - M) * LOG2E), ll[ww], L);
}
__device__ __forceinline__ unsigned lds_addr(const float* p) {
  return (unsigned)(size_t)(const __attribute__((address_space(3))) float*)p;
}

template <bool early>
__device__ __forceinline__ void body(float* lds, const int h, const int sp, const int w, const bf16* __restrict__ x,
                                     const bf16* __restrict__ kc, const bf16* __restrict__ vc,
                                     const bf16* __restrict__ wq, const bf16* __restrict__ wo,
                                     const bf16* __restrict__ w1, const bf16* __restrict__ w3,
                                     const bf16* __restrict__ w2, u64* __restrict__ gw, bf16* __restrict__ out,
                                     unsigned cpf) {
  const int b = blockIdx.x;
  const int tid = threadIdx.x, lane = tid & 63;
  const int g = lane >> 3, c = lane & 7;
  const int rec = h * NSP + sp;

  STAMP(0);
  const bool isF = sp < 6;
  u32x4 PR[8], xa, xb, xq;
  unsigned epoch = 0;
  u64 tk = 0;
  int ra = 0, RA0 = 0;
  bool va = false;
  if constexpr (early) {
    int s0_ = 240, s1_ = 80, s2_ = 240 * (sp & 1), s3_ = 80 * sp, s4_ = 64 * (sp >> 1), s5_ = 64 * w, s6_ = 24 * w;
    asm("" : "+s"(s0_), "+s"(s1_), "+s"(s2_), "+s"(s3_), "+s"(s4_), "+s"(s5_), "+s"(s6_));
    const int rw0 = isF ? s5_ : s6_;
    int lim = (isF ? s0_ : s1_) - rw0, lim2 = isF ? 64 : 24;
    asm("" : "+s"(lim), "+s"(lim2));
    lim = lim < lim2 ? lim : lim2;
    ra = rw0 + 8 * g;
    va = 8 * g < lim;
    RA0 = isF ? s2_ : s3_;
    int aoff = (int)__umul24(g, 8 * HQ * DH * 2) + 16 * c;
    PIN(aoff);
    aoff = va ? aoff : NOWHERE;
    int sa = ((RA0 + rw0) * (HQ * DH) + h * DH + (isF ? s4_ : 64)) * 2;
    PIN(aoff);
    asm volatile("" : "+s"(sa));
    SCHED_FENCE();
    const __amdgpu_buffer_rsrc_t rw = rsrc(wq, NOWHERE);
    PR[0] = __builtin_amdgcn_raw_buffer_load_b128(rw, aoff, sa, 2);
    SCHED_FENCE();
    tk = __builtin_amdgcn_s_memrealtime();
    SCHED_FENCE();
    asm volatile("s_load_dword %0, %1, %2" : "=&s"(epoch) : "s"(gw), "s"(E1P && w == 1 ? (int)GL * 8 + (sp * 8 + (b & 7)) * LSP : (int)GEP * 8) : "memory");
    SCHED_FENCE();
#pragma unroll
    for (int i = 1; i < 8; ++i) PR[i] = __builtin_amdgcn_raw_buffer_load_b128(rw, aoff, sa + i * (HQ * DH * 2), 2);
    SCHED_FENCE();
    SCHED_FENCE();
  }
  const unsigned short* xs = reinterpret_cast<const unsigned short*>(x);
  const size_t wgb = ((size_t)h * SK + (SK - WIN)) * DH;
  const __amdgpu_buffer_rsrc_t rk = rsrc(kc + wgb, WIN * DH * 2);
  const __amdgpu_buffer_rsrc_t rv = rsrc(vc + wgb, WIN * DH * 2);
  const int voff = (g * DH + 8 * c) * 2;
  auto tile = [&](int t) { return (t * 64 + h * HROT + w * 8 + sp) & 511; };
  u32x4 K[8][3], V[8][3];
  auto kld = [&](int t, int j) { K[t][j] = __builtin_amdgcn_raw_buffer_load_b128(rk, voff + 128 * j, tile(t) * 3072, 2); };
  auto vld = [&](int t, int j) { V[t][j] = __builtin_amdgcn_raw_buffer_load_b128(rv, voff + 128 * j, tile(t) * 3072, 2); };
  unsigned tag = 0;
  const unsigned clk = lds_addr(lds + L_CK);
  const int ph = (w * PV_) >> 3;
  const int r4 = lane >> 4, c16 = lane & 15;
  const int ch = c16 < 10 ? c16 : 9;
  const int fi = (h & 3) * 6 + (isF ? sp : 5), gl = fi >> 3, gI = (h >> 2) * 3 + gl, fr = fi & 7;
  u32x4 WO[6], X[6], W2[4];
  unsigned tch = 0;
  const int wooff = ((48 * w + r4 + (isF ? 24 * (g & 1) : 24)) * D + 64 * sp + 8 * c + (isF ? 0 : 128 * (g & 1))) * 2;
  typedef unsigned u32x2 __attribute__((ext_vector_type(2)));
  const __amdgpu_buffer_rsrc_t rl = rsrc(gw + GL, 16 * 64 * LSP);
  const bool pub = !((sp + (b >> 6)) & 3);
  const int lw_ = lane == 0 && pub ? (sp * 8 + (b & 7)) * LSP : NOWHERE;
  u32x2 lat = {0u, 0u};
  u64 own = 0, pre = 0;
  unsigned pro = 0, st1 = 0, old = 0;
  bool vslow = false, kslow = false;
  if constexpr (early) {
    u32x4 PF[5];
    const int F0 = (isF ? 160 * (fr >> 1) : 480) + 40 * w;
    const int fs = isF ? FF : HQ * DH;
    int s6_ = 64 * gI, s7_ = h * DH + 128 * (sp & 1);
    asm("" : "+s"(s6_), "+s"(s7_));
    const int boff = ((int)__umul24(g, fs) + 8 * c) * 2;
    const int sb = (F0 * fs + (isF ? s6_ : s7_)) * 2;
    const bf16* pf_ = (fr & 1) ? w3 : w1;
    asm("" : "+s"(pf_));
    const __amdgpu_buffer_rsrc_t rf = rsrc(isF ? pf_ : wq, NOWHERE);
    unsigned short xr[5];
#pragma unroll
    for (int i = 0; i < 5; ++i) PF[i] = __builtin_amdgcn_raw_buffer_load_b128(rf, boff, sb + i * 16 * fs, 2);
    SCHED_FENCE();
    if (!isF) {
#pragma unroll
      for (int i = 0; i < XS; ++i)
        X[i] = __builtin_amdgcn_raw_buffer_load_b128(rsrc(wo + (size_t)h * DH * D, DH * D * 2), wooff - 24 * D * 2 + i * (4 * D * 2), 0, 2);
    } else {
#pragma unroll
      for (int i = 0; i < XS; ++i) X[i] = u32x4{0u, 0u, 0u, 0u};
    }
    SCHED_FENCE();
    xa = *reinterpret_cast<const u32x4*>(x + 8 * lane);
    xb = *reinterpret_cast<const u32x4*>(x + 512 + 8 * (lane & 15));
    xq = *reinterpret_cast<const u32x4*>(x + RA0 + (va ? ra : 0));
#pragma unroll
    for (int i = 0; i < 5; ++i) xr[i] = xs[F0 + 8 * i + g];
    SCHED_FENCE();
    const unsigned epv = __hip_atomic_load(reinterpret_cast<unsigned*>(gw + GEP), __ATOMIC_RELAXED, __HIP_MEMORY_SCOPE_AGENT);
    SCHED_FENCE();
    asm volatile("" : "+s"(tk));
    if (tid < 16) reinterpret_cast<unsigned*>(lds)[L_CK + tid] = (unsigned)tk;
    asm volatile("s_waitcnt lgkmcnt(0)\n\ts_barrier" : "+s"(epoch)::"memory");
    STAMP(6);
    SCHED_FENCE();
#pragma unroll
    for (int t = 0; t < 3; ++t)
#pragma unroll
      for (int j = 0; j < 3; ++j) kld(t, j);
    SCHED_FENCE();
    float mb, den;
    {
      float xf[8], xg[8];
#pragma unroll
      for (int i = 0; i < 4; ++i) {
        xf[2 * i] = bf_lo(xa[i]);
        xf[2 * i + 1] = bf_hi(xa[i]);
        xg[2 * i] = bf_lo(xb[i]);
        xg[2 * i + 1] = bf_hi(xb[i]);
      }
      const bool lo16 = lane < 16;
      float sum = 0.f;
#pragma unroll
      for (int i = 0; i < 8; ++i) sum += xf[i] + (lo16 ? xg[i] : 0.f);
      sum = wave_sum(sum);
      const float mean = sum * (1.0f / D);
      float sq = 0.f;
#pragma unroll
      for (int i = 0; i < 8; ++i) {
        const float d1 = xf[i] - mean, d2 = xg[i] - mean;
        sq += d1 * d1 + (lo16 ? d2 * d2 : 0.f);
      }
      sq = wave_sum(sq);
      mb = bfr(mean);
      const float vb = bfr(sq * (1.0f / D));
      den = bfr(sqrtf(bfr(vb + 1e-5f)));
    }
    {
      unsigned xn[13];
#pragma unroll
      for (int i = 0; i < 4; ++i) {
        xn[2 * i] = fbits(bfr(bfr(bf_lo(xq[i]) - mb) / den));
        xn[2 * i + 1] = fbits(bfr(bfr(bf_hi(xq[i]) - mb) / den));
      }
#pragma unroll
      for (int i = 0; i < 5; ++i) xn[8 + i] = fbits(bfr(bfr(bf_lo(xr[i]) - mb) / den));
#pragma unroll
      for (int i = 0; i < 13; ++i) PIN(xn[i]);
      SCHED_FENCE();
      pace<t_round(2), 2>(clk, ph);
#pragma unroll
      for (int j = 0; j < 3; ++j) kld(3, j);
      TT(2);
      SCHED_FENCE();
      float fa[8], fg[8];
#pragma unroll
      for (int k = 0; k < 8; ++k) fa[k] = fg[k] = 0.f;
#pragma unroll
      for (int i = 0; i < 8; ++i)
#pragma unroll
        for (int k = 0; k < 4; ++k) {
          fa[2 * k] = dot2(PR[i][k], xn[i] >> 16, fa[2 * k]);
          fa[2 * k + 1] = dot2(PR[i][k], xn[i], fa[2 * k + 1]);
        }
      lds[L_QP + w * 128 + 8 * c + g] = xg_reduce8(fa, g & 1);
#pragma unroll
      for (int i = 0; i < 5; ++i)
#pragma unroll
        for (int k = 0; k < 4; ++k) {
          fg[2 * k] = dot2(PF[i][k], xn[8 + i] >> 16, fg[2 * k]);
          fg[2 * k + 1] = dot2(PF[i][k], xn[8 + i], fg[2 * k + 1]);
        }
      lds[L_QP + w * 128 + 64 + 8 * c + g] = xg_reduce8(fg, g & 1);
    }
    SCHED_FENCE();
    tag = __builtin_amdgcn_readfirstlane(epv) + 1u;
    if (tid == 0) reinterpret_cast<unsigned*>(lds)[L_SY + 3] = tag;
    LDS_DRAIN();
    if (lane == 0) old = lds_add(lds, L_SY);
    old = __builtin_amdgcn_readfirstlane(old);
    if (old == 3) {
      const float2* qp = reinterpret_cast<const float2*>(lds + L_QP) + lane;
      const float2 p0_ = qp[0], p1_ = qp[64], p2_ = qp[128], p3_ = qp[192];
      gst16(gw + GQ + (size_t)rec * QS + 2 * lane, fbits((p0_.x + p1_.x) + (p2_.x + p3_.x)),
            fbits((p0_.y + p1_.y) + (p2_.y + p3_.y)), tag);
    }
    STAMP(11);
  } else {
    if (tid >= NT - 8) reinterpret_cast<unsigned*>(lds)[L_SY + tid - (NT - 8)] = 0u;
    unsigned st0 = 0;
    {
      const bf16* tp = w == 4 ? kc + wgb : w == 5 ? (!isF ? wo + (size_t)h * DH * D : (fr & 1) ? w3 : w1) : w == 6 ? ((sp & 1) ? x : wq) : vc + wgb;
      asm volatile("s_waitcnt lgkmcnt(0)\n\ts_load_dword %0, %1, 0x0" : "=&s"(st0) : "s"(tp) : "memory");
    }
    SCHED_FENCE();
#pragma unroll
    for (int j = 0; j < 3; ++j) kld(0, j);
    SCHED_FENCE();
    asm volatile("s_barrier" ::: "memory");
    STAMP(6);
    SCHED_FENCE();
    if (w < 6) {
      const int l = lane < 42 ? lane : 41, k = l & 15;
      const int yg = 4 * (k < 8 || isF ? sp : sp + 2) + ((k >> 1) & 3);
      const size_t yo = l < 32 || !isF ? GY + (size_t)yg * YB + 32 * h + 16 * (h >> 4) + 8 * (k & 1)
                                       : GZ + (size_t)(fr * 5 + (k >> 1)) * YB + 32 * gI + 16 * (gI >> 4) + 8 * (k & 1);
      unsigned o4 = (unsigned)(l < 16 ? G2 + (size_t)rec * RS + 8 * l : yo);
      unsigned o5 = (unsigned)(GQ + (size_t)rec * QS + 8 * (lane < 16 ? lane : 15));
      if (GLP && pub && lane >= 16 && lane < 32 && ((PM >> (lane - 16)) & 1))
        o5 = (unsigned)GL + (unsigned)(((lane - 16) * 64 + sp * 8 + (b & 7)) * (LSP / 8));
      PIN(o4);
      PIN(o5);
      pre = gld(gw + (w == 4 ? o4 : o5));
    }
    if (w == 7 && h < 5)
      pro = __hip_atomic_load(reinterpret_cast<const unsigned*>(out + 80 * sp + 16 * h) + (lane & 7), __ATOMIC_RELAXED,
                              __HIP_MEMORY_SCOPE_AGENT);
    SCHED_FENCE();
    asm volatile("s_waitcnt lgkmcnt(0)" ::"s"(st0) : "memory");
#pragma unroll
    for (int j = 0; j < 3; ++j) {
      kld(1, j);
      THRN(5);
    }
    THRN(3);
    TT(1);
    own = __builtin_amdgcn_s_memrealtime() - SB_;
    __builtin_amdgcn_raw_buffer_store_b64(u32x2{(unsigned)own, (unsigned)(own >> 32)}, rl, w == 6 ? lw_ : NOWHERE, 0, SC1);
    SCHED_FENCE();
    if (LOC && w == 6 && lane < 16) {
      const unsigned o = (unsigned)own - (isF ? LOC : LOCO);
      if ((int)(o - reinterpret_cast<unsigned*>(lds)[L_CK + lane]) > 0) reinterpret_cast<unsigned*>(lds)[L_CK + lane] = o;
    }
    SCHED_FENCE();
    {
      const bf16* tp = (w & 1) ? wo + (size_t)h * DH * D : w2 + (size_t)gI * 64 * D;
      asm volatile("s_load_dword %0, %1, 0x0" : "=&s"(st1) : "s"(tp) : "memory");
    }
    SCHED_FENCE();
  }
  u32x4 qf[3];
  float sc[8], mw, lw, acc[24];
  unsigned pb[8];
  auto score = [&](int t) {
    float d = 0.f;
#pragma unroll
    for (int j = 0; j < 3; ++j)
#pragma unroll
      for (int i = 0; i < 4; ++i) d = dot2(K[t][j][i], qf[j][i], d);
    d = grp8_sum(d);
    sc[t] = bfr(bfr(d) * SCALE);
  };
  auto pv = [&](int t) {
    const unsigned plo = pb[t], phi = plo << 16;
#pragma unroll
    for (int j = 0; j < 3; ++j)
#pragma unroll
      for (int i = 0; i < 4; ++i) {
        acc[8 * j + 2 * i] = dot2(V[t][j][i], plo, t ? acc[8 * j + 2 * i] : 0.f);
        acc[8 * j + 2 * i + 1] = dot2(V[t][j][i], phi, t ? acc[8 * j + 2 * i + 1] : 0.f);
        PIN(acc[8 * j + 2 * i]);
        PIN(acc[8 * j + 2 * i + 1]);
      }
  };
  const bool hf = lane >> 5;
  const __amdgpu_buffer_rsrc_t rq_ = rsrc(gw + GQ, NWG * QS * 8);
  int go[9];
  {
    const int l16 = (lane & 31) * 16;
    const int qa = l16 + (8 * h + (hf ? 4 : 0)) * (QS * 8);
    go[0] = qa;
    go[1] = qa + QS * 8;
    go[2] = l16 + (8 * h + (hf ? 7 : 6)) * (QS * 8) + 512;
    go[3] = qa + 2 * QS * 8;
    go[4] = qa + 3 * QS * 8;
#pragma unroll
    for (int k = 0; k < 4; ++k) {
      const int m = 8 * gl + 2 * k + (hf ? 1 : 0);
      go[5 + k] = isF ? l16 + (32 * (h >> 2) + m + (((m * 43) >> 7) & 6)) * (QS * 8) + 512 : NOWHERE;
    }
  }
  const bool gath = old == GRW;
  u32x4 pa[9];
  constexpr int C0 = early ? CE : CL;
  static_assert(C0 + 3 < 15 && C0 > GR + 1 && C0 >= 6, "schedule");
  sfor<early ? 3 : 2, 16>([&](auto R) {
    constexpr int r = R.v, t = r >= 8 ? r - 8 : r + early;
    unsigned tl = 0;
    if constexpr (!early && MEAS && ((PM >> r) & 1)) tl = pace_m<t_round(r), r>(clk, ph); else pace<t_round(r), r>(clk, ph);
    if constexpr (!early && ((PM >> (r - 1)) & 1)) {
      lat = __builtin_amdgcn_raw_buffer_load_b64(rl, w == MW ? lane * LSP : NOWHERE, 64 * LSP * (r - 1), SC1);
      SCHED_FENCE();
    }
    if constexpr (r < 8 && t == 8) {
      const __amdgpu_buffer_rsrc_t rx = isF ? rsrc(w2 + ((size_t)gI * 64 + 16 * w) * D + 80 * fr, 16 * D * 2) : rsrc(wo + (size_t)h * DH * D, DH * D * 2);
#pragma unroll
      for (int i = 0; i < 4; ++i)
        W2[i] = __builtin_amdgcn_raw_buffer_load_b128(rx, isF ? (r4 * D + 8 * ch) * 2 : i < 6 - XS ? wooff - (24 - 4 * XS) * D * 2 : NOWHERE, i * (4 * D * 2), 2);
      tch = __builtin_amdgcn_raw_buffer_load_b32(rsrc(wo + (size_t)h * DH * D, DH * D * 2), tid ? NOWHERE : wooff, 0, 2);
    } else {
#pragma unroll
      for (int j = 0; j < 3; ++j) {
        if (r >= 8) vld(t, j); else kld(t, j);
        if (r == 15 && j == 2) break;
        if (early && r == GR + 1) continue;
        THRN(early && r == 8 ? NFL + 2 : NFL);
      }
    }
    TT(r);
    SCHED_FENCE();
    if constexpr (early) {
      if (r == GR && gath) {
#pragma unroll
        for (int s = 0; s < 9; ++s) pa[s] = __builtin_amdgcn_raw_buffer_load_b128(rq_, go[s], 0, SC1);
        SCHED_FENCE();
      }
      if (r == GR + 1) {
        if (gath) {
          const unsigned tf = isF ? tag : 0u;
          bool ok = true;
#pragma unroll
          for (int s = 0; s < 9; ++s) ok = ok & (pa[s][1] == (s < 5 ? tag : tf)) & (pa[s][3] == (s < 5 ? tag : tf));
          if (!__all(ok)) {
            for (unsigned spin = 0;; ++spin) {
              ok = true;
              asm volatile("" ::: "memory");
#pragma unroll
              for (int s = 0; s < 9; ++s) {
                pa[s] = __builtin_amdgcn_raw_buffer_load_b128(rq_, go[s], 0, SC1);
                ok = ok & (pa[s][1] == (s < 5 ? tag : tf)) & (pa[s][3] == (s < 5 ? tag : tf));
              }
              if (__all(ok) || spin >= SPIN_MAX) break;
              __builtin_amdgcn_s_sleep(1);
            }
          }
          float qo[2], qm[2], hu[2];
#pragma unroll
          for (int k = 0; k < 2; ++k) {
            auto val = [&](int s) { return __builtin_bit_cast(float, (unsigned)pa[s][2 * k]); };
            qo[k] = (val(0) + val(1)) + val(2);
            const float t_ = val(3) + val(4);
            qm[k] = sw32(t_, t_);
            const float u = bfr((val(5) + val(6)) + (val(7) + val(8)));
            auto sw = __builtin_amdgcn_permlane32_swap(fbits(u), fbits(u), false, false);
            hu[k] = bfr(bfr(silu(__builtin_bit_cast(float, (unsigned)sw[0]))) * __builtin_bit_cast(float, (unsigned)sw[1]));
          }
          reinterpret_cast<unsigned*>(lds + L_QS)[lane + (hf ? 32 : 0)] = bfpack(qo[0], qo[1]);
          if (hf) {
            reinterpret_cast<unsigned*>(lds + L_QS)[lane] = bfpack(qm[0], qm[1]);
          } else {
            *reinterpret_cast<float2*>(lds + L_H + 2 * lane) = float2{hu[0], hu[1]};
          }
          LDS_DRAIN();
          if (lane == 0) lds_add(lds, L_SY + 2);
        }
        STAMP(2);
      }
    } else {
      if (r == 2) asm volatile("" ::"s"(st1), "v"(cpf), "v"(pre), "v"(pro));
      if (r == AR) lat = __builtin_amdgcn_raw_buffer_load_b64(rl, w == 6 ? lane * LSP : NOWHERE, 0, SC1);
      if (r == AR + 1) {
        const u64 d = (((u64)lat[1] << 32) | lat[0]) - own + 200u;
        const bool ok = d <= 400u;
        const float s_ = wave_sum(ok ? (float)(unsigned)d : 0.f), n = (float)__builtin_popcountll(__ballot(ok));
        const unsigned o = (unsigned)own + (n > 0.f ? (unsigned)(s_ * __builtin_amdgcn_rcpf(n)) - 200u : 0u);
        if (w == 6 && lane < 16) reinterpret_cast<unsigned*>(lds)[L_CK + lane] = o;
      }
      if ((PM >> r) & 1) {
        const unsigned org = __builtin_amdgcn_readfirstlane(reinterpret_cast<const unsigned*>(lds)[L_CK + r]);
        tag = __builtin_amdgcn_readfirstlane(reinterpret_cast<const unsigned*>(lds)[L_SY + 3]);
        THRN(3);
        const unsigned now = NOWT() - 9;
        const int l = (int)((tl ? tl : now) - org) - (t_round(r) + ph);
        __builtin_amdgcn_raw_buffer_store_b64(u32x2{(unsigned)(l < -64 ? 0 : l + 64), tag}, rl, w == MW && l < ULIM ? lw_ : NOWHERE,
                                              64 * LSP * r, SC1);
      }
      SCHED_FENCE();
    }
    if (r == C0) {
      lds_wait(lds, L_SY + 2, 1u);
      STAMP(3);
#pragma unroll
      for (int j = 0; j < 3; ++j) qf[j] = *reinterpret_cast<const u32x4*>(lds + L_QS + (c + 8 * j) * 4);
#pragma unroll
      for (int k = 0; k < 4; ++k) score(k);
    } else if (r == C0 + 1) {
#pragma unroll
      for (int k = 4; k < 7; ++k) score(k);
    } else if (r == C0 + 2) {
      score(7);
      mw = sc[0];
#pragma unroll
      for (int k = 1; k < 8; ++k) mw = fmaxf(mw, sc[k]);
      mw = xg_max(mw);
      lw = 0.f;
#pragma unroll
      for (int k = 0; k < 8; ++k) {
        const float p = ex2((sc[k] - mw) * LOG2E);
        lw += p;
        pb[k] = bfbits(p);
      }
      lw = xg_sum(lw);
      if (lane == 0) {
        lds[L_ML + 2 * w] = mw;
        lds[L_ML + 2 * w + 1] = lw;
      }
      if (early) {
        float z[8];
#pragma unroll
        for (int n = 0; n < 8; ++n) z[n] = 0.f;
#pragma unroll
        for (int r = 0; r < 4; ++r) {
          const unsigned chi = fbits(lds[L_H + 16 * w + 4 * r + r4]), clo = chi >> 16;
#pragma unroll
          for (int n = 0; n < 4; ++n) {
            z[2 * n] = dot2(W2[r][n], clo, z[2 * n]);
            z[2 * n + 1] = dot2(W2[r][n], chi, z[2 * n + 1]);
          }
        }
        const float2 zr = rows_reduce(z);
        if (c16 < 10) *reinterpret_cast<float2*>(lds + L_Z + 80 * w + 8 * c16 + 2 * r4) = zr;
      }
      LDS_DRAIN();
      if (lane == 0) lds_add(lds, L_SY + 4);
      STAMP(4);
    } else if (r == C0 + 3) {
#pragma unroll
      for (int k = 0; k < t + 1 - PVL; ++k) pv(k);
    } else if (r > C0 + 3 && r < 15) {
      pv(t - PVL);
    }
    if constexpr (!early && ((PM >> (r - 1)) & 1)) {
      SCHED_FENCE();
      constexpr int p = r - 1;
      const float m = wave_sum((float)(lat[1] == tag && lat[0] < UMISS + 64 ? lat[0] : UMISS + 64)) * (1.f / 64) - 64.f;
      int d = 0;
      if (p <= 8) {
        kslow = m >= ATHR;
        if (kslow) d = (int)(m * (ASH / 4.f)) + AGX * (lane <= p ? 0 : lane < 8 ? lane - p : 8 - p);
      } else if (!vslow && m >= (p <= 10 ? (kslow ? UTA : UTHR) : UTH2)) {
        vslow = true;
        d = (int)(fmaxf(m, 0.f) * ((p <= 10 ? USH : USH2) / 4.f)) + (lane > p ? UGX * (lane - p) : 0);
      } else if (vslow && m < CT) {
        d = lane > p + 1 ? -UGX * (lane - p - 1) : 0;
      }
      if (w == MW && lane < 16) reinterpret_cast<unsigned*>(lds)[L_CK + lane] += d;
    }
  });
  asm volatile("" ::"v"(tch));
  SCHED_FENCE();
  const __amdgpu_buffer_rsrc_t rwo = rsrc(wo + (size_t)h * DH * D, DH * D * 2);
  lds_wait(lds, L_SY + 4, 8u);
  float f, M, L;
  wg_ml(lds, M, L);
  f = ex2((mw - M) * LOG2E) / L;
  const unsigned cw = fbits(fmaf(M, LOG2E, __builtin_amdgcn_logf(L)));
  if (!early) tag = __builtin_amdgcn_readfirstlane(reinterpret_cast<const unsigned*>(lds)[L_SY + 3]);
  PIN(f);
  SCHED_FENCE();
  const int l40 = lane < 40 ? lane : 39;
  u64* rdst = gw + G2 + (size_t)rec * RS + 32 * r4 + 2 * c16;
  u64* zdst = gw + GZ + (size_t)(fr * 5 + (l40 >> 3)) * YB + 32 * gI + 16 * (gI >> 4) + 2 * (l40 & 7);
  unsigned pdst = lds_addr(lds + L_PART + w * DH + 8 * c + g), psrc = lds_addr(lds + L_PART + 48 * r4 + 4 * c16);
  unsigned cnt6 = lds_addr(lds + L_SY + 6), one = 1u;
  u32x4 rq = {cw, tag, cw, tag};
  u64 m48 = 0x0fff0fff0fff0fffull, m52 = 0x1fff1fff1fff1fffull;
  asm volatile("" : "+s"(m48), "+s"(m52), "+{v[156:159]}"(rq));
  PIN(rdst);
  PIN(zdst);
  PIN(pdst);
  PIN(psrc);
  PIN(cnt6);
  PIN(one);
#pragma unroll
  for (int k = 7 - PVL; k < 7; ++k) pv(k);
  auto wo1 = [&]() {
    if (WT_) pace<T_LAST + WT_, 15>(clk, ph);
    SCHED_FENCE();
#pragma unroll
    for (int i = 0; i < W1N; ++i) WO[i] = __builtin_amdgcn_raw_buffer_load_b128(rwo, wooff, i * (4 * D * 2), 2);
    SCHED_FENCE();
  };
  {
    unsigned plo = pb[7], phi = plo << 16;
    PIN(plo);
    PIN(phi);
    sfor<0, 3>([&](auto J) {
      constexpr int j = J.v;
      SCHED_FENCE();
      float a[8];
#pragma unroll
      for (int i = 0; i < 4; ++i) {
        a[2 * i] = dot2(V[7][j][i], plo, acc[8 * j + 2 * i]);
        a[2 * i + 1] = dot2(V[7][j][i], phi, acc[8 * j + 2 * i + 1]);
      }
      xg_store<256 * j>(a, f, pdst);
    });
    if (early) wo1();
  }
  STAMP(5);
  unsigned arr;
  asm volatile(
      "s_mov_b64 exec, 1\n\tds_add_rtn_u32 v112, %[cnt], %[one]\n\ts_mov_b64 exec, %[m48]\n\t" SUM8("%[src]")
      "v_readfirstlane_b32 %[arr], v112\n\t" SUM8B
      "s_cmp_eq_u32 %[arr], 7\n\t" SUM8C
      "v_cvt_pk_bf16_f32 v156, v116, v117\n\tv_cvt_pk_bf16_f32 v158, v118, v119\n\t"
      "s_cselect_b64 exec, %[m52], 0\n\tglobal_store_dwordx4 %[dst], v[156:159], off sc1\n\ts_mov_b64 exec, -1\n\ts_nop 0"
      : [arr] "=&s"(arr), "+{v[156:159]}"(rq)
      : [cnt] "v"(cnt6), [one] "v"(one), [m48] "s"(m48), [m52] "s"(m52), [src] "v"(psrc), [dst] "v"(rdst)
      : CLOB_T, "scc", "memory");
  if (arr == 0 && isF) {
    const float2* zp = reinterpret_cast<const float2*>(lds + L_Z) + l40;
    const float2 z0 = zp[0], z1 = zp[40], z2 = zp[80], z3 = zp[120];
    gst16(zdst, fbits((z0.x + z1.x) + (z2.x + z3.x)), fbits((z0.y + z1.y) + (z2.y + z3.y)), tag);
  }
  if constexpr (!early) {
    if (w == 7 && h < 5) {
      const int col = 80 * sp + 16 * h + 2 * c + (lane >> 5);
      const float xv = bf_lo(xs[col]);
      const u64* py = gw + GZ + (size_t)(sp * 5 + h) * YB + 32 * g + 2 * c + 392;
      bf16* po = out + col;
      float gt = 0.f;
      u64 m3 = 0ull, ms = 0ull;
      PIN(po);
#pragma unroll 1
      for (int pass = 0; pass < 2; ++pass) {
        lds_wait(lds, L_SY + (pass ? 1 : 6), pass ? 4u : 8u);
        STAMP(8);
        asm volatile("" : "+s"(m3), "+s"(ms));
        float t;
        unsigned n;
        asm volatile(
            "v_mov_b32 v124, 0\n\tv_mov_b32 v125, %[tag]\n\tv_mov_b32 v126, 0\n\tv_mov_b32 v127, %[tag]\n\t"
            "s_mov_b64 vcc, -1\n\ts_mov_b32 %[n], 0\n"
            ".Lfp%=:\n\t"
            "s_add_u32 %[n], %[n], 1\n\ts_cmp_gt_u32 %[n], %[max]\n\ts_cbranch_scc1 .Lfd%=\n\t"
            "s_mov_b64 exec, vcc\n\t"
            "global_load_dwordx4 v[112:115], %[p], off offset:-3136 sc1\n\t"
            "global_load_dwordx4 v[116:119], %[p], off offset:-1088 sc1\n\t"
            "global_load_dwordx4 v[120:123], %[p], off offset:1088 sc1\n\t"
            "s_and_b64 exec, vcc, %[m3]\n\t"
            "global_load_dwordx4 v[124:127], %[p], off offset:3136 sc1\n\t"
            "s_mov_b64 exec, -1\n\ts_waitcnt vmcnt(0)\n\t"
            "v_bitop3_b32 v128, v113, v115, %[tag] bitop3:0x7e\n\tv_bitop3_b32 v129, v117, v119, %[tag] bitop3:0x7e\n\t"
            "v_bitop3_b32 v130, v121, v123, %[tag] bitop3:0x7e\n\tv_bitop3_b32 v131, v125, v127, %[tag] bitop3:0x7e\n\t"
            "v_or3_b32 v128, v128, v129, v130\n\tv_add_f32 v132, v112, v116\n\tv_or_b32 v128, v128, v131\n\t"
            "v_add_f32 v134, v120, v124\n\tv_cmp_ne_u32 vcc, 0, v128\n\tv_add_f32 v133, v114, v118\n\t"
            "s_cbranch_vccnz .Lfp%=\n"
            ".Lfd%=:\n\t"
            "v_add_f32 v135, v122, v126\n\tv_add_f32 v132, v132, v134\n\tv_add_f32 v133, v133, v135\n\ts_nop 1\n\t"
            "v_permlane32_swap_b32 v132, v133\n\tv_add_f32 v134, v132, v133\n\tv_add_f32 v135, v132, v133\n\ts_nop 1\n\t"
            "v_permlane16_swap_b32 v134, v135\n\tv_add_f32 v134, v134, v135\n\ts_nop 1\n\t"
            "v_add_f32_dpp v134, v134, v134 row_ror:8 row_mask:0xf bank_mask:0xf\n\t"
            "v_cvt_pk_bf16_f32 v134, v134, v134\n\tv_lshlrev_b32 %[t], 16, v134\n\t"
            "v_mul_f32 v134, %[t], %[gt]\n\tv_cvt_pk_bf16_f32 v134, v134, v134\n\tv_lshlrev_b32 v134, 16, v134\n\t"
            "v_add_f32 v134, %[xv], v134\n\tv_cvt_pk_bf16_f32 v134, v134, v134\n\t"
            "s_mov_b64 exec, %[ms]\n\tglobal_store_short %[po], v134, off sc1\n\ts_mov_b64 exec, -1"
            : [t] "=&v"(t), [n] "=&s"(n)
            : [tag] "s"(tag), [max] "n"(SPIN_MAX), [p] "v"(py), [m3] "s"(m3), [ms] "s"(ms), [gt] "v"(gt), [xv] "v"(xv), [po] "v"(po)
            : CLOB_T, "vcc", "scc", "memory");
        if (pass) break;
        gt = bfr(silu(t));
        py -= GZ - GY;
        m3 = ~0ull;
        ms = 0x000000ff000000ffull;
      }
      if (b == 0 && lane == 0)
        __hip_atomic_store(reinterpret_cast<unsigned*>(gw + GEP), tag, __ATOMIC_RELAXED, __HIP_MEMORY_SCOPE_AGENT);
      STAMP(10);
    }
    STAMP_FLUSH();
    asm volatile(".Lcode_lastb:");
    return;
  }
  pace<T_LAST + W2T, 15>(clk, ph);
#pragma unroll
  for (int i = W1N; i < 6; ++i) WO[i] = __builtin_amdgcn_raw_buffer_load_b128(rwo, wooff, i * (4 * D * 2), 2);
  unsigned dm0 = __builtin_amdgcn_raw_buffer_load_b32(rwo, NOWHERE, 0, 2), dm1 = __builtin_amdgcn_raw_buffer_load_b32(rwo, NOWHERE, 4, 2);
  STAMP(3);
  SCHED_FENCE();
  const int ly = isF ? lane & 31 : lane;
  u64* ydst = gw + GY + (size_t)(4 * sp + (ly >> 3) + 4 * (ly >> 5)) * YB + 32 * h + 16 * (h >> 4) + 2 * (ly & 7);
  static_assert(RS * 8 * 4 == 4608, "record r4 + 4 is 4608 bytes behind record r4");
  const u64* pr = gw + G2 + (size_t)(h * NSP + r4 + 2) * RS + 32 * w + 2 * c16;
  const u64 ownm = 0xffffull << (16 * (sp & 3));
  u64 own0 = sp < 4 ? ownm : 0ull, own1 = sp < 4 ? 0ull : ownm, my = isF ? 0xffffffffull : ~0ull;
  u64 for0 = m52 & ~own0, for1 = m52 & ~own1;
  own0 &= m48;
  own1 &= m48;
  unsigned osrc = lds_addr(lds + L_PART + 48 * w + 4 * c16), ysrc = lds_addr(lds + L_Y + 2 * ly), cnt1 = lds_addr(lds + L_SY + 1);
  unsigned ydl = lds_addr(lds + L_Y + 128 * w + (isF ? 8 * c + g : 8 * c16 + 2 * r4));
  asm volatile("" : "+s"(own0), "+s"(own1), "+s"(for0), "+s"(for1), "+s"(my));
  PIN(ydst);
  PIN(pr);
  PIN(osrc);
  PIN(ysrc);
  PIN(cnt1);
  PIN(ydl);
#pragma unroll
  for (int i = XS; i < 6; ++i) X[i] = W2[i - XS];
  u32x8 WP[3], XP[3];
#pragma unroll
  for (int p = 0; p < 3; ++p)
#pragma unroll
    for (int k = 0; k < 4; ++k) {
      XP[p][2 * k] = __builtin_amdgcn_perm(X[2 * p + 1][k], X[2 * p][k], 0x05040100u);
      XP[p][2 * k + 1] = __builtin_amdgcn_perm(X[2 * p + 1][k], X[2 * p][k], 0x07060302u);
    }
  asm volatile("" : "+{v[64:71]}"(XP[0]), "+{v[72:79]}"(XP[1]), "+{v[80:87]}"(XP[2]));
  SCHED_FENCE();
  auto perm_wo = [&]() {
#pragma unroll
    for (int p = 0; p < 3; ++p)
#pragma unroll
      for (int k = 0; k < 4; ++k) {
        WP[p][2 * k] = __builtin_amdgcn_perm(WO[2 * p + 1][k], WO[2 * p][k], 0x05040100u);
        WP[p][2 * k + 1] = __builtin_amdgcn_perm(WO[2 * p + 1][k], WO[2 * p][k], 0x07060302u);
      }
    asm volatile("" : "+{v[88:95]}"(WP[0]), "+{v[96:103]}"(WP[1]), "+{v[104:111]}"(WP[2]));
    SCHED_FENCE();
    STAMP(1);
  };
  lds_wait(lds, L_SY + 6, 8u);
  if (PT_) pace<T_LAST + PT_, 15>(clk, 0);
  STAMP(4);
  u32x4 ra0, ra1;
  asm volatile(
      "v_mov_b32 v148, %[cw]\n\tv_mov_b32 v149, %[tag]\n\tv_mov_b32 v151, %[tag]\n\t"
      "v_mov_b32 v152, %[cw]\n\tv_mov_b32 v153, %[tag]\n\tv_mov_b32 v155, %[tag]\n\t"
      "s_mov_b64 exec, %[for0]\n\tglobal_load_dwordx4 v[148:151], %[p], off offset:-2304 sc1\n\t"
      "s_mov_b64 exec, %[for1]\n\tglobal_load_dwordx4 v[152:155], %[p], off offset:2304 sc1\n\t"
      "s_mov_b64 exec, %[m48]\n\t" SUM8("%[src]") SUM8B SUM8C
      "s_mov_b64 exec, %[own0]\n\tv_cvt_pk_bf16_f32 v148, v116, v117\n\tv_cvt_pk_bf16_f32 v150, v118, v119\n\t"
      "s_mov_b64 exec, %[own1]\n\tv_cvt_pk_bf16_f32 v152, v116, v117\n\tv_cvt_pk_bf16_f32 v154, v118, v119\n\t"
      "s_mov_b64 exec, -1"
      : "=&{v[148:151]}"(ra0), "=&{v[152:155]}"(ra1)
      : [cw] "v"(cw), [tag] "s"(tag), [for0] "s"(for0), [for1] "s"(for1), [p] "v"(pr), [own0] "s"(own0), [own1] "s"(own1),
        [m48] "s"(m48), [src] "v"(osrc)
      : CLOB_T, "memory");
  SCHED_FENCE();
  perm_wo();
  unsigned np;
  asm volatile(
      "s_mov_b32 %[n], 0\n\t"
      "s_mov_b64 exec, -1\n\tv_mov_b64 v[160:161], 0\n\tv_mov_b64 v[162:163], 0\n\tv_mov_b64 v[164:165], 0\n\t"
      "v_mov_b64 v[166:167], 0\n\ts_branch .Lrc%=\n"
      ".Lrp%=:\n\t"
      "s_add_u32 %[n], %[n], 1\n\ts_cmp_gt_u32 %[n], %[max]\n\ts_cbranch_scc1 .Lrd%=\n\t"
      "v_cmp_ne_u32 vcc, 0, v112\n\tv_cmp_ne_u32 s[98:99], 0, v113\n\ts_sleep " STR(RSLEEP) "\n\t"
      "s_mov_b64 exec, vcc\n\tglobal_load_dwordx4 v[148:151], %[p], off offset:-2304 sc1\n\t"
      "s_mov_b64 exec, s[98:99]\n\tglobal_load_dwordx4 v[152:155], %[p], off offset:2304 sc1\n\t"
      "s_mov_b64 exec, -1\n"
      ".Lrc%=:\n\t"
      "s_waitcnt vmcnt(0)\n\t"
      "v_bitop3_b32 v112, v149, v151, %[tag] bitop3:0x7e\n\tv_bitop3_b32 v113, v153, v155, %[tag] bitop3:0x7e\n\t"
      "v_max_f32 v115, v148, v152\n\tv_or_b32 v114, v112, v113\n\tv_max_f32 v116, v148, v152\n\t"
      "v_cmp_ne_u32 vcc, 0, v114\n\tv_lshlrev_b32 v122, 16, v148\n\ts_cbranch_vccnz .Lrp%=\n"
      ".Lrd%=:\n\t"
      "v_permlane16_swap_b32 v115, v116\n\tv_max_f32 v117, v115, v116\n\tv_max_f32 v118, v115, v116\n\t"
      "v_and_b32 v123, 0xffff0000, v148\n\tv_lshlrev_b32 v124, 16, v150\n\t"
      "v_permlane32_swap_b32 v117, v118\n\tv_max_f32 v117, v117, v118\n\t"
      "v_sub_f32 v115, v148, v117\n\tv_sub_f32 v116, v152, v117\n\tv_exp_f32 v115, v115\n\tv_exp_f32 v116, v116\n\t"
      "v_and_b32 v125, 0xffff0000, v150\n\tv_lshlrev_b32 v126, 16, v152\n\t"
      "v_add_f32 v117, v115, v116\n\tv_add_f32 v118, v115, v116\n\t"
      "v_mul_f32_dpp v122, v115, v122" BC12 "v_mul_f32_dpp v123, v115, v123" BC12
      "v_permlane16_swap_b32 v117, v118\n\tv_add_f32 v119, v117, v118\n\tv_add_f32 v120, v117, v118\n\t"
      "v_mul_f32_dpp v124, v115, v124" BC12 "v_mul_f32_dpp v125, v115, v125" BC12
      "v_permlane32_swap_b32 v119, v120\n\tv_add_f32 v119, v119, v120\n\tv_lshlrev_b32 v128, 16, v154\n\t"
      "v_rcp_f32 v119, v119\n\tv_and_b32 v127, 0xffff0000, v152\n\tv_and_b32 v129, 0xffff0000, v154\n\t"
      "v_fmac_f32_dpp v122, v116, v126" BC12 "v_fmac_f32_dpp v124, v116, v128" BC12
      "v_fmac_f32_dpp v123, v116, v127" BC12 "v_fmac_f32_dpp v125, v116, v129" BC12
      "v_permlane32_swap_b32 v122, v124\n\tv_add_f32 v122, v122, v124\n\t"
      "v_permlane32_swap_b32 v123, v125\n\tv_add_f32 v123, v123, v125\n\t"
      "v_mov_b32_dpp v120, v119" BC12 "s_nop 0\n\t"
      "v_permlane16_swap_b32 v122, v123\n\tv_add_f32 v122, v122, v123\n\t"
      "v_mul_f32 v123, v122, v120\n\ts_cmp_lt_u32 %[sp], 6\n\t"
      "v_mul_f32_dpp v124, v122, v120 quad_perm:[1,0,3,2] row_mask:0xf bank_mask:0xf\n\t"
      "v_cvt_pk_bf16_f32 v124, v123, v124\n\t"
      "s_cbranch_scc1 .Lyf%=\n\ts_nop 0\n\t"
      DOT8(64, 65, 66, 67, 68, 69, 70, 71, 0) DOT8(72, 73, 74, 75, 76, 77, 78, 79, 2) DOT8(80, 81, 82, 83, 84, 85, 86, 87, 4)
      DOT8(88, 89, 90, 91, 92, 93, 94, 95, 6) DOT8(96, 97, 98, 99, 100, 101, 102, 103, 8)
      DOT8(104, 105, 106, 107, 108, 109, 110, 111, 10) YRED
      "v_pk_add_f32 v[160:161], v[160:161], v[162:163]\n\tds_write_b64 %[ydl], v[160:161]\n"
      ".Lys%=:\n\t"
      "s_mov_b64 exec, 1\n\tds_add_rtn_u32 v112, %[cnt], %[one]\n\ts_mov_b64 exec, -1\n\t"
      "ds_read2st64_b64 v[116:119], %[ysrc] offset0:0 offset1:1\n\tds_read2st64_b64 v[120:123], %[ysrc] offset0:2 offset1:3\n\t"
      "s_waitcnt lgkmcnt(2)\n\tv_readfirstlane_b32 s98, v112\n\ts_waitcnt lgkmcnt(1)\n\t"
      "v_pk_add_f32 v[116:117], v[116:117], v[118:119]\n\ts_cmp_eq_u32 s98, 3\n\ts_cselect_b64 exec, %[my], 0\n\t"
      "s_waitcnt lgkmcnt(0)\n\tv_pk_add_f32 v[120:121], v[120:121], v[122:123]\n\ts_nop 0\n\t"
      "v_add_f32 v156, v116, v120\n\tv_add_f32 v158, v117, v121\n\t"
      "global_store_dwordx4 %[dst], v[156:159], off sc1\n\ts_mov_b64 exec, -1\n\ts_branch .Lye%=\n"
      ".Lyf%=:\n\ts_nop 0\n\t"
      "v_mov_b32_dpp v125, v124 row_newbcast:0 row_mask:0xf bank_mask:0x3\n\t"
      "v_mov_b32_dpp v126, v124 row_newbcast:2 row_mask:0xf bank_mask:0x3\n\t"
      "v_mov_b32_dpp v127, v124 row_newbcast:4 row_mask:0xf bank_mask:0x3\n\t"
      "v_mov_b32_dpp v125, v124 row_newbcast:6 row_mask:0xf bank_mask:0xc\n\t"
      "v_mov_b32_dpp v126, v124 row_newbcast:8 row_mask:0xf bank_mask:0xc\n\t"
      "v_mov_b32_dpp v127, v124 row_newbcast:10 row_mask:0xf bank_mask:0xc\n\t"
      DOT8F(88, 89, 90, 91, 92, 93, 94, 95, 125) DOT8F(96, 97, 98, 99, 100, 101, 102, 103, 126)
      DOT8F(104, 105, 106, 107, 108, 109, 110, 111, 127) YRED
      "v_add_f32 v160, v160, v162\n\tv_add_f32 v161, v161, v163\n\ts_nop 0\n\t"
      "v_add_f32_dpp v160, v160, v160 row_ror:8 row_mask:0xf bank_mask:0x3\n\t"
      "v_add_f32_dpp v160, v161, v161 row_ror:8 row_mask:0xf bank_mask:0xc\n\t"
      "ds_write_b32 %[ydl], v160\n\ts_branch .Lys%=\n"
      ".Lye%=:"
      : [n] "=&s"(np), "+{v[156:159]}"(rq), "+{v[148:151]}"(ra0), "+{v[152:155]}"(ra1)
      : [tag] "s"(tag), [p] "v"(pr), [max] "n"(SPIN_MAX), [sp] "s"(sp), [ydl] "v"(ydl), [cnt] "v"(cnt1), [one] "v"(one),
        [ysrc] "v"(ysrc), [my] "s"(my), [dst] "v"(ydst), "{v[64:71]}"(XP[0]), "{v[72:79]}"(XP[1]), "{v[80:87]}"(XP[2]),
        "{v[88:95]}"(WP[0]), "{v[96:103]}"(WP[1]), "{v[104:111]}"(WP[2])
      : CLOB_T, "v160", "v161", "v162", "v163", "v164", "v165", "v166", "v167", "s98", "s99", "vcc", "scc", "memory");
  STAMP(9);
  asm volatile("" ::"v"(dm0), "v"(dm1));
  STAMP_FLUSH();
  asm volatile(".Lcode_last:");
}

__global__ void __launch_bounds__(NT, 2)
fused_decode(const bf16* __restrict__ wq, const bf16* __restrict__ kc, const bf16* __restrict__ w1, const bf16* __restrict__ w3,
             u64* __restrict__ gw, const bf16* __restrict__ x, const bf16* __restrict__ vc,
             const bf16* __restrict__ wo, const bf16* __restrict__ w2, bf16* __restrict__ out) {
  __shared__ __attribute__((aligned(16))) float lds[L_END];
  const int b = blockIdx.x;
  const int xcd = b & 7, jj = b >> 3;
  const int h = xcd * 4 + (jj >> 3), sp = jj & 7;
  const int w = __builtin_amdgcn_readfirstlane(threadIdx.x >> 6);
  unsigned cpf = threadIdx.x;
  {
    u64 pc;
    unsigned len;
    asm volatile(
        "s_cmp_lt_u32 %[w], 4\n\ts_cbranch_scc1 .Lnocpf\n\t"
        "s_getpc_b64 %[pc]\n.Lcode_here:\n\tv_xor_b32 %[c], %[hi], %[c]\n\tv_lshlrev_b32 %[c], 6, %[c]\n\t"
        "s_mov_b32 %[len], .Lcode_last-.Lcode_here-4\n\ts_max_i32 %[len], %[len], .Lcode_lastb-.Lcode_here+508\n\t"
        "v_min_u32 %[c], %[len], %[c]\n\tglobal_load_dword %[c], %[c], %[pc]\n.Lnocpf:"
        : [c] "+v"(cpf), [pc] "=&s"(pc), [len] "=&s"(len) : [w] "s"(w), [hi] "s"((b & CPM) << 5) : "scc", "memory");
  }
  SCHED_FENCE();
  if (w >= 4)
    body<false>(lds, h, sp, w, x, kc, vc, wq, wo, w1, w3, w2, gw, out, cpf);
  else
    body<true>(lds, h, sp, w, x, kc, vc, wq, wo, w1, w3, w2, gw, out, cpf);
}

}

extern "C" void kernel_launch(void* const* d_in, const int* in_sizes, int n_in, void* d_out, int out_size, void* d_ws,
                              size_t ws_size, hipStream_t stream) {
  const bf16* x = (const bf16*)d_in[0];
  const bf16* kc = (const bf16*)d_in[1];
  const bf16* vc = (const bf16*)d_in[2];
  const bf16* wq = (const bf16*)d_in[3];
  const bf16* wo = (const bf16*)d_in[4];
  const bf16* w1 = (const bf16*)d_in[6];
  const bf16* w3 = (const bf16*)d_in[7];
  const bf16* w2 = (const bf16*)d_in[8];
  u64* gw = (u64*)d_ws;
  fused_decode<<<NWG, NT, 0, stream>>>(wq, kc, w1, w3, gw, x, vc, wo, w2, (bf16*)d_out);
}
```

```cpp
#include <hip/hip_runtime.h>
#include <hip/hip_bf16.h>
#include <stdint.h>

typedef __hip_bfloat16 bf16;
typedef unsigned long long u64;
typedef unsigned u32x4 __attribute__((ext_vector_type(4)));
typedef unsigned u32x8 __attribute__((ext_vector_type(8)));
typedef float f32x4 __attribute__((ext_vector_type(4)));
typedef float f32x2 __attribute__((ext_vector_type(2)));
typedef __bf16 bf16x2_t __attribute__((ext_vector_type(2)));

namespace {

constexpr int D = 640, HQ = 32, DH = 192, SK = 32768, WIN = 4096, FF = 1536;
constexpr int NSP = 8;
constexpr int NWG = HQ * NSP;
constexpr int NT = 512;
constexpr float SCALE = 0.07216878364870322f;
constexpr float LOG2E = 1.4426950408889634f;
constexpr unsigned SPIN_MAX = 1u << 16;
constexpr int SC1 = 16;
constexpr int NOWHERE = 1 << 30;

#define PV_ 81
#define PK_ 82
#define PX_ PK_
#define TS_ 358
#define SB_ 246
#define LOC 30
#define WT_ 98
#define W2T 132
#define W1N 3
#define PT_ 215
#define NFL 7
#define CE 7
#define CL 8
#define AR 3
#define GR 5
#define GRW 2
#define RSLEEP 1
#define MW 4
#define PM 0x1440
#define MEAS 1
#define ATHR 20
#define ASH 4
#define AGX 7
#define UTHR 8
#define UTH2 40
#define USH 4
#define UGX 9
#define ULIM 20
#define UMISS 60
#define PVL 1
#define LSP 16
#define GLP 1
#define E1P 1
#define UTA 5
#define USH2 3
#define CT 3
#define CPM 8
#define XS 6
#define LOCO 60
#define VT 10
#define CSH 30
#define HROT 16
constexpr int t_round(int r) { return r <= 2 ? TS_ : t_round(r - 1) + (r > 8 ? PV_ : r == 8 ? PX_ : PK_); }
constexpr int T_LAST = t_round(15);
constexpr int t_late(int i) { return t_round(i); }
constexpr int t_early(int i) { return t_round(i); }

constexpr int QS = 144;
constexpr int YB = 2 * (16 * 32 + 16);
constexpr int RS = 144;
constexpr size_t GQ = 0;
constexpr size_t G2 = GQ + (size_t)NWG * QS;
constexpr size_t GY = G2 + (size_t)NWG * RS;
constexpr size_t GZ = GY + 16;
constexpr size_t GEP = GY + (size_t)NSP * 5 * YB;
constexpr size_t GL = GEP + 16;

__device__ __forceinline__ unsigned fbits(float x) { return __builtin_bit_cast(unsigned, x); }
__device__ __forceinline__ u64 gld(const u64* p) {
  return __hip_atomic_load(p, __ATOMIC_RELAXED, __HIP_MEMORY_SCOPE_AGENT);
}
__device__ __forceinline__ float bf_lo(unsigned u) { return __builtin_bit_cast(float, u << 16); }
__device__ __forceinline__ float bf_hi(unsigned u) { return __builtin_bit_cast(float, u & 0xffff0000u); }
__device__ __forceinline__ float bfr(float x) { return __bfloat162float(__float2bfloat16(x)); }
__device__ __forceinline__ unsigned short bfbits(float x) {
  return __builtin_bit_cast(unsigned short, __float2bfloat16(x));
}
__device__ __forceinline__ float dot2(unsigned a, unsigned b, float c) {
  return __builtin_amdgcn_fdot2_f32_bf16(__builtin_bit_cast(bf16x2_t, a), __builtin_bit_cast(bf16x2_t, b), c, false);
}
template <int CTRL>
__device__ __forceinline__ float dpp(float x) {
  return __builtin_bit_cast(float, __builtin_amdgcn_mov_dpp(__builtin_bit_cast(int, x), CTRL, 0xf, 0xf, true));
}
__device__ __forceinline__ float grp8_sum(float x) {
  x += dpp<0xB1>(x);
  x += dpp<0x4E>(x);
  x += dpp<0x141>(x);
  return x;
}
__device__ __forceinline__ float sw16(float a, float b) {
  auto s = __builtin_amdgcn_permlane16_swap(fbits(a), fbits(b), false, false);
  return __builtin_bit_cast(float, (unsigned)s[0]) + __builtin_bit_cast(float, (unsigned)s[1]);
}
__device__ __forceinline__ float sw32(float a, float b) {
  auto s = __builtin_amdgcn_permlane32_swap(fbits(a), fbits(b), false, false);
  return __builtin_bit_cast(float, (unsigned)s[0]) + __builtin_bit_cast(float, (unsigned)s[1]);
}
__device__ __forceinline__ float xg_sum(float x) {
  x += dpp<0x128>(x);
  x = sw16(x, x);
  return sw32(x, x);
}
__device__ __forceinline__ float xg_max(float x) {
  x = fmaxf(x, dpp<0x128>(x));
  auto s = __builtin_amdgcn_permlane16_swap(fbits(x), fbits(x), false, false);
  x = fmaxf(__builtin_bit_cast(float, (unsigned)s[0]), __builtin_bit_cast(float, (unsigned)s[1]));
  auto t = __builtin_amdgcn_permlane32_swap(fbits(x), fbits(x), false, false);
  return fmaxf(__builtin_bit_cast(float, (unsigned)t[0]), __builtin_bit_cast(float, (unsigned)t[1]));
}
__device__ __forceinline__ float wave_sum(float x) { return xg_sum(grp8_sum(x)); }
__device__ __forceinline__ float ex2(float x) { return __builtin_amdgcn_exp2f(x); }
__device__ __forceinline__ float silu(float x) { return x / (1.0f + __expf(-x)); }
__device__ __forceinline__ float xg_reduce8(const float (&a)[8], bool g0) {
  float r[4];
#pragma unroll
  for (int i = 0; i < 4; ++i) r[i] = sw32(a[i], a[i + 4]);
  const float s0 = sw16(r[0], r[2]), s1 = sw16(r[1], r[3]);
  const float keep = g0 ? s1 : s0, send = g0 ? s0 : s1;
  return keep + dpp<0x128>(send);
}
__device__ __forceinline__ __amdgpu_buffer_rsrc_t rsrc(const void* p, unsigned bytes) {
  return __builtin_amdgcn_make_buffer_rsrc(const_cast<void*>(p), 0, bytes, 0x00020000);
}
__device__ __forceinline__ void gst16(u64* p, unsigned v0, unsigned v1, unsigned tag) {
  const u32x4 o = {v0, tag, v1, tag};
  asm volatile("global_store_dwordx4 %0, %1, off sc1\n\ts_nop 1" ::"v"(p), "v"(o) : "memory");
}
__device__ __forceinline__ unsigned bfpack(float a, float b) {
  return __builtin_bit_cast(unsigned, __builtin_convertvector(f32x2{a, b}, bf16x2_t));
}
__device__ __forceinline__ float2 rows_reduce(const float (&v)[8]) {
  float r[4];
#pragma unroll
  for (int i = 0; i < 4; ++i) r[i] = sw32(v[i], v[i + 4]);
  return float2{sw16(r[0], r[2]), sw16(r[1], r[3])};
}
template <int OFF>
__device__ __forceinline__ void xg_store(const float (&v)[8], float f, unsigned a) {
  float p[4], q[4], t, u;
#pragma unroll
  for (int i = 0; i < 4; ++i) {
    auto s = __builtin_amdgcn_permlane32_swap(fbits(v[i]), fbits(v[i + 4]), false, false);
    p[i] = __builtin_bit_cast(float, (unsigned)s[0]);
    q[i] = __builtin_bit_cast(float, (unsigned)s[1]);
  }
  const f32x2 r01 = f32x2{p[0], p[1]} + f32x2{q[0], q[1]}, r23 = f32x2{p[2], p[3]} + f32x2{q[2], q[3]};
  auto s0 = __builtin_amdgcn_permlane16_swap(fbits(r01[0]), fbits(r23[0]), false, false);
  auto s1 = __builtin_amdgcn_permlane16_swap(fbits(r01[1]), fbits(r23[1]), false, false);
  asm volatile("v_add_f32 %0, %2, %3\n\tv_add_f32 %1, %4, %5\n\ts_nop 0\n\t"
               "v_add_f32_dpp %0, %0, %0 row_ror:8 row_mask:0xf bank_mask:0x3\n\t"
               "v_add_f32_dpp %0, %1, %1 row_ror:8 row_mask:0xf bank_mask:0xc\n\t"
               "v_mul_f32 %0, %6, %0\n\tds_write_b32 %7, %0 offset:%8"
               : "=&v"(t), "=&v"(u)
               : "v"((unsigned)s0[0]), "v"((unsigned)s0[1]), "v"((unsigned)s1[0]), "v"((unsigned)s1[1]), "v"(f), "v"(a), "n"(OFF)
               : "memory");
}
#define SUM8(a)                                                                                                          \
  "ds_read_b128 v[116:119], " a "\n\tds_read_b128 v[120:123], " a " offset:768\n\t"                                      \
  "ds_read_b128 v[124:127], " a " offset:1536\n\tds_read_b128 v[128:131], " a " offset:2304\n\t"                         \
  "ds_read_b128 v[132:135], " a " offset:3072\n\tds_read_b128 v[136:139], " a " offset:3840\n\t"                         \
  "ds_read_b128 v[140:143], " a " offset:4608\n\tds_read_b128 v[144:147], " a " offset:5376\n\ts_waitcnt lgkmcnt(6)\n\t"
#define SUM8B                                                                                                            \
  "v_pk_add_f32 v[116:117], v[116:117], v[120:121]\n\tv_pk_add_f32 v[118:119], v[118:119], v[122:123]\n\t"               \
  "s_waitcnt lgkmcnt(4)\n\t"                                                                                             \
  "v_pk_add_f32 v[124:125], v[124:125], v[128:129]\n\tv_pk_add_f32 v[126:127], v[126:127], v[130:131]\n\t"               \
  "v_pk_add_f32 v[116:117], v[116:117], v[124:125]\n\tv_pk_add_f32 v[118:119], v[118:119], v[126:127]\n\t"
#define SUM8C                                                                                                            \
  "s_waitcnt lgkmcnt(2)\n\t"                                                                                             \
  "v_pk_add_f32 v[132:133], v[132:133], v[136:137]\n\tv_pk_add_f32 v[134:135], v[134:135], v[138:139]\n\t"               \
  "s_waitcnt lgkmcnt(0)\n\t"                                                                                             \
  "v_pk_add_f32 v[140:141], v[140:141], v[144:145]\n\tv_pk_add_f32 v[142:143], v[142:143], v[146:147]\n\t"               \
  "v_pk_add_f32 v[132:133], v[132:133], v[140:141]\n\tv_pk_add_f32 v[134:135], v[134:135], v[142:143]\n\t"               \
  "v_pk_add_f32 v[116:117], v[116:117], v[132:133]\n\tv_pk_add_f32 v[118:119], v[118:119], v[134:135]\n\t"
#define BC12 " row_newbcast:12 row_mask:0xf bank_mask:0xf\n\t"
#define DOTB(y, a, n) "v_dot2c_f32_bf16_dpp v" #y ", v124, v" #a " row_newbcast:" #n " row_mask:0xf bank_mask:0xf\n\t"
#define DOT8(a, b, c, d, e, f, g, h, n) \
  DOTB(160, a, n) DOTB(161, b, n) DOTB(162, c, n) DOTB(163, d, n) DOTB(164, e, n) DOTB(165, f, n) DOTB(166, g, n) DOTB(167, h, n)
#define DOTF(y, a, n) "v_dot2c_f32_bf16 v" #y ", v" #n ", v" #a "\n\t"
#define DOT8F(a, b, c, d, e, f, g, h, n) \
  DOTF(160, a, n) DOTF(161, b, n) DOTF(162, c, n) DOTF(163, d, n) DOTF(164, e, n) DOTF(165, f, n) DOTF(166, g, n) DOTF(167, h, n)
#define YRED                                                                                                             \
  "v_permlane32_swap_b32 v160, v164\n\tv_permlane32_swap_b32 v161, v165\n\tv_permlane32_swap_b32 v162, v166\n\t"         \
  "v_permlane32_swap_b32 v163, v167\n\tv_pk_add_f32 v[160:161], v[160:161], v[164:165]\n\t"                              \
  "v_pk_add_f32 v[162:163], v[162:163], v[166:167]\n\ts_nop 1\n\t"                                                       \
  "v_permlane16_swap_b32 v160, v162\n\tv_permlane16_swap_b32 v161, v163\n\t"
#define CLOB_T                                                                                                     \
  "v112", "v113", "v114", "v115", "v116", "v117", "v118", "v119", "v120", "v121", "v122", "v123", "v124", "v125",        \
      "v126", "v127", "v128", "v129", "v130", "v131", "v132", "v133", "v134", "v135", "v136", "v137", "v138", "v139",    \
      "v140", "v141", "v142", "v143", "v144", "v145", "v146", "v147"
#define SCHED_FENCE() __builtin_amdgcn_sched_barrier(0)
#define STR2(x) #x
#define STR(x) STR2(x)
#define THRN(n) do { SCHED_FENCE(); asm volatile("s_waitcnt vmcnt(%0)" ::"n"(n) : "memory"); SCHED_FENCE(); } while (0)
#define NOWT() ((unsigned)__builtin_amdgcn_s_memrealtime())
template <int N>
struct IC {
  static constexpr int v = N;
};
template <int I, int E, class F>
__device__ __forceinline__ void sfor(F&& f) {
  if constexpr (I < E) {
    f(IC<I>{});
    sfor<I + 1, E>(f);
  }
}
#define LDS_DRAIN() asm volatile("s_waitcnt lgkmcnt(0)" ::: "memory")
__device__ __forceinline__ unsigned lds_add(float* lds, int idx) {
  return __hip_atomic_fetch_add(reinterpret_cast<unsigned*>(lds + idx), 1u, __ATOMIC_RELAXED, __HIP_MEMORY_SCOPE_WORKGROUP);
}
__device__ __forceinline__ void lds_wait(float* lds, int idx, unsigned n) {
  SCHED_FENCE();
#pragma unroll 1
  for (unsigned spin = 0; spin < (SPIN_MAX << 4); ++spin) {
    if (__hip_atomic_load(reinterpret_cast<unsigned*>(lds + idx), __ATOMIC_RELAXED, __HIP_MEMORY_SCOPE_WORKGROUP) >= n) break;
    __builtin_amdgcn_s_sleep(1);
  }
  asm volatile("" ::: "memory");
  SCHED_FENCE();
}
template <int T, int R>
__device__ __forceinline__ void pace(unsigned clk, int ph) {
  unsigned o;
  SCHED_FENCE();
  asm volatile(
      "s_branch .Lpb%=\n"
      ".Lpa%=:\n\ts_sleep 1\n"
      ".Lpb%=:\n\tds_read_b32 %0, %1 offset:%4\n\ts_memrealtime s[98:99]\n\ts_waitcnt lgkmcnt(0)\n\tv_readfirstlane_b32 s99, %0\n\t"
      "s_sub_u32 s98, s98, %2\n\ts_sub_u32 s98, s98, s99\n\ts_cmp_lt_i32 s98, %3\n\ts_cbranch_scc1 .Lpa%="
      : "=&v"(o) : "v"(clk), "s"(ph), "n"(T), "n"(4 * R) : "s98", "s99", "scc", "memory");
  SCHED_FENCE();
}

template <int T, int R>
__device__ __forceinline__ unsigned pace_m(unsigned clk, int ph) {
  unsigned o, tl;
  SCHED_FENCE();
  asm volatile(
      "s_mov_b32 %1, 0\n\ts_branch .Lpb%=\n"
      ".Lpa%=:\n\ts_sleep 1\n"
      ".Lpb%=:\n\tds_read_b32 %0, %2 offset:%5\n\ts_getreg_b32 s97, hwreg(HW_REG_IB_STS, 0, 4)\n\ts_memrealtime s[98:99]\n\t"
      "s_or_b32 s97, s97, %1\n\ts_waitcnt lgkmcnt(0)\n\ts_cselect_b32 %1, %1, s98\n\tv_readfirstlane_b32 s99, %0\n\t"
      "s_sub_u32 s98, s98, %3\n\ts_sub_u32 s98, s98, s99\n\ts_cmp_lt_i32 s98, %4\n\ts_cbranch_scc1 .Lpa%="
      : "=&v"(o), "=&s"(tl) : "v"(clk), "s"(ph), "n"(T), "n"(4 * R) : "s97", "s98", "s99", "scc", "memory");
  SCHED_FENCE();
  return tl;
}

#define STAMP(i)
#define STAMP_FLUSH()
#define TT(i)
constexpr int L_QP = 0;
constexpr int L_QS = L_QP + 4 * 128;
constexpr int L_PART = L_QS + 96;
constexpr int L_ML = L_PART + 8 * DH;
constexpr int L_Z = L_ML + 16;
constexpr int L_Y = L_Z + 320;
constexpr int L_H = L_Y + 512;
constexpr int L_SY = L_H + 64;
constexpr int L_CK = L_SY + 8;
constexpr int L_END = L_CK + 16;

#define PIN(x) asm volatile("" : "+v"(x))

__device__ __forceinline__ void wg_ml(const float* lds, float& M, float& L) {
  float mm[8], ll[8];
#pragma unroll
  for (int i = 0; i < 4; ++i) {
    const f32x4 t = *reinterpret_cast<const f32x4*>(lds + L_ML + 4 * i);
    mm[2 * i] = t[0];
    ll[2 * i] = t[1];
    mm[2 * i + 1] = t[2];
    ll[2 * i + 1] = t[3];
  }
  M = mm[0];
#pragma unroll
  for (int ww = 1; ww < 8; ++ww) M = fmaxf(M, mm[ww]);
  L = 0.f;
#pragma unroll
  for (int ww = 0; ww < 8; ++ww) L = fmaf(ex2((mm[ww] - M) * LOG2E), ll[ww], L);
}
__device__ __forceinline__ unsigned lds_addr(const float* p) {
  return (unsigned)(size_t)(const __attribute__((address_space(3))) float*)p;
}

template <bool early>
__device__ __forceinline__ void body(float* lds, const int h, const int sp, const int w, const bf16* __restrict__ x,
                                     const bf16* __restrict__ kc, const bf16* __restrict__ vc,
                                     const bf16* __restrict__ wq, const bf16* __restrict__ wo,
                                     const bf16* __restrict__ w1, const bf16* __restrict__ w3,
                                     const bf16* __restrict__ w2, u64* __restrict__ gw, bf16* __restrict__ out,
                                     unsigned cpf) {
  const int b = blockIdx.x;
  const int tid = threadIdx.x, lane = tid & 63;
  const int g = lane >> 3, c = lane & 7;
  const int rec = h * NSP + sp;

  STAMP(0);
  const bool isF = sp < 6;
  u32x4 PR[8], xa, xb, xq;
  unsigned epoch = 0;
  u64 tk = 0;
  int ra = 0, RA0 = 0;
  bool va = false;
  if constexpr (early) {
    int s0_ = 240, s1_ = 80, s2_ = 240 * (sp & 1), s3_ = 80 * sp, s4_ = 64 * (sp >> 1), s5_ = 64 * w, s6_ = 24 * w;
    asm("" : "+s"(s0_), "+s"(s1_), "+s"(s2_), "+s"(s3_), "+s"(s4_), "+s"(s5_), "+s"(s6_));
    const int rw0 = isF ? s5_ : s6_;
    int lim = (isF ? s0_ : s1_) - rw0, lim2 = isF ? 64 : 24;
    asm("" : "+s"(lim), "+s"(lim2));
    lim = lim < lim2 ? lim : lim2;
    ra = rw0 + 8 * g;
    va = 8 * g < lim;
    RA0 = isF ? s2_ : s3_;
    int aoff = (int)__umul24(g, 8 * HQ * DH * 2) + 16 * c;
    PIN(aoff);
    aoff = va ? aoff : NOWHERE;
    int sa = ((RA0 + rw0) * (HQ * DH) + h * DH + (isF ? s4_ : 64)) * 2;
    PIN(aoff);
    asm volatile("" : "+s"(sa));
    SCHED_FENCE();
    const __amdgpu_buffer_rsrc_t rw = rsrc(wq, NOWHERE);
    PR[0] = __builtin_amdgcn_raw_buffer_load_b128(rw, aoff, sa, 2);
    SCHED_FENCE();
    tk = __builtin_amdgcn_s_memrealtime();
    SCHED_FENCE();
    asm volatile("s_load_dword %0, %1, %2" : "=&s"(epoch) : "s"(gw), "s"(E1P && w == 1 ? (int)GL * 8 + (sp * 8 + (b & 7)) * LSP : (int)GEP * 8) : "memory");
    SCHED_FENCE();
#pragma unroll
    for (int i = 1; i < 8; ++i) PR[i] = __builtin_amdgcn_raw_buffer_load_b128(rw, aoff, sa + i * (HQ * DH * 2), 2);
    SCHED_FENCE();
    SCHED_FENCE();
  }
  const unsigned short* xs = reinterpret_cast<const unsigned short*>(x);
  const size_t wgb = ((size_t)h * SK + (SK - WIN)) * DH;
  const __amdgpu_buffer_rsrc_t rk = rsrc(kc + wgb, WIN * DH * 2);
  const __amdgpu_buffer_rsrc_t rv = rsrc(vc + wgb, WIN * DH * 2);
  const int voff = (g * DH + 8 * c) * 2;
  auto tile = [&](int t) { return (t * 64 + h * HROT + w * 8 + sp) & 511; };
  u32x4 K[8][3], V[8][3];
  auto kld = [&](int t, int j) { K[t][j] = __builtin_amdgcn_raw_buffer_load_b128(rk, voff + 128 * j, tile(t) * 3072, 2); };
  auto vld = [&](int t, int j) { V[t][j] = __builtin_amdgcn_raw_buffer_load_b128(rv, voff + 128 * j, tile(t) * 3072, 2); };
  unsigned tag = 0;
  const unsigned clk = lds_addr(lds + L_CK);
  const int ph = (w * PV_) >> 3;
  const int r4 = lane >> 4, c16 = lane & 15;
  const int ch = c16 < 10 ? c16 : 9;
  const int fi = (h & 3) * 6 + (isF ? sp : 5), gl = fi >> 3, gI = (h >> 2) * 3 + gl, fr = fi & 7;
  u32x4 WO[6], X[6], W2[4];
  unsigned tch = 0;
  const int wooff = ((48 * w + r4 + (isF ? 24 * (g & 1) : 24)) * D + 64 * sp + 8 * c + (isF ? 0 : 128 * (g & 1))) * 2;
  typedef unsigned u32x2 __attribute__((ext_vector_type(2)));
  const __amdgpu_buffer_rsrc_t rl = rsrc(gw + GL, 16 * 64 * LSP);
  const bool pub = !((sp + (b >> 6)) & 3);
  const int lw_ = lane == 0 && pub ? (sp * 8 + (b & 7)) * LSP : NOWHERE;
  u32x2 lat = {0u, 0u};
  auto fetch = [&](int off) {
    SCHED_FENCE();
    asm volatile("buffer_load_dwordx2 %0, %1, %2, %3 offen sc1" : "+v"(lat) : "v"(lane * LSP), "s"(rl), "s"(off) : "memory");
    SCHED_FENCE();
  };
  u64 own = 0, pre = 0;
  unsigned pro = 0, st1 = 0, old = 0;
  bool vslow = false, kslow = false;
  float sh = 0.f;
  if constexpr (early) {
    u32x4 PF[5];
    const int F0 = (isF ? 160 * (fr >> 1) : 480) + 40 * w;
    const int fs = isF ? FF : HQ * DH;
    int s6_ = 64 * gI, s7_ = h * DH + 128 * (sp & 1);
    asm("" : "+s"(s6_), "+s"(s7_));
    const int boff = ((int)__umul24(g, fs) + 8 * c) * 2;
    const int sb = (F0 * fs + (isF ? s6_ : s7_)) * 2;
    const bf16* pf_ = (fr & 1) ? w3 : w1;
    asm("" : "+s"(pf_));
    const __amdgpu_buffer_rsrc_t rf = rsrc(isF ? pf_ : wq, NOWHERE);
    unsigned short xr[5];
#pragma unroll
    for (int i = 0; i < 5; ++i) PF[i] = __builtin_amdgcn_raw_buffer_load_b128(rf, boff, sb + i * 16 * fs, 2);
    SCHED_FENCE();
    if (!isF) {
#pragma unroll
      for (int i = 0; i < XS; ++i)
        X[i] = __builtin_amdgcn_raw_buffer_load_b128(rsrc(wo + (size_t)h * DH * D, DH * D * 2), wooff - 24 * D * 2 + i * (4 * D * 2), 0, 2);
    } else {
#pragma unroll
      for (int i = 0; i < XS; ++i) X[i] = u32x4{0u, 0u, 0u, 0u};
    }
    SCHED_FENCE();
    xa = *reinterpret_cast<const u32x4*>(x + 8 * lane);
    xb = *reinterpret_cast<const u32x4*>(x + 512 + 8 * (lane & 15));
    xq = *reinterpret_cast<const u32x4*>(x + RA0 + (va ? ra : 0));
#pragma unroll
    for (int i = 0; i < 5; ++i) xr[i] = xs[F0 + 8 * i + g];
    SCHED_FENCE();
    const unsigned epv = __hip_atomic_load(reinterpret_cast<unsigned*>(gw + GEP), __ATOMIC_RELAXED, __HIP_MEMORY_SCOPE_AGENT);
    SCHED_FENCE();
    asm volatile("" : "+s"(tk));
    if (tid < 16) reinterpret_cast<unsigned*>(lds)[L_CK + tid] = (unsigned)tk;
    asm volatile("s_waitcnt lgkmcnt(0)\n\ts_barrier" : "+s"(epoch)::"memory");
    STAMP(6);
    SCHED_FENCE();
#pragma unroll
    for (int t = 0; t < 3; ++t)
#pragma unroll
      for (int j = 0; j < 3; ++j) kld(t, j);
    SCHED_FENCE();
    float mb, den;
    {
      float xf[8], xg[8];
#pragma unroll
      for (int i = 0; i < 4; ++i) {
        xf[2 * i] = bf_lo(xa[i]);
        xf[2 * i + 1] = bf_hi(xa[i]);
        xg[2 * i] = bf_lo(xb[i]);
        xg[2 * i + 1] = bf_hi(xb[i]);
      }
      const bool lo16 = lane < 16;
      float sum = 0.f;
#pragma unroll
      for (int i = 0; i < 8; ++i) sum += xf[i] + (lo16 ? xg[i] : 0.f);
      sum = wave_sum(sum);
      const float mean = sum * (1.0f / D);
      float sq = 0.f;
#pragma unroll
      for (int i = 0; i < 8; ++i) {
        const float d1 = xf[i] - mean, d2 = xg[i] - mean;
        sq += d1 * d1 + (lo16 ? d2 * d2 : 0.f);
      }
      sq = wave_sum(sq);
      mb = bfr(mean);
      const float vb = bfr(sq * (1.0f / D));
      den = bfr(sqrtf(bfr(vb + 1e-5f)));
    }
    {
      unsigned xn[13];
#pragma unroll
      for (int i = 0; i < 4; ++i) {
        xn[2 * i] = fbits(bfr(bfr(bf_lo(xq[i]) - mb) / den));
        xn[2 * i + 1] = fbits(bfr(bfr(bf_hi(xq[i]) - mb) / den));
      }
#pragma unroll
      for (int i = 0; i < 5; ++i) xn[8 + i] = fbits(bfr(bfr(bf_lo(xr[i]) - mb) / den));
#pragma unroll
      for (int i = 0; i < 13; ++i) PIN(xn[i]);
      SCHED_FENCE();
      pace<t_round(2), 2>(clk, ph);
#pragma unroll
      for (int j = 0; j < 3; ++j) kld(3, j);
      TT(2);
      SCHED_FENCE();
      float fa[8], fg[8];
#pragma unroll
      for (int k = 0; k < 8; ++k) fa[k] = fg[k] = 0.f;
#pragma unroll
      for (int i = 0; i < 8; ++i)
#pragma unroll
        for (int k = 0; k < 4; ++k) {
          fa[2 * k] = dot2(PR[i][k], xn[i] >> 16, fa[2 * k]);
          fa[2 * k + 1] = dot2(PR[i][k], xn[i], fa[2 * k + 1]);
        }
      lds[L_QP + w * 128 + 8 * c + g] = xg_reduce8(fa, g & 1);
#pragma unroll
      for (int i = 0; i < 5; ++i)
#pragma unroll
        for (int k = 0; k < 4; ++k) {
          fg[2 * k] = dot2(PF[i][k], xn[8 + i] >> 16, fg[2 * k]);
          fg[2 * k + 1] = dot2(PF[i][k], xn[8 + i], fg[2 * k + 1]);
        }
      lds[L_QP + w * 128 + 64 + 8 * c + g] = xg_reduce8(fg, g & 1);
    }
    SCHED_FENCE();
    tag = __builtin_amdgcn_readfirstlane(epv) + 1u;
    if (tid == 0) reinterpret_cast<unsigned*>(lds)[L_SY + 3] = tag;
    LDS_DRAIN();
    if (lane == 0) old = lds_add(lds, L_SY);
    old = __builtin_amdgcn_readfirstlane(old);
    if (old == 3) {
      const float2* qp = reinterpret_cast<const float2*>(lds + L_QP) + lane;
      const float2 p0_ = qp[0], p1_ = qp[64], p2_ = qp[128], p3_ = qp[192];
      gst16(gw + GQ + (size_t)rec * QS + 2 * lane, fbits((p0_.x + p1_.x) + (p2_.x + p3_.x)),
            fbits((p0_.y + p1_.y) + (p2_.y + p3_.y)), tag);
    }
    STAMP(11);
  } else {
    if (tid >= NT - 8) reinterpret_cast<unsigned*>(lds)[L_SY + tid - (NT - 8)] = 0u;
    unsigned st0 = 0;
    {
      const bf16* tp = w == 4 ? kc + wgb : w == 5 ? (!isF ? wo + (size_t)h * DH * D : (fr & 1) ? w3 : w1) : w == 6 ? ((sp & 1) ? x : wq) : vc + wgb;
      asm volatile("s_waitcnt lgkmcnt(0)\n\ts_load_dword %0, %1, 0x0" : "=&s"(st0) : "s"(tp) : "memory");
    }
    SCHED_FENCE();
#pragma unroll
    for (int j = 0; j < 3; ++j) kld(0, j);
    SCHED_FENCE();
    asm volatile("s_barrier" ::: "memory");
    STAMP(6);
    SCHED_FENCE();
    if (w < 6) {
      const int l = lane < 42 ? lane : 41, k = l & 15;
      const int yg = 4 * (k < 8 || isF ? sp : sp + 2) + ((k >> 1) & 3);
      const size_t yo = l < 32 || !isF ? GY + (size_t)yg * YB + 32 * h + 16 * (h >> 4) + 8 * (k & 1)
                                       : GZ + (size_t)(fr * 5 + (k >> 1)) * YB + 32 * gI + 16 * (gI >> 4) + 8 * (k & 1);
      unsigned o4 = (unsigned)(l < 16 ? G2 + (size_t)rec * RS + 8 * l : yo);
      unsigned o5 = (unsigned)(GQ + (size_t)rec * QS + 8 * (lane < 16 ? lane : 15));
      if (GLP && pub && lane >= 16 && lane < 32 && ((PM >> (lane - 16)) & 1))
        o5 = (unsigned)GL + (unsigned)(((lane - 16) * 64 + sp * 8 + (b & 7)) * (LSP / 8));
      PIN(o4);
      PIN(o5);
      pre = gld(gw + (w == 4 ? o4 : o5));
    }
    if (w == 7 && h < 5)
      pro = __hip_atomic_load(reinterpret_cast<const unsigned*>(out + 80 * sp + 16 * h) + (lane & 7), __ATOMIC_RELAXED,
                              __HIP_MEMORY_SCOPE_AGENT);
    SCHED_FENCE();
    asm volatile("s_waitcnt lgkmcnt(0)" ::"s"(st0) : "memory");
#pragma unroll
    for (int j = 0; j < 3; ++j) {
      kld(1, j);
      THRN(5);
    }
    THRN(3);
    TT(1);
    own = __builtin_amdgcn_s_memrealtime() - SB_;
    __builtin_amdgcn_raw_buffer_store_b64(u32x2{(unsigned)own, (unsigned)(own >> 32)}, rl, w == 6 ? lw_ : NOWHERE, 0, SC1);
    SCHED_FENCE();
    if (LOC && w == 6 && lane < 16) {
      const unsigned o = (unsigned)own - (isF ? LOC : LOCO);
      if ((int)(o - reinterpret_cast<unsigned*>(lds)[L_CK + lane]) > 0) reinterpret_cast<unsigned*>(lds)[L_CK + lane] = o;
    }
    SCHED_FENCE();
    {
      const bf16* tp = (w & 1) ? wo + (size_t)h * DH * D : w2 + (size_t)gI * 64 * D;
      asm volatile("s_load_dword %0, %1, 0x0" : "=&s"(st1) : "s"(tp) : "memory");
    }
    SCHED_FENCE();
  }
  u32x4 qf[3];
  float sc[8], mw, lw, acc[24];
  unsigned pb[8];
  auto score = [&](int t) {
    float d = 0.f;
#pragma unroll
    for (int j = 0; j < 3; ++j)
#pragma unroll
      for (int i = 0; i < 4; ++i) d = dot2(K[t][j][i], qf[j][i], d);
    d = grp8_sum(d);
    sc[t] = bfr(bfr(d) * SCALE);
  };
  auto pv = [&](int t) {
    const unsigned plo = pb[t], phi = plo << 16;
#pragma unroll
    for (int j = 0; j < 3; ++j)
#pragma unroll
      for (int i = 0; i < 4; ++i) {
        acc[8 * j + 2 * i] = dot2(V[t][j][i], plo, t ? acc[8 * j + 2 * i] : 0.f);
        acc[8 * j + 2 * i + 1] = dot2(V[t][j][i], phi, t ? acc[8 * j + 2 * i + 1] : 0.f);
        PIN(acc[8 * j + 2 * i]);
        PIN(acc[8 * j + 2 * i + 1]);
      }
  };
  const bool hf = lane >> 5;
  const __amdgpu_buffer_rsrc_t rq_ = rsrc(gw + GQ, NWG * QS * 8);
  int go[9];
  {
    const int l16 = (lane & 31) * 16;
    const int qa = l16 + (8 * h + (hf ? 4 : 0)) * (QS * 8);
    go[0] = qa;
    go[1] = qa + QS * 8;
    go[2] = l16 + (8 * h + (hf ? 7 : 6)) * (QS * 8) + 512;
    go[3] = qa + 2 * QS * 8;
    go[4] = qa + 3 * QS * 8;
#pragma unroll
    for (int k = 0; k < 4; ++k) {
      const int m = 8 * gl + 2 * k + (hf ? 1 : 0);
      go[5 + k] = isF ? l16 + (32 * (h >> 2) + m + (((m * 43) >> 7) & 6)) * (QS * 8) + 512 : NOWHERE;
    }
  }
  const bool gath = old == GRW;
  u32x4 pa[9];
  constexpr int C0 = early ? CE : CL;
  static_assert(C0 + 3 < 15 && C0 > GR + 1 && C0 >= 6, "schedule");
  sfor<early ? 3 : 2, 16>([&](auto R) {
    constexpr int r = R.v, t = r >= 8 ? r - 8 : r + early;
    unsigned tl = 0;
    if constexpr (!early && MEAS && ((PM >> r) & 1)) tl = pace_m<t_round(r), r>(clk, ph); else pace<t_round(r), r>(clk, ph);
    if constexpr (!early && ((PM >> (r - 1)) & 1)) {
      if (w == MW) fetch(64 * LSP * (r - 1));
    }
    if constexpr (r < 8 && t == 8) {
      const __amdgpu_buffer_rsrc_t rx = isF ? rsrc(w2 + ((size_t)gI * 64 + 16 * w) * D + 80 * fr, 16 * D * 2) : rsrc(wo + (size_t)h * DH * D, DH * D * 2);
#pragma unroll
      for (int i = 0; i < 4; ++i)
        W2[i] = __builtin_amdgcn_raw_buffer_load_b128(rx, isF ? (r4 * D + 8 * ch) * 2 : i < 6 - XS ? wooff - (24 - 4 * XS) * D * 2 : NOWHERE, i * (4 * D * 2), 2);
      tch = __builtin_amdgcn_raw_buffer_load_b32(rsrc(wo + (size_t)h * DH * D, DH * D * 2), tid ? NOWHERE : wooff, 0, 2);
    } else {
#pragma unroll
      for (int j = 0; j < 3; ++j) {
        if (r >= 8) vld(t, j); else kld(t, j);
        if (r == 15 && j == 2) break;
        if (early && r == GR + 1) continue;
        THRN(early && r == 8 ? NFL + 2 : NFL);
      }
    }
    TT(r);
    SCHED_FENCE();
    if constexpr (early) {
      if (r == GR && gath) {
#pragma unroll
        for (int s = 0; s < 9; ++s) pa[s] = __builtin_amdgcn_raw_buffer_load_b128(rq_, go[s], 0, SC1);
        SCHED_FENCE();
      }
      if (r == GR + 1) {
        if (gath) {
          const unsigned tf = isF ? tag : 0u;
          bool ok = true;
#pragma unroll
          for (int s = 0; s < 9; ++s) ok = ok & (pa[s][1] == (s < 5 ? tag : tf)) & (pa[s][3] == (s < 5 ? tag : tf));
          if (!__all(ok)) {
            for (unsigned spin = 0;; ++spin) {
              ok = true;
              asm volatile("" ::: "memory");
#pragma unroll
              for (int s = 0; s < 9; ++s) {
                pa[s] = __builtin_amdgcn_raw_buffer_load_b128(rq_, go[s], 0, SC1);
                ok = ok & (pa[s][1] == (s < 5 ? tag : tf)) & (pa[s][3] == (s < 5 ? tag : tf));
              }
              if (__all(ok) || spin >= SPIN_MAX) break;
              __builtin_amdgcn_s_sleep(1);
            }
          }
          float qo[2], qm[2], hu[2];
#pragma unroll
          for (int k = 0; k < 2; ++k) {
            auto val = [&](int s) { return __builtin_bit_cast(float, (unsigned)pa[s][2 * k]); };
            qo[k] = (val(0) + val(1)) + val(2);
            const float t_ = val(3) + val(4);
            qm[k] = sw32(t_, t_);
            const float u = bfr((val(5) + val(6)) + (val(7) + val(8)));
            auto sw = __builtin_amdgcn_permlane32_swap(fbits(u), fbits(u), false, false);
            hu[k] = bfr(bfr(silu(__builtin_bit_cast(float, (unsigned)sw[0]))) * __builtin_bit_cast(float, (unsigned)sw[1]));
          }
          reinterpret_cast<unsigned*>(lds + L_QS)[lane + (hf ? 32 : 0)] = bfpack(qo[0], qo[1]);
          if (hf) {
            reinterpret_cast<unsigned*>(lds + L_QS)[lane] = bfpack(qm[0], qm[1]);
          } else {
            *reinterpret_cast<float2*>(lds + L_H + 2 * lane) = float2{hu[0], hu[1]};
          }
          LDS_DRAIN();
          if (lane == 0) lds_add(lds, L_SY + 2);
        }
        STAMP(2);
      }
    } else {
      if (r == 2) asm volatile("" ::"s"(st1), "v"(cpf), "v"(pre), "v"(pro));
      if (r == AR && w == 6) fetch(0);
      if (r == AR + 1 && w == 6) {
        asm volatile("s_waitcnt vmcnt(3)" : "+v"(lat)::"memory");
        const u64 d = (((u64)lat[1] << 32) | lat[0]) - own + 200u;
        const bool ok = d <= 400u;
        const float s_ = wave_sum(ok ? (float)(unsigned)d : 0.f), n = (float)__builtin_popcountll(__ballot(ok));
        const unsigned o = (unsigned)own + (n > 0.f ? (unsigned)(s_ * __builtin_amdgcn_rcpf(n)) - 200u : 0u);
        if (w == 6 && lane < 16) reinterpret_cast<unsigned*>(lds)[L_CK + lane] = o;
      }
      if (r == AR + 2) {
        lds_wait(lds, L_SY, 4u);
        tag = __builtin_amdgcn_readfirstlane(reinterpret_cast<const unsigned*>(lds)[L_SY + 3]);
      }
      static_assert(!(PM & ((1 << (AR + 2)) - 1)), "tag");
      if ((PM >> r) & 1) {
        const unsigned org = __builtin_amdgcn_readfirstlane(reinterpret_cast<const unsigned*>(lds)[L_CK + r]);
        if (w == MW && pub && !tl) THRN(3);
        const unsigned now = NOWT() - 9;
        const int l = (int)((tl ? tl : now) - org) - (t_round(r) + ph);
        __builtin_amdgcn_raw_buffer_store_b64(u32x2{(unsigned)(l < -64 ? 0 : l + 64), tag}, rl, w == MW && l < ULIM ? lw_ : NOWHERE,
                                              64 * LSP * r, SC1);
      }
      SCHED_FENCE();
    }
    if (r == C0) {
      lds_wait(lds, L_SY + 2, 1u);
      STAMP(3);
#pragma unroll
      for (int j = 0; j < 3; ++j) qf[j] = *reinterpret_cast<const u32x4*>(lds + L_QS + (c + 8 * j) * 4);
#pragma unroll
      for (int k = 0; k < 4; ++k) score(k);
    } else if (r == C0 + 1) {
#pragma unroll
      for (int k = 4; k < 7; ++k) score(k);
    } else if (r == C0 + 2) {
      score(7);
      mw = sc[0];
#pragma unroll
      for (int k = 1; k < 8; ++k) mw = fmaxf(mw, sc[k]);
      mw = xg_max(mw);
      lw = 0.f;
#pragma unroll
      for (int k = 0; k < 8; ++k) {
        const float p = ex2((sc[k] - mw) * LOG2E);
        lw += p;
        pb[k] = bfbits(p);
      }
      lw = xg_sum(lw);
      if (lane == 0) {
        lds[L_ML + 2 * w] = mw;
        lds[L_ML + 2 * w + 1] = lw;
      }
      if (early) {
        float z[8];
#pragma unroll
        for (int n = 0; n < 8; ++n) z[n] = 0.f;
#pragma unroll
        for (int r = 0; r < 4; ++r) {
          const unsigned chi = fbits(lds[L_H + 16 * w + 4 * r + r4]), clo = chi >> 16;
#pragma unroll
          for (int n = 0; n < 4; ++n) {
            z[2 * n] = dot2(W2[r][n], clo, z[2 * n]);
            z[2 * n + 1] = dot2(W2[r][n], chi, z[2 * n + 1]);
          }
        }
        const float2 zr = rows_reduce(z);
        if (c16 < 10) *reinterpret_cast<float2*>(lds + L_Z + 80 * w + 8 * c16 + 2 * r4) = zr;
      }
      LDS_DRAIN();
      if (lane == 0) lds_add(lds, L_SY + 4);
      STAMP(4);
    } else if (r == C0 + 3) {
#pragma unroll
      for (int k = 0; k < t + 1 - PVL; ++k) pv(k);
    } else if (r > C0 + 3 && r < 15) {
      pv(t - PVL);
    }
    if constexpr (!early && ((PM >> (r - 1)) & 1)) {
      SCHED_FENCE();
      if (w == MW) {
      const int lr = lane ? lane : 15;
      static_assert(!((PM >> r) & 1), "three requests behind the fetch");
      asm volatile("s_waitcnt vmcnt(3)" : "+v"(lat)::"memory");
      constexpr int p = r - 1;
      const float m = wave_sum((float)(lat[1] == tag && lat[0] < UMISS + 64 ? lat[0] : UMISS + 64)) * (1.f / 64) - 64.f;
      int d = 0;
      if (p <= 8) {
        kslow = m >= ATHR;
        if (kslow) d = (int)(m * (ASH / 4.f)) + AGX * (lr <= p ? 0 : lr < 8 ? lr - p : 8 - p);
      } else if (!vslow && m >= (p <= 10 ? (kslow ? UTA : UTHR) : UTH2)) {
        vslow = true;
        sh = fmaxf(m, 0.f) * ((p <= 10 ? USH : USH2) / 4.f);
        d = (int)sh + (lr > p ? UGX * (lr - p) : 0) + (lane ? 0 : VT);
      } else if (vslow && m < CT && sh < CSH) {
        d = (lr > p + 1 ? -UGX * (lr - p - 1) : 0) - (lane ? 0 : VT);
      }
      if (lane < 16) reinterpret_cast<unsigned*>(lds)[L_CK + lane] += d;
      }
    }
  });
  asm volatile("" ::"v"(tch));
  SCHED_FENCE();
  const __amdgpu_buffer_rsrc_t rwo = rsrc(wo + (size_t)h * DH * D, DH * D * 2);
  lds_wait(lds, L_SY + 4, 8u);
  float f, M, L;
  wg_ml(lds, M, L);
  f = ex2((mw - M) * LOG2E) / L;
  const unsigned cw = fbits(fmaf(M, LOG2E, __builtin_amdgcn_logf(L)));
  PIN(f);
  SCHED_FENCE();
  const int l40 = lane < 40 ? lane : 39;
  u64* rdst = gw + G2 + (size_t)rec * RS + 32 * r4 + 2 * c16;
  u64* zdst = gw + GZ + (size_t)(fr * 5 + (l40 >> 3)) * YB + 32 * gI + 16 * (gI >> 4) + 2 * (l40 & 7);
  unsigned pdst = lds_addr(lds + L_PART + w * DH + 8 * c + g), psrc = lds_addr(lds + L_PART + 48 * r4 + 4 * c16);
  unsigned cnt6 = lds_addr(lds + L_SY + 6), one = 1u;
  u32x4 rq = {cw, tag, cw, tag};
  u64 m48 = 0x0fff0fff0fff0fffull, m52 = 0x1fff1fff1fff1fffull;
  asm volatile("" : "+s"(m48), "+s"(m52), "+{v[156:159]}"(rq));
  PIN(rdst);
  PIN(zdst);
  PIN(pdst);
  PIN(psrc);
  PIN(cnt6);
  PIN(one);
#pragma unroll
  for (int k = 7 - PVL; k < 7; ++k) pv(k);
  auto wo1 = [&]() {
    if (WT_) pace<T_LAST + WT_, 0>(clk, ph);
    SCHED_FENCE();
#pragma unroll
    for (int i = 0; i < W1N; ++i) WO[i] = __builtin_amdgcn_raw_buffer_load_b128(rwo, wooff, i * (4 * D * 2), 2);
    SCHED_FENCE();
  };
  {
    unsigned plo = pb[7], phi = plo << 16;
    PIN(plo);
    PIN(phi);
    sfor<0, 3>([&](auto J) {
      constexpr int j = J.v;
      SCHED_FENCE();
      float a[8];
#pragma unroll
      for (int i = 0; i < 4; ++i) {
        a[2 * i] = dot2(V[7][j][i], plo, acc[8 * j + 2 * i]);
        a[2 * i + 1] = dot2(V[7][j][i], phi, acc[8 * j + 2 * i + 1]);
      }
      xg_store<256 * j>(a, f, pdst);
    });
    if (early) wo1();
  }
  STAMP(5);
  unsigned arr;
  asm volatile(
      "s_mov_b64 exec, 1\n\tds_add_rtn_u32 v112, %[cnt], %[one]\n\ts_mov_b64 exec, %[m48]\n\t" SUM8("%[src]")
      "v_readfirstlane_b32 %[arr], v112\n\t" SUM8B
      "s_cmp_eq_u32 %[arr], 7\n\t" SUM8C
      "v_cvt_pk_bf16_f32 v156, v116, v117\n\tv_cvt_pk_bf16_f32 v158, v118, v119\n\t"
      "s_cselect_b64 exec, %[m52], 0\n\tglobal_store_dwordx4 %[dst], v[156:159], off sc1\n\ts_mov_b64 exec, -1\n\ts_nop 0"
      : [arr] "=&s"(arr), "+{v[156:159]}"(rq)
      : [cnt] "v"(cnt6), [one] "v"(one), [m48] "s"(m48), [m52] "s"(m52), [src] "v"(psrc), [dst] "v"(rdst)
      : CLOB_T, "scc", "memory");
  if (arr == 0 && isF) {
    const float2* zp = reinterpret_cast<const float2*>(lds + L_Z) + l40;
    const float2 z0 = zp[0], z1 = zp[40], z2 = zp[80], z3 = zp[120];
    gst16(zdst, fbits((z0.x + z1.x) + (z2.x + z3.x)), fbits((z0.y + z1.y) + (z2.y + z3.y)), tag);
  }
  if constexpr (!early) {
    if (w == 7 && h < 5) {
      const int col = 80 * sp + 16 * h + 2 * c + (lane >> 5);
      const float xv = bf_lo(xs[col]);
      const u64* py = gw + GZ + (size_t)(sp * 5 + h) * YB + 32 * g + 2 * c + 392;
      bf16* po = out + col;
      float gt = 0.f;
      u64 m3 = 0ull, ms = 0ull;
      PIN(po);
#pragma unroll 1
      for (int pass = 0; pass < 2; ++pass) {
        lds_wait(lds, L_SY + (pass ? 1 : 6), pass ? 4u : 8u);
        STAMP(8);
        asm volatile("" : "+s"(m3), "+s"(ms));
        float t;
        unsigned n;
        asm volatile(
            "v_mov_b32 v124, 0\n\tv_mov_b32 v125, %[tag]\n\tv_mov_b32 v126, 0\n\tv_mov_b32 v127, %[tag]\n\t"
            "s_mov_b64 vcc, -1\n\ts_mov_b32 %[n], 0\n"
            ".Lfp%=:\n\t"
            "s_add_u32 %[n], %[n], 1\n\ts_cmp_gt_u32 %[n], %[max]\n\ts_cbranch_scc1 .Lfd%=\n\t"
            "s_mov_b64 exec, vcc\n\t"
            "global_load_dwordx4 v[112:115], %[p], off offset:-3136 sc1\n\t"
            "global_load_dwordx4 v[116:119], %[p], off offset:-1088 sc1\n\t"
            "global_load_dwordx4 v[120:123], %[p], off offset:1088 sc1\n\t"
            "s_and_b64 exec, vcc, %[m3]\n\t"
            "global_load_dwordx4 v[124:127], %[p], off offset:3136 sc1\n\t"
            "s_mov_b64 exec, -1\n\ts_waitcnt vmcnt(0)\n\t"
            "v_bitop3_b32 v128, v113, v115, %[tag] bitop3:0x7e\n\tv_bitop3_b32 v129, v117, v119, %[tag] bitop3:0x7e\n\t"
            "v_bitop3_b32 v130, v121, v123, %[tag] bitop3:0x7e\n\tv_bitop3_b32 v131, v125, v127, %[tag] bitop3:0x7e\n\t"
            "v_or3_b32 v128, v128, v129, v130\n\tv_add_f32 v132, v112, v116\n\tv_or_b32 v128, v128, v131\n\t"
            "v_add_f32 v134, v120, v124\n\tv_cmp_ne_u32 vcc, 0, v128\n\tv_add_f32 v133, v114, v118\n\t"
            "s_cbranch_vccnz .Lfp%=\n"
            ".Lfd%=:\n\t"
            "v_add_f32 v135, v122, v126\n\tv_add_f32 v132, v132, v134\n\tv_add_f32 v133, v133, v135\n\ts_nop 1\n\t"
            "v_permlane32_swap_b32 v132, v133\n\tv_add_f32 v134, v132, v133\n\tv_add_f32 v135, v132, v133\n\ts_nop 1\n\t"
            "v_permlane16_swap_b32 v134, v135\n\tv_add_f32 v134, v134, v135\n\ts_nop 1\n\t"
            "v_add_f32_dpp v134, v134, v134 row_ror:8 row_mask:0xf bank_mask:0xf\n\t"
            "v_cvt_pk_bf16_f32 v134, v134, v134\n\tv_lshlrev_b32 %[t], 16, v134\n\t"
            "v_mul_f32 v134, %[t], %[gt]\n\tv_cvt_pk_bf16_f32 v134, v134, v134\n\tv_lshlrev_b32 v134, 16, v134\n\t"
            "v_add_f32 v134, %[xv], v134\n\tv_cvt_pk_bf16_f32 v134, v134, v134\n\t"
            "s_mov_b64 exec, %[ms]\n\tglobal_store_short %[po], v134, off sc1\n\ts_mov_b64 exec, -1"
            : [t] "=&v"(t), [n] "=&s"(n)
            : [tag] "s"(tag), [max] "n"(SPIN_MAX), [p] "v"(py), [m3] "s"(m3), [ms] "s"(ms), [gt] "v"(gt), [xv] "v"(xv), [po] "v"(po)
            : CLOB_T, "vcc", "scc", "memory");
        if (pass) break;
        gt = bfr(silu(t));
        py -= GZ - GY;
        m3 = ~0ull;
        ms = 0x000000ff000000ffull;
      }
      if (b == 0 && lane == 0)
        __hip_atomic_store(reinterpret_cast<unsigned*>(gw + GEP), tag, __ATOMIC_RELAXED, __HIP_MEMORY_SCOPE_AGENT);
      STAMP(10);
    }
    STAMP_FLUSH();
    asm volatile(".Lcode_lastb:");
    return;
  }
  pace<T_LAST + W2T, 0>(clk, ph);
#pragma unroll
  for (int i = W1N; i < 6; ++i) WO[i] = __builtin_amdgcn_raw_buffer_load_b128(rwo, wooff, i * (4 * D * 2), 2);
  unsigned dm0 = __builtin_amdgcn_raw_buffer_load_b32(rwo, NOWHERE, 0, 2), dm1 = __builtin_amdgcn_raw_buffer_load_b32(rwo, NOWHERE, 4, 2);
  STAMP(3);
  SCHED_FENCE();
  const int ly = isF ? lane & 31 : lane;
  u64* ydst = gw + GY + (size_t)(4 * sp + (ly >> 3) + 4 * (ly >> 5)) * YB + 32 * h + 16 * (h >> 4) + 2 * (ly & 7);
  static_assert(RS * 8 * 4 == 4608, "record r4 + 4 is 4608 bytes behind record r4");
  const u64* pr = gw + G2 + (size_t)(h * NSP + r4 + 2) * RS + 32 * w + 2 * c16;
  const u64 ownm = 0xffffull << (16 * (sp & 3));
  u64 own0 = sp < 4 ? ownm : 0ull, own1 = sp < 4 ? 0ull : ownm, my = isF ? 0xffffffffull : ~0ull;
  u64 for0 = m52 & ~own0, for1 = m52 & ~own1;
  own0 &= m48;
  own1 &= m48;
  unsigned osrc = lds_addr(lds + L_PART + 48 * w + 4 * c16), ysrc = lds_addr(lds + L_Y + 2 * ly), cnt1 = lds_addr(lds + L_SY + 1);
  unsigned ydl = lds_addr(lds + L_Y + 128 * w + (isF ? 8 * c + g : 8 * c16 + 2 * r4));
  asm volatile("" : "+s"(own0), "+s"(own1), "+s"(for0), "+s"(for1), "+s"(my));
  PIN(ydst);
  PIN(pr);
  PIN(osrc);
  PIN(ysrc);
  PIN(cnt1);
  PIN(ydl);
#pragma unroll
  for (int i = XS; i < 6; ++i) X[i] = W2[i - XS];
  u32x8 WP[3], XP[3];
#pragma unroll
  for (int p = 0; p < 3; ++p)
#pragma unroll
    for (int k = 0; k < 4; ++k) {
      XP[p][2 * k] = __builtin_amdgcn_perm(X[2 * p + 1][k], X[2 * p][k], 0x05040100u);
      XP[p][2 * k + 1] = __builtin_amdgcn_perm(X[2 * p + 1][k], X[2 * p][k], 0x07060302u);
    }
  asm volatile("" : "+{v[64:71]}"(XP[0]), "+{v[72:79]}"(XP[1]), "+{v[80:87]}"(XP[2]));
  SCHED_FENCE();
  auto perm_wo = [&]() {
#pragma unroll
    for (int p = 0; p < 3; ++p)
#pragma unroll
      for (int k = 0; k < 4; ++k) {
        WP[p][2 * k] = __builtin_amdgcn_perm(WO[2 * p + 1][k], WO[2 * p][k], 0x05040100u);
        WP[p][2 * k + 1] = __builtin_amdgcn_perm(WO[2 * p + 1][k], WO[2 * p][k], 0x07060302u);
      }
    asm volatile("" : "+{v[88:95]}"(WP[0]), "+{v[96:103]}"(WP[1]), "+{v[104:111]}"(WP[2]));
    SCHED_FENCE();
    STAMP(1);
  };
  lds_wait(lds, L_SY + 6, 8u);
  if (PT_) pace<T_LAST + PT_, 0>(clk, 0);
  STAMP(4);
  u32x4 ra0, ra1;
  asm volatile(
      "v_mov_b32 v148, %[cw]\n\tv_mov_b32 v149, %[tag]\n\tv_mov_b32 v151, %[tag]\n\t"
      "v_mov_b32 v152, %[cw]\n\tv_mov_b32 v153, %[tag]\n\tv_mov_b32 v155, %[tag]\n\t"
      "s_mov_b64 exec, %[for0]\n\tglobal_load_dwordx4 v[148:151], %[p], off offset:-2304 sc1\n\t"
      "s_mov_b64 exec, %[for1]\n\tglobal_load_dwordx4 v[152:155], %[p], off offset:2304 sc1\n\t"
      "s_mov_b64 exec, %[m48]\n\t" SUM8("%[src]") SUM8B SUM8C
      "s_mov_b64 exec, %[own0]\n\tv_cvt_pk_bf16_f32 v148, v116, v117\n\tv_cvt_pk_bf16_f32 v150, v118, v119\n\t"
      "s_mov_b64 exec, %[own1]\n\tv_cvt_pk_bf16_f32 v152, v116, v117\n\tv_cvt_pk_bf16_f32 v154, v118, v119\n\t"
      "s_mov_b64 exec, -1"
      : "=&{v[148:151]}"(ra0), "=&{v[152:155]}"(ra1)
      : [cw] "v"(cw), [tag] "s"(tag), [for0] "s"(for0), [for1] "s"(for1), [p] "v"(pr), [own0] "s"(own0), [own1] "s"(own1),
        [m48] "s"(m48), [src] "v"(osrc)
      : CLOB_T, "memory");
  SCHED_FENCE();
  perm_wo();
  unsigned np;
  asm volatile(
      "s_mov_b32 %[n], 0\n\t"
      "s_mov_b64 exec, -1\n\tv_mov_b64 v[160:161], 0\n\tv_mov_b64 v[162:163], 0\n\tv_mov_b64 v[164:165], 0\n\t"
      "v_mov_b64 v[166:167], 0\n\ts_branch .Lrc%=\n"
      ".Lrp%=:\n\t"
      "s_add_u32 %[n], %[n], 1\n\ts_cmp_gt_u32 %[n], %[max]\n\ts_cbranch_scc1 .Lrd%=\n\t"
      "v_cmp_ne_u32 vcc, 0, v112\n\tv_cmp_ne_u32 s[98:99], 0, v113\n\ts_sleep " STR(RSLEEP) "\n\t"
      "s_mov_b64 exec, vcc\n\tglobal_load_dwordx4 v[148:151], %[p], off offset:-2304 sc1\n\t"
      "s_mov_b64 exec, s[98:99]\n\tglobal_load_dwordx4 v[152:155], %[p], off offset:2304 sc1\n\t"
      "s_mov_b64 exec, -1\n"
      ".Lrc%=:\n\t"
      "s_waitcnt vmcnt(0)\n\t"
      "v_bitop3_b32 v112, v149, v151, %[tag] bitop3:0x7e\n\tv_bitop3_b32 v113, v153, v155, %[tag] bitop3:0x7e\n\t"
      "v_max_f32 v115, v148, v152\n\tv_or_b32 v114, v112, v113\n\tv_max_f32 v116, v148, v152\n\t"
      "v_cmp_ne_u32 vcc, 0, v114\n\tv_lshlrev_b32 v122, 16, v148\n\ts_cbranch_vccnz .Lrp%=\n"
      ".Lrd%=:\n\t"
      "v_permlane16_swap_b32 v115, v116\n\tv_max_f32 v117, v115, v116\n\tv_max_f32 v118, v115, v116\n\t"
      "v_and_b32 v123, 0xffff0000, v148\n\tv_lshlrev_b32 v124, 16, v150\n\t"
      "v_permlane32_swap_b32 v117, v118\n\tv_max_f32 v117, v117, v118\n\t"
      "v_sub_f32 v115, v148, v117\n\tv_sub_f32 v116, v152, v117\n\tv_exp_f32 v115, v115\n\tv_exp_f32 v116, v116\n\t"
      "v_and_b32 v125, 0xffff0000, v150\n\tv_lshlrev_b32 v126, 16, v152\n\t"
      "v_add_f32 v117, v115, v116\n\tv_add_f32 v118, v115, v116\n\t"
      "v_mul_f32_dpp v122, v115, v122" BC12 "v_mul_f32_dpp v123, v115, v123" BC12
      "v_permlane16_swap_b32 v117, v118\n\tv_add_f32 v119, v117, v118\n\tv_add_f32 v120, v117, v118\n\t"
      "v_mul_f32_dpp v124, v115, v124" BC12 "v_mul_f32_dpp v125, v115, v125" BC12
      "v_permlane32_swap_b32 v119, v120\n\tv_add_f32 v119, v119, v120\n\tv_lshlrev_b32 v128, 16, v154\n\t"
      "v_rcp_f32 v119, v119\n\tv_and_b32 v127, 0xffff0000, v152\n\tv_and_b32 v129, 0xffff0000, v154\n\t"
      "v_fmac_f32_dpp v122, v116, v126" BC12 "v_fmac_f32_dpp v124, v116, v128" BC12
      "v_fmac_f32_dpp v123, v116, v127" BC12 "v_fmac_f32_dpp v125, v116, v129" BC12
      "v_permlane32_swap_b32 v122, v124\n\tv_add_f32 v122, v122, v124\n\t"
      "v_permlane32_swap_b32 v123, v125\n\tv_add_f32 v123, v123, v125\n\t"
      "v_mov_b32_dpp v120, v119" BC12 "s_nop 0\n\t"
      "v_permlane16_swap_b32 v122, v123\n\tv_add_f32 v122, v122, v123\n\t"
      "v_mul_f32 v123, v122, v120\n\ts_cmp_lt_u32 %[sp], 6\n\t"
      "v_mul_f32_dpp v124, v122, v120 quad_perm:[1,0,3,2] row_mask:0xf bank_mask:0xf\n\t"
      "v_cvt_pk_bf16_f32 v124, v123, v124\n\t"
      "s_cbranch_scc1 .Lyf%=\n\ts_nop 0\n\t"
      DOT8(64, 65, 66, 67, 68, 69, 70, 71, 0) DOT8(72, 73, 74, 75, 76, 77, 78, 79, 2) DOT8(80, 81, 82, 83, 84, 85, 86, 87, 4)
      DOT8(88, 89, 90, 91, 92, 93, 94, 95, 6) DOT8(96, 97, 98, 99, 100, 101, 102, 103, 8)
      DOT8(104, 105, 106, 107, 108, 109, 110, 111, 10) YRED
      "v_pk_add_f32 v[160:161], v[160:161], v[162:163]\n\tds_write_b64 %[ydl], v[160:161]\n"
      ".Lys%=:\n\t"
      "s_mov_b64 exec, 1\n\tds_add_rtn_u32 v112, %[cnt], %[one]\n\ts_mov_b64 exec, -1\n\t"
      "ds_read2st64_b64 v[116:119], %[ysrc] offset0:0 offset1:1\n\tds_read2st64_b64 v[120:123], %[ysrc] offset0:2 offset1:3\n\t"
      "s_waitcnt lgkmcnt(2)\n\tv_readfirstlane_b32 s98, v112\n\ts_waitcnt lgkmcnt(1)\n\t"
      "v_pk_add_f32 v[116:117], v[116:117], v[118:119]\n\ts_cmp_eq_u32 s98, 3\n\ts_cselect_b64 exec, %[my], 0\n\t"
      "s_waitcnt lgkmcnt(0)\n\tv_pk_add_f32 v[120:121], v[120:121], v[122:123]\n\ts_nop 0\n\t"
      "v_add_f32 v156, v116, v120\n\tv_add_f32 v158, v117, v121\n\t"
      "global_store_dwordx4 %[dst], v[156:159], off sc1\n\ts_mov_b64 exec, -1\n\ts_branch .Lye%=\n"
      ".Lyf%=:\n\ts_nop 0\n\t"
      "v_mov_b32_dpp v125, v124 row_newbcast:0 row_mask:0xf bank_mask:0x3\n\t"
      "v_mov_b32_dpp v126, v124 row_newbcast:2 row_mask:0xf bank_mask:0x3\n\t"
      "v_mov_b32_dpp v127, v124 row_newbcast:4 row_mask:0xf bank_mask:0x3\n\t"
      "v_mov_b32_dpp v125, v124 row_newbcast:6 row_mask:0xf bank_mask:0xc\n\t"
      "v_mov_b32_dpp v126, v124 row_newbcast:8 row_mask:0xf bank_mask:0xc\n\t"
      "v_mov_b32_dpp v127, v124 row_newbcast:10 row_mask:0xf bank_mask:0xc\n\t"
      DOT8F(88, 89, 90, 91, 92, 93, 94, 95, 125) DOT8F(96, 97, 98, 99, 100, 101, 102, 103, 126)
      DOT8F(104, 105, 106, 107, 108, 109, 110, 111, 127) YRED
      "v_add_f32 v160, v160, v162\n\tv_add_f32 v161, v161, v163\n\ts_nop 0\n\t"
      "v_add_f32_dpp v160, v160, v160 row_ror:8 row_mask:0xf bank_mask:0x3\n\t"
      "v_add_f32_dpp v160, v161, v161 row_ror:8 row_mask:0xf bank_mask:0xc\n\t"
      "ds_write_b32 %[ydl], v160\n\ts_branch .Lys%=\n"
      ".Lye%=:"
      : [n] "=&s"(np), "+{v[156:159]}"(rq), "+{v[148:151]}"(ra0), "+{v[152:155]}"(ra1)
      : [tag] "s"(tag), [p] "v"(pr), [max] "n"(SPIN_MAX), [sp] "s"(sp), [ydl] "v"(ydl), [cnt] "v"(cnt1), [one] "v"(one),
        [ysrc] "v"(ysrc), [my] "s"(my), [dst] "v"(ydst), "{v[64:71]}"(XP[0]), "{v[72:79]}"(XP[1]), "{v[80:87]}"(XP[2]),
        "{v[88:95]}"(WP[0]), "{v[96:103]}"(WP[1]), "{v[104:111]}"(WP[2])
      : CLOB_T, "v160", "v161", "v162", "v163", "v164", "v165", "v166", "v167", "s98", "s99", "vcc", "scc", "memory");
  STAMP(9);
  asm volatile("" ::"v"(dm0), "v"(dm1));
  STAMP_FLUSH();
  asm volatile(".Lcode_last:");
}

__global__ void __launch_bounds__(NT, 2)
fused_decode(const bf16* __restrict__ wq, const bf16* __restrict__ kc, const bf16* __restrict__ w1, const bf16* __restrict__ w3,
             u64* __restrict__ gw, const bf16* __restrict__ x, const bf16* __restrict__ vc,
             const bf16* __restrict__ wo, const bf16* __restrict__ w2, bf16* __restrict__ out) {
  __shared__ __attribute__((aligned(16))) float lds[L_END];
  const int b = blockIdx.x;
  const int xcd = b & 7, jj = b >> 3;
  const int h = xcd * 4 + (jj >> 3), sp = jj & 7;
  const int w = __builtin_amdgcn_readfirstlane(threadIdx.x >> 6);
  unsigned cpf = threadIdx.x;
  {
    u64 pc;
    unsigned len;
    asm volatile(
        "s_cmp_lt_u32 %[w], 4\n\ts_cbranch_scc1 .Lnocpf\n\t"
        "s_getpc_b64 %[pc]\n.Lcode_here:\n\tv_xor_b32 %[c], %[hi], %[c]\n\tv_lshlrev_b32 %[c], 6, %[c]\n\t"
        "s_mov_b32 %[len], .Lcode_last-.Lcode_here-4\n\ts_max_i32 %[len], %[len], .Lcode_lastb-.Lcode_here+508\n\t"
        "v_min_u32 %[c], %[len], %[c]\n\tglobal_load_dword %[c], %[c], %[pc]\n.Lnocpf:"
        : [c] "+v"(cpf), [pc] "=&s"(pc), [len] "=&s"(len) : [w] "s"(w), [hi] "s"((b & CPM) << 5) : "scc", "memory");
  }
  SCHED_FENCE();
  if (w >= 4)
    body<false>(lds, h, sp, w, x, kc, vc, wq, wo, w1, w3, w2, gw, out, cpf);
  else
    body<true>(lds, h, sp, w, x, kc, vc, wq, wo, w1, w3, w2, gw, out, cpf);
}

}

extern "C" void kernel_launch(void* const* d_in, const int* in_sizes, int n_in, void* d_out, int out_size, void* d_ws,
                              size_t ws_size, hipStream_t stream) {
  const bf16* x = (const bf16*)d_in[0];
  const bf16* kc = (const bf16*)d_in[1];
  const bf16* vc = (const bf16*)d_in[2];
  const bf16* wq = (const bf16*)d_in[3];
  const bf16* wo = (const bf16*)d_in[4];
  const bf16* w1 = (const bf16*)d_in[6];
  const bf16* w3 = (const bf16*)d_in[7];
  const bf16* w2 = (const bf16*)d_in[8];
  u64* gw = (u64*)d_ws;
  fused_decode<<<NWG, NT, 0, stream>>>(wq, kc, w1, w3, gw, x, vc, wo, w2, (bf16*)d_out);
}
```

```cpp
#include <hip/hip_runtime.h>
#include <hip/hip_bf16.h>
#include <stdint.h>

typedef __hip_bfloat16 bf16;
typedef unsigned long long u64;
typedef unsigned u32x4 __attribute__((ext_vector_type(4)));
typedef unsigned u32x8 __attribute__((ext_vector_type(8)));
typedef float f32x4 __attribute__((ext_vector_type(4)));
typedef float f32x2 __attribute__((ext_vector_type(2)));
typedef __bf16 bf16x2_t __attribute__((ext_vector_type(2)));

namespace {

constexpr int D = 640, HQ = 32, DH = 192, SK = 32768, WIN = 4096, FF = 1536;
constexpr int NSP = 8;
constexpr int NWG = HQ * NSP;
constexpr int NT = 512;
constexpr float SCALE = 0.07216878364870322f;
constexpr float LOG2E = 1.4426950408889634f;
constexpr unsigned SPIN_MAX = 1u << 16;
constexpr int SC1 = 16;
constexpr int NOWHERE = 1 << 30;

constexpr int PK_ = 82, PV_ = 81;
constexpr int TS_ = 358;
constexpr int SB_ = 246;
constexpr int LOC = 30, LOCO = 60;
constexpr int WT_ = 98, W2T = 132;
constexpr int PT_ = 215;
constexpr int NFL = 7;
constexpr int CE = 7, CL = 8;
constexpr int AR = 3;
constexpr int GR = 5, GRW = 2;
constexpr int MW = 4;
constexpr int PM = 0x1440;
constexpr int ULIM = 20, UMISS = 60;
constexpr int ATHR = 20, AGX = 7;
constexpr int UTHR = 8, UTA = 5, UTH2 = 40, UGX = 9, VT = 10;
constexpr int CT = 3, CSH = 30;
constexpr int LSP = 16;
constexpr int t_round(int r) { return r <= 2 ? TS_ : t_round(r - 1) + (r > 8 ? PV_ : PK_); }
constexpr int T_LAST = t_round(15);

constexpr int QS = 144;
constexpr int YB = 2 * (16 * 32 + 16);
constexpr int RS = 144;
constexpr size_t GQ = 0;
constexpr size_t G2 = GQ + (size_t)NWG * QS;
constexpr size_t GY = G2 + (size_t)NWG * RS;
constexpr size_t GZ = GY + 16;
constexpr size_t GEP = GY + (size_t)NSP * 5 * YB;
constexpr size_t GL = GEP + 16;

__device__ __forceinline__ unsigned fbits(float x) { return __builtin_bit_cast(unsigned, x); }
__device__ __forceinline__ u64 gld(const u64* p) {
  return __hip_atomic_load(p, __ATOMIC_RELAXED, __HIP_MEMORY_SCOPE_AGENT);
}
__device__ __forceinline__ float bf_lo(unsigned u) { return __builtin_bit_cast(float, u << 16); }
__device__ __forceinline__ float bf_hi(unsigned u) { return __builtin_bit_cast(float, u & 0xffff0000u); }
__device__ __forceinline__ float bfr(float x) { return __bfloat162float(__float2bfloat16(x)); }
__device__ __forceinline__ unsigned short bfbits(float x) {
  return __builtin_bit_cast(unsigned short, __float2bfloat16(x));
}
__device__ __forceinline__ float dot2(unsigned a, unsigned b, float c) {
  return __builtin_amdgcn_fdot2_f32_bf16(__builtin_bit_cast(bf16x2_t, a), __builtin_bit_cast(bf16x2_t, b), c, false);
}
template <int CTRL>
__device__ __forceinline__ float dpp(float x) {
  return __builtin_bit_cast(float, __builtin_amdgcn_mov_dpp(__builtin_bit_cast(int, x), CTRL, 0xf, 0xf, true));
}
__device__ __forceinline__ float grp8_sum(float x) {
  x += dpp<0xB1>(x);
  x += dpp<0x4E>(x);
  x += dpp<0x141>(x);
  return x;
}
__device__ __forceinline__ float sw16(float a, float b) {
  auto s = __builtin_amdgcn_permlane16_swap(fbits(a), fbits(b), false, false);
  return __builtin_bit_cast(float, (unsigned)s[0]) + __builtin_bit_cast(float, (unsigned)s[1]);
}
__device__ __forceinline__ float sw32(float a, float b) {
  auto s = __builtin_amdgcn_permlane32_swap(fbits(a), fbits(b), false, false);
  return __builtin_bit_cast(float, (unsigned)s[0]) + __builtin_bit_cast(float, (unsigned)s[1]);
}
__device__ __forceinline__ float xg_sum(float x) {
  x += dpp<0x128>(x);
  x = sw16(x, x);
  return sw32(x, x);
}
__device__ __forceinline__ float xg_max(float x) {
  x = fmaxf(x, dpp<0x128>(x));
  auto s = __builtin_amdgcn_permlane16_swap(fbits(x), fbits(x), false, false);
  x = fmaxf(__builtin_bit_cast(float, (unsigned)s[0]), __builtin_bit_cast(float, (unsigned)s[1]));
  auto t = __builtin_amdgcn_permlane32_swap(fbits(x), fbits(x), false, false);
  return fmaxf(__builtin_bit_cast(float, (unsigned)t[0]), __builtin_bit_cast(float, (unsigned)t[1]));
}
__device__ __forceinline__ float wave_sum(float x) { return xg_sum(grp8_sum(x)); }
__device__ __forceinline__ float ex2(float x) { return __builtin_amdgcn_exp2f(x); }
__device__ __forceinline__ float silu(float x) { return x / (1.0f + __expf(-x)); }
__device__ __forceinline__ float xg_reduce8(const float (&a)[8], bool g0) {
  float r[4];
#pragma unroll
  for (int i = 0; i < 4; ++i) r[i] = sw32(a[i], a[i + 4]);
  const float s0 = sw16(r[0], r[2]), s1 = sw16(r[1], r[3]);
  const float keep = g0 ? s1 : s0, send = g0 ? s0 : s1;
  return keep + dpp<0x128>(send);
}
__device__ __forceinline__ __amdgpu_buffer_rsrc_t rsrc(const void* p, unsigned bytes) {
  return __builtin_amdgcn_make_buffer_rsrc(const_cast<void*>(p), 0, bytes, 0x00020000);
}
__device__ __forceinline__ void gst16(u64* p, unsigned v0, unsigned v1, unsigned tag) {
  const u32x4 o = {v0, tag, v1, tag};
  asm volatile("global_store_dwordx4 %0, %1, off sc1\n\ts_nop 1" ::"v"(p), "v"(o) : "memory");
}
__device__ __forceinline__ unsigned bfpack(float a, float b) {
  return __builtin_bit_cast(unsigned, __builtin_convertvector(f32x2{a, b}, bf16x2_t));
}
__device__ __forceinline__ float2 rows_reduce(const float (&v)[8]) {
  float r[4];
#pragma unroll
  for (int i = 0; i < 4; ++i) r[i] = sw32(v[i], v[i + 4]);
  return float2{sw16(r[0], r[2]), sw16(r[1], r[3])};
}
template <int OFF>
__device__ __forceinline__ void xg_store(const float (&v)[8], float f, unsigned a) {
  float p[4], q[4], t, u;
#pragma unroll
  for (int i = 0; i < 4; ++i) {
    auto s = __builtin_amdgcn_permlane32_swap(fbits(v[i]), fbits(v[i + 4]), false, false);
    p[i] = __builtin_bit_cast(float, (unsigned)s[0]);
    q[i] = __builtin_bit_cast(float, (unsigned)s[1]);
  }
  const f32x2 r01 = f32x2{p[0], p[1]} + f32x2{q[0], q[1]}, r23 = f32x2{p[2], p[3]} + f32x2{q[2], q[3]};
  auto s0 = __builtin_amdgcn_permlane16_swap(fbits(r01[0]), fbits(r23[0]), false, false);
  auto s1 = __builtin_amdgcn_permlane16_swap(fbits(r01[1]), fbits(r23[1]), false, false);
  asm volatile("v_add_f32 %0, %2, %3\n\tv_add_f32 %1, %4, %5\n\ts_nop 0\n\t"
               "v_add_f32_dpp %0, %0, %0 row_ror:8 row_mask:0xf bank_mask:0x3\n\t"
               "v_add_f32_dpp %0, %1, %1 row_ror:8 row_mask:0xf bank_mask:0xc\n\t"
               "v_mul_f32 %0, %6, %0\n\tds_write_b32 %7, %0 offset:%8"
               : "=&v"(t), "=&v"(u)
               : "v"((unsigned)s0[0]), "v"((unsigned)s0[1]), "v"((unsigned)s1[0]), "v"((unsigned)s1[1]), "v"(f), "v"(a), "n"(OFF)
               : "memory");
}
#define SUM8(a)                                                                                                          \
  "ds_read_b128 v[116:119], " a "\n\tds_read_b128 v[120:123], " a " offset:768\n\t"                                      \
  "ds_read_b128 v[124:127], " a " offset:1536\n\tds_read_b128 v[128:131], " a " offset:2304\n\t"                         \
  "ds_read_b128 v[132:135], " a " offset:3072\n\tds_read_b128 v[136:139], " a " offset:3840\n\t"                         \
  "ds_read_b128 v[140:143], " a " offset:4608\n\tds_read_b128 v[144:147], " a " offset:5376\n\ts_waitcnt lgkmcnt(6)\n\t"
#define SUM8B                                                                                                            \
  "v_pk_add_f32 v[116:117], v[116:117], v[120:121]\n\tv_pk_add_f32 v[118:119], v[118:119], v[122:123]\n\t"               \
  "s_waitcnt lgkmcnt(4)\n\t"                                                                                             \
  "v_pk_add_f32 v[124:125], v[124:125], v[128:129]\n\tv_pk_add_f32 v[126:127], v[126:127], v[130:131]\n\t"               \
  "v_pk_add_f32 v[116:117], v[116:117], v[124:125]\n\tv_pk_add_f32 v[118:119], v[118:119], v[126:127]\n\t"
#define SUM8C                                                                                                            \
  "s_waitcnt lgkmcnt(2)\n\t"                                                                                             \
  "v_pk_add_f32 v[132:133], v[132:133], v[136:137]\n\tv_pk_add_f32 v[134:135], v[134:135], v[138:139]\n\t"               \
  "s_waitcnt lgkmcnt(0)\n\t"                                                                                             \
  "v_pk_add_f32 v[140:141], v[140:141], v[144:145]\n\tv_pk_add_f32 v[142:143], v[142:143], v[146:147]\n\t"               \
  "v_pk_add_f32 v[132:133], v[132:133], v[140:141]\n\tv_pk_add_f32 v[134:135], v[134:135], v[142:143]\n\t"               \
  "v_pk_add_f32 v[116:117], v[116:117], v[132:133]\n\tv_pk_add_f32 v[118:119], v[118:119], v[134:135]\n\t"
#define BC12 " row_newbcast:12 row_mask:0xf bank_mask:0xf\n\t"
#define DOTB(y, a, n) "v_dot2c_f32_bf16_dpp v" #y ", v124, v" #a " row_newbcast:" #n " row_mask:0xf bank_mask:0xf\n\t"
#define DOT8(a, b, c, d, e, f, g, h, n) \
  DOTB(160, a, n) DOTB(161, b, n) DOTB(162, c, n) DOTB(163, d, n) DOTB(164, e, n) DOTB(165, f, n) DOTB(166, g, n) DOTB(167, h, n)
#define DOTF(y, a, n) "v_dot2c_f32_bf16 v" #y ", v" #n ", v" #a "\n\t"
#define DOT8F(a, b, c, d, e, f, g, h, n) \
  DOTF(160, a, n) DOTF(161, b, n) DOTF(162, c, n) DOTF(163, d, n) DOTF(164, e, n) DOTF(165, f, n) DOTF(166, g, n) DOTF(167, h, n)
#define YRED                                                                                                             \
  "v_permlane32_swap_b32 v160, v164\n\tv_permlane32_swap_b32 v161, v165\n\tv_permlane32_swap_b32 v162, v166\n\t"         \
  "v_permlane32_swap_b32 v163, v167\n\tv_pk_add_f32 v[160:161], v[160:161], v[164:165]\n\t"                              \
  "v_pk_add_f32 v[162:163], v[162:163], v[166:167]\n\ts_nop 1\n\t"                                                       \
  "v_permlane16_swap_b32 v160, v162\n\tv_permlane16_swap_b32 v161, v163\n\t"
#define CLOB_T                                                                                                     \
  "v112", "v113", "v114", "v115", "v116", "v117", "v118", "v119", "v120", "v121", "v122", "v123", "v124", "v125",        \
      "v126", "v127", "v128", "v129", "v130", "v131", "v132", "v133", "v134", "v135", "v136", "v137", "v138", "v139",    \
      "v140", "v141", "v142", "v143", "v144", "v145", "v146", "v147"
#define SCHED_FENCE() __builtin_amdgcn_sched_barrier(0)
#define THRN(n) do { SCHED_FENCE(); asm volatile("s_waitcnt vmcnt(%0)" ::"n"(n) : "memory"); SCHED_FENCE(); } while (0)
#define NOWT() ((unsigned)__builtin_amdgcn_s_memrealtime())
template <int N>
struct IC {
  static constexpr int v = N;
};
template <int I, int E, class F>
__device__ __forceinline__ void sfor(F&& f) {
  if constexpr (I < E) {
    f(IC<I>{});
    sfor<I + 1, E>(f);
  }
}
#define LDS_DRAIN() asm volatile("s_waitcnt lgkmcnt(0)" ::: "memory")
__device__ __forceinline__ unsigned lds_add(float* lds, int idx) {
  return __hip_atomic_fetch_add(reinterpret_cast<unsigned*>(lds + idx), 1u, __ATOMIC_RELAXED, __HIP_MEMORY_SCOPE_WORKGROUP);
}
__device__ __forceinline__ void lds_wait(float* lds, int idx, unsigned n) {
  SCHED_FENCE();
#pragma unroll 1
  for (unsigned spin = 0; spin < (SPIN_MAX << 4); ++spin) {
    if (__hip_atomic_load(reinterpret_cast<unsigned*>(lds + idx), __ATOMIC_RELAXED, __HIP_MEMORY_SCOPE_WORKGROUP) >= n) break;
    __builtin_amdgcn_s_sleep(1);
  }
  asm volatile("" ::: "memory");
  SCHED_FENCE();
}
template <int T, int R>
__device__ __forceinline__ void pace(unsigned clk, int ph) {
  unsigned o;
  SCHED_FENCE();
  asm volatile(
      "s_branch .Lpb%=\n"
      ".Lpa%=:\n\ts_sleep 1\n"
      ".Lpb%=:\n\tds_read_b32 %0, %1 offset:%4\n\ts_memrealtime s[98:99]\n\ts_waitcnt lgkmcnt(0)\n\tv_readfirstlane_b32 s99, %0\n\t"
      "s_sub_u32 s98, s98, %2\n\ts_sub_u32 s98, s98, s99\n\ts_cmp_lt_i32 s98, %3\n\ts_cbranch_scc1 .Lpa%="
      : "=&v"(o) : "v"(clk), "s"(ph), "n"(T), "n"(4 * R) : "s98", "s99", "scc", "memory");
  SCHED_FENCE();
}

template <int T, int R>
__device__ __forceinline__ unsigned pace_m(unsigned clk, int ph) {
  unsigned o, tl;
  SCHED_FENCE();
  asm volatile(
      "s_mov_b32 %1, 0\n\ts_branch .Lpb%=\n"
      ".Lpa%=:\n\ts_sleep 1\n"
      ".Lpb%=:\n\tds_read_b32 %0, %2 offset:%5\n\ts_getreg_b32 s97, hwreg(HW_REG_IB_STS, 0, 4)\n\ts_memrealtime s[98:99]\n\t"
      "s_or_b32 s97, s97, %1\n\ts_waitcnt lgkmcnt(0)\n\ts_cselect_b32 %1, %1, s98\n\tv_readfirstlane_b32 s99, %0\n\t"
      "s_sub_u32 s98, s98, %3\n\ts_sub_u32 s98, s98, s99\n\ts_cmp_lt_i32 s98, %4\n\ts_cbranch_scc1 .Lpa%="
      : "=&v"(o), "=&s"(tl) : "v"(clk), "s"(ph), "n"(T), "n"(4 * R) : "s97", "s98", "s99", "scc", "memory");
  SCHED_FENCE();
  return tl;
}

constexpr int L_QP = 0;
constexpr int L_QS = L_QP + 4 * 128;
constexpr int L_PART = L_QS + 96;
constexpr int L_ML = L_PART + 8 * DH;
constexpr int L_Z = L_ML + 16;
constexpr int L_Y = L_Z + 320;
constexpr int L_H = L_Y + 512;
constexpr int L_SY = L_H + 64;
constexpr int L_CK = L_SY + 8;
constexpr int L_END = L_CK + 16;

#define PIN(x) asm volatile("" : "+v"(x))

__device__ __forceinline__ void wg_ml(const float* lds, float& M, float& L) {
  float mm[8], ll[8];
#pragma unroll
  for (int i = 0; i < 4; ++i) {
    const f32x4 t = *reinterpret_cast<const f32x4*>(lds + L_ML + 4 * i);
    mm[2 * i] = t[0];
    ll[2 * i] = t[1];
    mm[2 * i + 1] = t[2];
    ll[2 * i + 1] = t[3];
  }
  M = mm[0];
#pragma unroll
  for (int ww = 1; ww < 8; ++ww) M = fmaxf(M, mm[ww]);
  L = 0.f;
#pragma unroll
  for (int ww = 0; ww < 8; ++ww) L = fmaf(ex2((mm[ww] - M) * LOG2E), ll[ww], L);
}
__device__ __forceinline__ unsigned lds_addr(const float* p) {
  return (unsigned)(size_t)(const __attribute__((address_space(3))) float*)p;
}

template <bool early>
__device__ __forceinline__ void body(float* lds, const int h, const int sp, const int w, const bf16* __restrict__ x,
                                     const bf16* __restrict__ kc, const bf16* __restrict__ vc,
                                     const bf16* __restrict__ wq, const bf16* __restrict__ wo,
                                     const bf16* __restrict__ w1, const bf16* __restrict__ w3,
                                     const bf16* __restrict__ w2, u64* __restrict__ gw, bf16* __restrict__ out,
                                     unsigned cpf) {
  const int b = blockIdx.x;
  const int tid = threadIdx.x, lane = tid & 63;
  const int g = lane >> 3, c = lane & 7;
  const int rec = h * NSP + sp;

  const bool isF = sp < 6;
  u32x4 PR[8], xa, xb, xq;
  unsigned epoch = 0;
  u64 tk = 0;
  int ra = 0, RA0 = 0;
  bool va = false;
  if constexpr (early) {
    int s0_ = 240, s1_ = 80, s2_ = 240 * (sp & 1), s3_ = 80 * sp, s4_ = 64 * (sp >> 1), s5_ = 64 * w, s6_ = 24 * w;
    asm("" : "+s"(s0_), "+s"(s1_), "+s"(s2_), "+s"(s3_), "+s"(s4_), "+s"(s5_), "+s"(s6_));
    const int rw0 = isF ? s5_ : s6_;
    int lim = (isF ? s0_ : s1_) - rw0, lim2 = isF ? 64 : 24;
    asm("" : "+s"(lim), "+s"(lim2));
    lim = lim < lim2 ? lim : lim2;
    ra = rw0 + 8 * g;
    va = 8 * g < lim;
    RA0 = isF ? s2_ : s3_;
    int aoff = (int)__umul24(g, 8 * HQ * DH * 2) + 16 * c;
    PIN(aoff);
    aoff = va ? aoff : NOWHERE;
    int sa = ((RA0 + rw0) * (HQ * DH) + h * DH + (isF ? s4_ : 64)) * 2;
    PIN(aoff);
    asm volatile("" : "+s"(sa));
    SCHED_FENCE();
    const __amdgpu_buffer_rsrc_t rw = rsrc(wq, NOWHERE);
    PR[0] = __builtin_amdgcn_raw_buffer_load_b128(rw, aoff, sa, 2);
    SCHED_FENCE();
    tk = __builtin_amdgcn_s_memrealtime();
    SCHED_FENCE();
    asm volatile("s_load_dword %0, %1, %2" : "=&s"(epoch) : "s"(gw), "s"(w == 1 ? (int)GL * 8 + (sp * 8 + (b & 7)) * LSP : (int)GEP * 8) : "memory");
    SCHED_FENCE();
#pragma unroll
    for (int i = 1; i < 8; ++i) PR[i] = __builtin_amdgcn_raw_buffer_load_b128(rw, aoff, sa + i * (HQ * DH * 2), 2);
    SCHED_FENCE();
    SCHED_FENCE();
  }
  const unsigned short* xs = reinterpret_cast<const unsigned short*>(x);
  const size_t wgb = ((size_t)h * SK + (SK - WIN)) * DH;
  const __amdgpu_buffer_rsrc_t rk = rsrc(kc + wgb, WIN * DH * 2);
  const __amdgpu_buffer_rsrc_t rv = rsrc(vc + wgb, WIN * DH * 2);
  const int voff = (g * DH + 8 * c) * 2;
  auto tile = [&](int t) { return (t * 64 + h * 16 + w * 8 + sp) & 511; };
  u32x4 K[8][3], V[8][3];
  auto kld = [&](int t, int j) { K[t][j] = __builtin_amdgcn_raw_buffer_load_b128(rk, voff + 128 * j, tile(t) * 3072, 2); };
  auto vld = [&](int t, int j) { V[t][j] = __builtin_amdgcn_raw_buffer_load_b128(rv, voff + 128 * j, tile(t) * 3072, 2); };
  unsigned tag = 0;
  const unsigned clk = lds_addr(lds + L_CK);
  const int ph = (w * PV_) >> 3;
  const int r4 = lane >> 4, c16 = lane & 15;
  const int ch = c16 < 10 ? c16 : 9;
  const int fi = (h & 3) * 6 + (isF ? sp : 5), gl = fi >> 3, gI = (h >> 2) * 3 + gl, fr = fi & 7;
  u32x4 WO[6], X[6], W2[4];
  unsigned tch = 0;
  const int wooff = ((48 * w + r4 + (isF ? 24 * (g & 1) : 24)) * D + 64 * sp + 8 * c + (isF ? 0 : 128 * (g & 1))) * 2;
  typedef unsigned u32x2 __attribute__((ext_vector_type(2)));
  const __amdgpu_buffer_rsrc_t rl = rsrc(gw + GL, 16 * 64 * LSP);
  const bool pub = !((sp + (b >> 6)) & 3);
  const int lw_ = lane == 0 && pub ? (sp * 8 + (b & 7)) * LSP : NOWHERE;
  u32x2 lat = {0u, 0u};
  auto fetch = [&](int off) {
    SCHED_FENCE();
    asm volatile("buffer_load_dwordx2 %0, %1, %2, %3 offen sc1" : "+v"(lat) : "v"(lane * LSP), "s"(rl), "s"(off) : "memory");
    SCHED_FENCE();
  };
  u64 own = 0, pre = 0;
  unsigned pro = 0, st1 = 0, old = 0;
  bool vslow = false, kslow = false;
  float sh = 0.f;
  if constexpr (early) {
    u32x4 PF[5];
    const int F0 = (isF ? 160 * (fr >> 1) : 480) + 40 * w;
    const int fs = isF ? FF : HQ * DH;
    int s6_ = 64 * gI, s7_ = h * DH + 128 * (sp & 1);
    asm("" : "+s"(s6_), "+s"(s7_));
    const int boff = ((int)__umul24(g, fs) + 8 * c) * 2;
    const int sb = (F0 * fs + (isF ? s6_ : s7_)) * 2;
    const bf16* pf_ = (fr & 1) ? w3 : w1;
    asm("" : "+s"(pf_));
    const __amdgpu_buffer_rsrc_t rf = rsrc(isF ? pf_ : wq, NOWHERE);
    unsigned short xr[5];
#pragma unroll
    for (int i = 0; i < 5; ++i) PF[i] = __builtin_amdgcn_raw_buffer_load_b128(rf, boff, sb + i * 16 * fs, 2);
    SCHED_FENCE();
    if (!isF) {
#pragma unroll
      for (int i = 0; i < 6; ++i)
        X[i] = __builtin_amdgcn_raw_buffer_load_b128(rsrc(wo + (size_t)h * DH * D, DH * D * 2), wooff - 24 * D * 2 + i * (4 * D * 2), 0, 2);
    } else {
#pragma unroll
      for (int i = 0; i < 6; ++i) X[i] = u32x4{0u, 0u, 0u, 0u};
    }
    SCHED_FENCE();
    xa = *reinterpret_cast<const u32x4*>(x + 8 * lane);
    xb = *reinterpret_cast<const u32x4*>(x + 512 + 8 * (lane & 15));
    xq = *reinterpret_cast<const u32x4*>(x + RA0 + (va ? ra : 0));
#pragma unroll
    for (int i = 0; i < 5; ++i) xr[i] = xs[F0 + 8 * i + g];
    SCHED_FENCE();
    const unsigned epv = __hip_atomic_load(reinterpret_cast<unsigned*>(gw + GEP), __ATOMIC_RELAXED, __HIP_MEMORY_SCOPE_AGENT);
    SCHED_FENCE();
    asm volatile("" : "+s"(tk));
    if (tid < 16) reinterpret_cast<unsigned*>(lds)[L_CK + tid] = (unsigned)tk;
    asm volatile("s_waitcnt lgkmcnt(0)\n\ts_barrier" : "+s"(epoch)::"memory");
    SCHED_FENCE();
#pragma unroll
    for (int t = 0; t < 3; ++t)
#pragma unroll
      for (int j = 0; j < 3; ++j) kld(t, j);
    SCHED_FENCE();
    float mb, den;
    {
      float xf[8], xg[8];
#pragma unroll
      for (int i = 0; i < 4; ++i) {
        xf[2 * i] = bf_lo(xa[i]);
        xf[2 * i + 1] = bf_hi(xa[i]);
        xg[2 * i] = bf_lo(xb[i]);
        xg[2 * i + 1] = bf_hi(xb[i]);
      }
      const bool lo16 = lane < 16;
      float sum = 0.f;
#pragma unroll
      for (int i = 0; i < 8; ++i) sum += xf[i] + (lo16 ? xg[i] : 0.f);
      sum = wave_sum(sum);
      const float mean = sum * (1.0f / D);
      float sq = 0.f;
#pragma unroll
      for (int i = 0; i < 8; ++i) {
        const float d1 = xf[i] - mean, d2 = xg[i] - mean;
        sq += d1 * d1 + (lo16 ? d2 * d2 : 0.f);
      }
      sq = wave_sum(sq);
      mb = bfr(mean);
      const float vb = bfr(sq * (1.0f / D));
      den = bfr(sqrtf(bfr(vb + 1e-5f)));
    }
    {
      unsigned xn[13];
#pragma unroll
      for (int i = 0; i < 4; ++i) {
        xn[2 * i] = fbits(bfr(bfr(bf_lo(xq[i]) - mb) / den));
        xn[2 * i + 1] = fbits(bfr(bfr(bf_hi(xq[i]) - mb) / den));
      }
#pragma unroll
      for (int i = 0; i < 5; ++i) xn[8 + i] = fbits(bfr(bfr(bf_lo(xr[i]) - mb) / den));
#pragma unroll
      for (int i = 0; i < 13; ++i) PIN(xn[i]);
      SCHED_FENCE();
      pace<t_round(2), 2>(clk, ph);
#pragma unroll
      for (int j = 0; j < 3; ++j) kld(3, j);
      SCHED_FENCE();
      float fa[8], fg[8];
#pragma unroll
      for (int k = 0; k < 8; ++k) fa[k] = fg[k] = 0.f;
#pragma unroll
      for (int i = 0; i < 8; ++i)
#pragma unroll
        for (int k = 0; k < 4; ++k) {
          fa[2 * k] = dot2(PR[i][k], xn[i] >> 16, fa[2 * k]);
          fa[2 * k + 1] = dot2(PR[i][k], xn[i], fa[2 * k + 1]);
        }
      lds[L_QP + w * 128 + 8 * c + g] = xg_reduce8(fa, g & 1);
#pragma unroll
      for (int i = 0; i < 5; ++i)
#pragma unroll
        for (int k = 0; k < 4; ++k) {
          fg[2 * k] = dot2(PF[i][k], xn[8 + i] >> 16, fg[2 * k]);
          fg[2 * k + 1] = dot2(PF[i][k], xn[8 + i], fg[2 * k + 1]);
        }
      lds[L_QP + w * 128 + 64 + 8 * c + g] = xg_reduce8(fg, g & 1);
    }
    SCHED_FENCE();
    tag = __builtin_amdgcn_readfirstlane(epv) + 1u;
    if (tid == 0) reinterpret_cast<unsigned*>(lds)[L_SY + 3] = tag;
    LDS_DRAIN();
    if (lane == 0) old = lds_add(lds, L_SY);
    old = __builtin_amdgcn_readfirstlane(old);
    if (old == 3) {
      const float2* qp = reinterpret_cast<const float2*>(lds + L_QP) + lane;
      const float2 p0_ = qp[0], p1_ = qp[64], p2_ = qp[128], p3_ = qp[192];
      gst16(gw + GQ + (size_t)rec * QS + 2 * lane, fbits((p0_.x + p1_.x) + (p2_.x + p3_.x)),
            fbits((p0_.y + p1_.y) + (p2_.y + p3_.y)), tag);
    }
  } else {
    if (tid >= NT - 8) reinterpret_cast<unsigned*>(lds)[L_SY + tid - (NT - 8)] = 0u;
    unsigned st0 = 0;
    {
      const bf16* tp = w == 4 ? kc + wgb : w == 5 ? (!isF ? wo + (size_t)h * DH * D : (fr & 1) ? w3 : w1) : w == 6 ? ((sp & 1) ? x : wq) : vc + wgb;
      asm volatile("s_waitcnt lgkmcnt(0)\n\ts_load_dword %0, %1, 0x0" : "=&s"(st0) : "s"(tp) : "memory");
    }
    SCHED_FENCE();
#pragma unroll
    for (int j = 0; j < 3; ++j) kld(0, j);
    SCHED_FENCE();
    asm volatile("s_barrier" ::: "memory");
    SCHED_FENCE();
    if (w < 6) {
      const int l = lane < 42 ? lane : 41, k = l & 15;
      const int yg = 4 * (k < 8 || isF ? sp : sp + 2) + ((k >> 1) & 3);
      const size_t yo = l < 32 || !isF ? GY + (size_t)yg * YB + 32 * h + 16 * (h >> 4) + 8 * (k & 1)
                                       : GZ + (size_t)(fr * 5 + (k >> 1)) * YB + 32 * gI + 16 * (gI >> 4) + 8 * (k & 1);
      unsigned o4 = (unsigned)(l < 16 ? G2 + (size_t)rec * RS + 8 * l : yo);
      unsigned o5 = (unsigned)(GQ + (size_t)rec * QS + 8 * (lane < 16 ? lane : 15));
      if (pub && lane >= 16 && lane < 32 && ((PM >> (lane - 16)) & 1))
        o5 = (unsigned)GL + (unsigned)(((lane - 16) * 64 + sp * 8 + (b & 7)) * (LSP / 8));
      PIN(o4);
      PIN(o5);
      pre = gld(gw + (w == 4 ? o4 : o5));
    }
    if (w == 7 && h < 5)
      pro = __hip_atomic_load(reinterpret_cast<const unsigned*>(out + 80 * sp + 16 * h) + (lane & 7), __ATOMIC_RELAXED,
                              __HIP_MEMORY_SCOPE_AGENT);
    SCHED_FENCE();
    asm volatile("s_waitcnt lgkmcnt(0)" ::"s"(st0) : "memory");
#pragma unroll
    for (int j = 0; j < 3; ++j) {
      kld(1, j);
      THRN(5);
    }
    THRN(3);
    own = __builtin_amdgcn_s_memrealtime() - SB_;
    __builtin_amdgcn_raw_buffer_store_b64(u32x2{(unsigned)own, (unsigned)(own >> 32)}, rl, w == 6 ? lw_ : NOWHERE, 0, SC1);
    SCHED_FENCE();
    if (w == 6 && lane < 16) {
      const unsigned o = (unsigned)own - (isF ? LOC : LOCO);
      if ((int)(o - reinterpret_cast<unsigned*>(lds)[L_CK + lane]) > 0) reinterpret_cast<unsigned*>(lds)[L_CK + lane] = o;
    }
    SCHED_FENCE();
    {
      const bf16* tp = (w & 1) ? wo + (size_t)h * DH * D : w2 + (size_t)gI * 64 * D;
      asm volatile("s_load_dword %0, %1, 0x0" : "=&s"(st1) : "s"(tp) : "memory");
    }
    SCHED_FENCE();
  }
  u32x4 qf[3];
  float sc[8], mw, lw, acc[24];
  unsigned pb[8];
  auto score = [&](int t) {
    float d = 0.f;
#pragma unroll
    for (int j = 0; j < 3; ++j)
#pragma unroll
      for (int i = 0; i < 4; ++i) d = dot2(K[t][j][i], qf[j][i], d);
    d = grp8_sum(d);
    sc[t] = bfr(bfr(d) * SCALE);
  };
  auto pv = [&](int t) {
    const unsigned plo = pb[t], phi = plo << 16;
#pragma unroll
    for (int j = 0; j < 3; ++j)
#pragma unroll
      for (int i = 0; i < 4; ++i) {
        acc[8 * j + 2 * i] = dot2(V[t][j][i], plo, t ? acc[8 * j + 2 * i] : 0.f);
        acc[8 * j + 2 * i + 1] = dot2(V[t][j][i], phi, t ? acc[8 * j + 2 * i + 1] : 0.f);
        PIN(acc[8 * j + 2 * i]);
        PIN(acc[8 * j + 2 * i + 1]);
      }
  };
  const bool hf = lane >> 5;
  const __amdgpu_buffer_rsrc_t rq_ = rsrc(gw + GQ, NWG * QS * 8);
  int go[9];
  {
    const int l16 = (lane & 31) * 16;
    const int qa = l16 + (8 * h + (hf ? 4 : 0)) * (QS * 8);
    go[0] = qa;
    go[1] = qa + QS * 8;
    go[2] = l16 + (8 * h + (hf ? 7 : 6)) * (QS * 8) + 512;
    go[3] = qa + 2 * QS * 8;
    go[4] = qa + 3 * QS * 8;
#pragma unroll
    for (int k = 0; k < 4; ++k) {
      const int m = 8 * gl + 2 * k + (hf ? 1 : 0);
      go[5 + k] = isF ? l16 + (32 * (h >> 2) + m + (((m * 43) >> 7) & 6)) * (QS * 8) + 512 : NOWHERE;
    }
  }
  const bool gath = old == GRW;
  u32x4 pa[9];
  constexpr int C0 = early ? CE : CL;
  static_assert(C0 + 3 < 15 && C0 > GR + 1 && C0 >= 6, "schedule");
  sfor<early ? 3 : 2, 16>([&](auto R) {
    constexpr int r = R.v, t = r >= 8 ? r - 8 : r + early;
    unsigned tl = 0;
    if constexpr (!early && ((PM >> r) & 1)) tl = pace_m<t_round(r), r>(clk, ph); else pace<t_round(r), r>(clk, ph);
    if constexpr (!early && ((PM >> (r - 1)) & 1)) {
      if (w == MW) fetch(64 * LSP * (r - 1));
    }
    if constexpr (r < 8 && t == 8) {
      const __amdgpu_buffer_rsrc_t rx = isF ? rsrc(w2 + ((size_t)gI * 64 + 16 * w) * D + 80 * fr, 16 * D * 2) : rsrc(wo + (size_t)h * DH * D, DH * D * 2);
#pragma unroll
      for (int i = 0; i < 4; ++i)
        W2[i] = __builtin_amdgcn_raw_buffer_load_b128(rx, isF ? (r4 * D + 8 * ch) * 2 : NOWHERE, i * (4 * D * 2), 2);
      tch = __builtin_amdgcn_raw_buffer_load_b32(rsrc(wo + (size_t)h * DH * D, DH * D * 2), tid ? NOWHERE : wooff, 0, 2);
    } else {
#pragma unroll
      for (int j = 0; j < 3; ++j) {
        if (r >= 8) vld(t, j); else kld(t, j);
        if (r == 15 && j == 2) break;
        if (early && r == GR + 1) continue;
        THRN(early && r == 8 ? NFL + 2 : NFL);
      }
    }
    SCHED_FENCE();
    if constexpr (early) {
      if (r == GR && gath) {
#pragma unroll
        for (int s = 0; s < 9; ++s) pa[s] = __builtin_amdgcn_raw_buffer_load_b128(rq_, go[s], 0, SC1);
        SCHED_FENCE();
      }
      if (r == GR + 1) {
        if (gath) {
          const unsigned tf = isF ? tag : 0u;
          bool ok = true;
#pragma unroll
          for (int s = 0; s < 9; ++s) ok = ok & (pa[s][1] == (s < 5 ? tag : tf)) & (pa[s][3] == (s < 5 ? tag : tf));
          if (!__all(ok)) {
            for (unsigned spin = 0;; ++spin) {
              ok = true;
              asm volatile("" ::: "memory");
#pragma unroll
              for (int s = 0; s < 9; ++s) {
                pa[s] = __builtin_amdgcn_raw_buffer_load_b128(rq_, go[s], 0, SC1);
                ok = ok & (pa[s][1] == (s < 5 ? tag : tf)) & (pa[s][3] == (s < 5 ? tag : tf));
              }
              if (__all(ok) || spin >= SPIN_MAX) break;
              __builtin_amdgcn_s_sleep(1);
            }
          }
          float qo[2], qm[2], hu[2];
#pragma unroll
          for (int k = 0; k < 2; ++k) {
            auto val = [&](int s) { return __builtin_bit_cast(float, (unsigned)pa[s][2 * k]); };
            qo[k] = (val(0) + val(1)) + val(2);
            const float t_ = val(3) + val(4);
            qm[k] = sw32(t_, t_);
            const float u = bfr((val(5) + val(6)) + (val(7) + val(8)));
            auto sw = __builtin_amdgcn_permlane32_swap(fbits(u), fbits(u), false, false);
            hu[k] = bfr(bfr(silu(__builtin_bit_cast(float, (unsigned)sw[0]))) * __builtin_bit_cast(float, (unsigned)sw[1]));
          }
          reinterpret_cast<unsigned*>(lds + L_QS)[lane + (hf ? 32 : 0)] = bfpack(qo[0], qo[1]);
          if (hf) {
            reinterpret_cast<unsigned*>(lds + L_QS)[lane] = bfpack(qm[0], qm[1]);
          } else {
            *reinterpret_cast<float2*>(lds + L_H + 2 * lane) = float2{hu[0], hu[1]};
          }
          LDS_DRAIN();
          if (lane == 0) lds_add(lds, L_SY + 2);
        }
      }
    } else {
      if (r == 2) asm volatile("" ::"s"(st1), "v"(cpf), "v"(pre), "v"(pro));
      if (r == AR && w == 6) fetch(0);
      if (r == AR + 1 && w == 6) {
        asm volatile("s_waitcnt vmcnt(3)" : "+v"(lat)::"memory");
        const u64 d = (((u64)lat[1] << 32) | lat[0]) - own + 200u;
        const bool ok = d <= 400u;
        const float s_ = wave_sum(ok ? (float)(unsigned)d : 0.f), n = (float)__builtin_popcountll(__ballot(ok));
        const unsigned o = (unsigned)own + (n > 0.f ? (unsigned)(s_ * __builtin_amdgcn_rcpf(n)) - 200u : 0u);
        if (w == 6 && lane < 16) reinterpret_cast<unsigned*>(lds)[L_CK + lane] = o;
      }
      if (r == AR + 2) {
        lds_wait(lds, L_SY, 4u);
        tag = __builtin_amdgcn_readfirstlane(reinterpret_cast<const unsigned*>(lds)[L_SY + 3]);
      }
      static_assert(!(PM & ((1 << (AR + 2)) - 1)), "tag");
      if ((PM >> r) & 1) {
        const unsigned org = __builtin_amdgcn_readfirstlane(reinterpret_cast<const unsigned*>(lds)[L_CK + r]);
        if (w == MW && pub && !tl) THRN(3);
        const unsigned now = NOWT() - 9;
        const int l = (int)((tl ? tl : now) - org) - (t_round(r) + ph);
        __builtin_amdgcn_raw_buffer_store_b64(u32x2{(unsigned)(l < -64 ? 0 : l + 64), tag}, rl, w == MW && l < ULIM ? lw_ : NOWHERE,
                                              64 * LSP * r, SC1);
      }
      SCHED_FENCE();
    }
    if (r == C0) {
      lds_wait(lds, L_SY + 2, 1u);
#pragma unroll
      for (int j = 0; j < 3; ++j) qf[j] = *reinterpret_cast<const u32x4*>(lds + L_QS + (c + 8 * j) * 4);
#pragma unroll
      for (int k = 0; k < 4; ++k) score(k);
    } else if (r == C0 + 1) {
#pragma unroll
      for (int k = 4; k < 7; ++k) score(k);
    } else if (r == C0 + 2) {
      score(7);
      mw = sc[0];
#pragma unroll
      for (int k = 1; k < 8; ++k) mw = fmaxf(mw, sc[k]);
      mw = xg_max(mw);
      lw = 0.f;
#pragma unroll
      for (int k = 0; k < 8; ++k) {
        const float p = ex2((sc[k] - mw) * LOG2E);
        lw += p;
        pb[k] = bfbits(p);
      }
      lw = xg_sum(lw);
      if (lane == 0) {
        lds[L_ML + 2 * w] = mw;
        lds[L_ML + 2 * w + 1] = lw;
      }
      if (early) {
        float z[8];
#pragma unroll
        for (int n = 0; n < 8; ++n) z[n] = 0.f;
#pragma unroll
        for (int r = 0; r < 4; ++r) {
          const unsigned chi = fbits(lds[L_H + 16 * w + 4 * r + r4]), clo = chi >> 16;
#pragma unroll
          for (int n = 0; n < 4; ++n) {
            z[2 * n] = dot2(W2[r][n], clo, z[2 * n]);
            z[2 * n + 1] = dot2(W2[r][n], chi, z[2 * n + 1]);
          }
        }
        const float2 zr = rows_reduce(z);
        if (c16 < 10) *reinterpret_cast<float2*>(lds + L_Z + 80 * w + 8 * c16 + 2 * r4) = zr;
      }
      LDS_DRAIN();
      if (lane == 0) lds_add(lds, L_SY + 4);
    } else if (r == C0 + 3) {
#pragma unroll
      for (int k = 0; k < t; ++k) pv(k);
    } else if (r > C0 + 3 && r < 15) {
      pv(t - 1);
    }
    if constexpr (!early && ((PM >> (r - 1)) & 1)) {
      SCHED_FENCE();
      if (w == MW) {
      const int lr = lane ? lane : 15;
      static_assert(!((PM >> r) & 1), "three requests behind the fetch");
      asm volatile("s_waitcnt vmcnt(3)" : "+v"(lat)::"memory");
      constexpr int p = r - 1;
      const float m = wave_sum((float)(lat[1] == tag && lat[0] < UMISS + 64 ? lat[0] : UMISS + 64)) * (1.f / 64) - 64.f;
      int d = 0;
      if (p <= 8) {
        kslow = m >= ATHR;
        if (kslow) d = (int)m + AGX * (lr <= p ? 0 : lr < 8 ? lr - p : 8 - p);
      } else if (!vslow && m >= (p <= 10 ? (kslow ? UTA : UTHR) : UTH2)) {
        vslow = true;
        sh = fmaxf(m, 0.f) * (p <= 10 ? 1.f : 0.75f);
        d = (int)sh + (lr > p ? UGX * (lr - p) : 0) + (lane ? 0 : VT);
      } else if (vslow && m < CT && sh < CSH) {
        d = (lr > p + 1 ? -UGX * (lr - p - 1) : 0) - (lane ? 0 : VT);
      }
      if (lane < 16) reinterpret_cast<unsigned*>(lds)[L_CK + lane] += d;
      }
    }
  });
  asm volatile("" ::"v"(tch));
  SCHED_FENCE();
  const __amdgpu_buffer_rsrc_t rwo = rsrc(wo + (size_t)h * DH * D, DH * D * 2);
  lds_wait(lds, L_SY + 4, 8u);
  float f, M, L;
  wg_ml(lds, M, L);
  f = ex2((mw - M) * LOG2E) / L;
  const unsigned cw = fbits(fmaf(M, LOG2E, __builtin_amdgcn_logf(L)));
  PIN(f);
  SCHED_FENCE();
  const int l40 = lane < 40 ? lane : 39;
  u64* rdst = gw + G2 + (size_t)rec * RS + 32 * r4 + 2 * c16;
  u64* zdst = gw + GZ + (size_t)(fr * 5 + (l40 >> 3)) * YB + 32 * gI + 16 * (gI >> 4) + 2 * (l40 & 7);
  unsigned pdst = lds_addr(lds + L_PART + w * DH + 8 * c + g), psrc = lds_addr(lds + L_PART + 48 * r4 + 4 * c16);
  unsigned cnt6 = lds_addr(lds + L_SY + 6), one = 1u;
  u32x4 rq = {cw, tag, cw, tag};
  u64 m48 = 0x0fff0fff0fff0fffull, m52 = 0x1fff1fff1fff1fffull;
  asm volatile("" : "+s"(m48), "+s"(m52), "+{v[156:159]}"(rq));
  PIN(rdst);
  PIN(zdst);
  PIN(pdst);
  PIN(psrc);
  PIN(cnt6);
  PIN(one);
  pv(6);
  auto wo1 = [&]() {
    pace<T_LAST + WT_, 0>(clk, ph);
    SCHED_FENCE();
#pragma unroll
    for (int i = 0; i < 3; ++i) WO[i] = __builtin_amdgcn_raw_buffer_load_b128(rwo, wooff, i * (4 * D * 2), 2);
    SCHED_FENCE();
  };
  {
    unsigned plo = pb[7], phi = plo << 16;
    PIN(plo);
    PIN(phi);
    sfor<0, 3>([&](auto J) {
      constexpr int j = J.v;
      SCHED_FENCE();
      float a[8];
#pragma unroll
      for (int i = 0; i < 4; ++i) {
        a[2 * i] = dot2(V[7][j][i], plo, acc[8 * j + 2 * i]);
        a[2 * i + 1] = dot2(V[7][j][i], phi, acc[8 * j + 2 * i + 1]);
      }
      xg_store<256 * j>(a, f, pdst);
    });
    if (early) wo1();
  }
  unsigned arr;
  asm volatile(
      "s_mov_b64 exec, 1\n\tds_add_rtn_u32 v112, %[cnt], %[one]\n\ts_mov_b64 exec, %[m48]\n\t" SUM8("%[src]")
      "v_readfirstlane_b32 %[arr], v112\n\t" SUM8B
      "s_cmp_eq_u32 %[arr], 7\n\t" SUM8C
      "v_cvt_pk_bf16_f32 v156, v116, v117\n\tv_cvt_pk_bf16_f32 v158, v118, v119\n\t"
      "s_cselect_b64 exec, %[m52], 0\n\tglobal_store_dwordx4 %[dst], v[156:159], off sc1\n\ts_mov_b64 exec, -1\n\ts_nop 0"
      : [arr] "=&s"(arr), "+{v[156:159]}"(rq)
      : [cnt] "v"(cnt6), [one] "v"(one), [m48] "s"(m48), [m52] "s"(m52), [src] "v"(psrc), [dst] "v"(rdst)
      : CLOB_T, "scc", "memory");
  if (arr == 0 && isF) {
    const float2* zp = reinterpret_cast<const float2*>(lds + L_Z) + l40;
    const float2 z0 = zp[0], z1 = zp[40], z2 = zp[80], z3 = zp[120];
    gst16(zdst, fbits((z0.x + z1.x) + (z2.x + z3.x)), fbits((z0.y + z1.y) + (z2.y + z3.y)), tag);
  }
  if constexpr (!early) {
    if (w == 7 && h < 5) {
      const int col = 80 * sp + 16 * h + 2 * c + (lane >> 5);
      const float xv = bf_lo(xs[col]);
      const u64* py = gw + GZ + (size_t)(sp * 5 + h) * YB + 32 * g + 2 * c + 392;
      bf16* po = out + col;
      float gt = 0.f;
      u64 m3 = 0ull, ms = 0ull;
      PIN(po);
#pragma unroll 1
      for (int pass = 0; pass < 2; ++pass) {
        lds_wait(lds, L_SY + (pass ? 1 : 6), pass ? 4u : 8u);
        asm volatile("" : "+s"(m3), "+s"(ms));
        float t;
        unsigned n;
        asm volatile(
            "v_mov_b32 v124, 0\n\tv_mov_b32 v125, %[tag]\n\tv_mov_b32 v126, 0\n\tv_mov_b32 v127, %[tag]\n\t"
            "s_mov_b64 vcc, -1\n\ts_mov_b32 %[n], 0\n"
            ".Lfp%=:\n\t"
            "s_add_u32 %[n], %[n], 1\n\ts_cmp_gt_u32 %[n], %[max]\n\ts_cbranch_scc1 .Lfd%=\n\t"
            "s_mov_b64 exec, vcc\n\t"
            "global_load_dwordx4 v[112:115], %[p], off offset:-3136 sc1\n\t"
            "global_load_dwordx4 v[116:119], %[p], off offset:-1088 sc1\n\t"
            "global_load_dwordx4 v[120:123], %[p], off offset:1088 sc1\n\t"
            "s_and_b64 exec, vcc, %[m3]\n\t"
            "global_load_dwordx4 v[124:127], %[p], off offset:3136 sc1\n\t"
            "s_mov_b64 exec, -1\n\ts_waitcnt vmcnt(0)\n\t"
            "v_bitop3_b32 v128, v113, v115, %[tag] bitop3:0x7e\n\tv_bitop3_b32 v129, v117, v119, %[tag] bitop3:0x7e\n\t"
            "v_bitop3_b32 v130, v121, v123, %[tag] bitop3:0x7e\n\tv_bitop3_b32 v131, v125, v127, %[tag] bitop3:0x7e\n\t"
            "v_or3_b32 v128, v128, v129, v130\n\tv_add_f32 v132, v112, v116\n\tv_or_b32 v128, v128, v131\n\t"
            "v_add_f32 v134, v120, v124\n\tv_cmp_ne_u32 vcc, 0, v128\n\tv_add_f32 v133, v114, v118\n\t"
            "s_cbranch_vccnz .Lfp%=\n"
            ".Lfd%=:\n\t"
            "v_add_f32 v135, v122, v126\n\tv_add_f32 v132, v132, v134\n\tv_add_f32 v133, v133, v135\n\ts_nop 1\n\t"
            "v_permlane32_swap_b32 v132, v133\n\tv_add_f32 v134, v132, v133\n\tv_add_f32 v135, v132, v133\n\ts_nop 1\n\t"
            "v_permlane16_swap_b32 v134, v135\n\tv_add_f32 v134, v134, v135\n\ts_nop 1\n\t"
            "v_add_f32_dpp v134, v134, v134 row_ror:8 row_mask:0xf bank_mask:0xf\n\t"
            "v_cvt_pk_bf16_f32 v134, v134, v134\n\tv_lshlrev_b32 %[t], 16, v134\n\t"
            "v_mul_f32 v134, %[t], %[gt]\n\tv_cvt_pk_bf16_f32 v134, v134, v134\n\tv_lshlrev_b32 v134, 16, v134\n\t"
            "v_add_f32 v134, %[xv], v134\n\tv_cvt_pk_bf16_f32 v134, v134, v134\n\t"
            "s_mov_b64 exec, %[ms]\n\tglobal_store_short %[po], v134, off sc1\n\ts_mov_b64 exec, -1"
            : [t] "=&v"(t), [n] "=&s"(n)
            : [tag] "s"(tag), [max] "n"(SPIN_MAX), [p] "v"(py), [m3] "s"(m3), [ms] "s"(ms), [gt] "v"(gt), [xv] "v"(xv), [po] "v"(po)
            : CLOB_T, "vcc", "scc", "memory");
        if (pass) break;
        gt = bfr(silu(t));
        py -= GZ - GY;
        m3 = ~0ull;
        ms = 0x000000ff000000ffull;
      }
      if (b == 0 && lane == 0)
        __hip_atomic_store(reinterpret_cast<unsigned*>(gw + GEP), tag, __ATOMIC_RELAXED, __HIP_MEMORY_SCOPE_AGENT);
    }
    asm volatile(".Lcode_lastb:");
    return;
  }
  pace<T_LAST + W2T, 0>(clk, ph);
#pragma unroll
  for (int i = 3; i < 6; ++i) WO[i] = __builtin_amdgcn_raw_buffer_load_b128(rwo, wooff, i * (4 * D * 2), 2);
  unsigned dm0 = __builtin_amdgcn_raw_buffer_load_b32(rwo, NOWHERE, 0, 2), dm1 = __builtin_amdgcn_raw_buffer_load_b32(rwo, NOWHERE, 4, 2);
  SCHED_FENCE();
  const int ly = isF ? lane & 31 : lane;
  u64* ydst = gw + GY + (size_t)(4 * sp + (ly >> 3) + 4 * (ly >> 5)) * YB + 32 * h + 16 * (h >> 4) + 2 * (ly & 7);
  static_assert(RS * 8 * 4 == 4608, "record r4 + 4 is 4608 bytes behind record r4");
  const u64* pr = gw + G2 + (size_t)(h * NSP + r4 + 2) * RS + 32 * w + 2 * c16;
  const u64 ownm = 0xffffull << (16 * (sp & 3));
  u64 own0 = sp < 4 ? ownm : 0ull, own1 = sp < 4 ? 0ull : ownm, my = isF ? 0xffffffffull : ~0ull;
  u64 for0 = m52 & ~own0, for1 = m52 & ~own1;
  own0 &= m48;
  own1 &= m48;
  unsigned osrc = lds_addr(lds + L_PART + 48 * w + 4 * c16), ysrc = lds_addr(lds + L_Y + 2 * ly), cnt1 = lds_addr(lds + L_SY + 1);
  unsigned ydl = lds_addr(lds + L_Y + 128 * w + (isF ? 8 * c + g : 8 * c16 + 2 * r4));
  asm volatile("" : "+s"(own0), "+s"(own1), "+s"(for0), "+s"(for1), "+s"(my));
  PIN(ydst);
  PIN(pr);
  PIN(osrc);
  PIN(ysrc);
  PIN(cnt1);
  PIN(ydl);
  u32x8 WP[3], XP[3];
#pragma unroll
  for (int p = 0; p < 3; ++p)
#pragma unroll
    for (int k = 0; k < 4; ++k) {
      XP[p][2 * k] = __builtin_amdgcn_perm(X[2 * p + 1][k], X[2 * p][k], 0x05040100u);
      XP[p][2 * k + 1] = __builtin_amdgcn_perm(X[2 * p + 1][k], X[2 * p][k], 0x07060302u);
    }
  asm volatile("" : "+{v[64:71]}"(XP[0]), "+{v[72:79]}"(XP[1]), "+{v[80:87]}"(XP[2]));
  SCHED_FENCE();
  auto perm_wo = [&]() {
#pragma unroll
    for (int p = 0; p < 3; ++p)
#pragma unroll
      for (int k = 0; k < 4; ++k) {
        WP[p][2 * k] = __builtin_amdgcn_perm(WO[2 * p + 1][k], WO[2 * p][k], 0x05040100u);
        WP[p][2 * k + 1] = __builtin_amdgcn_perm(WO[2 * p + 1][k], WO[2 * p][k], 0x07060302u);
      }
    asm volatile("" : "+{v[88:95]}"(WP[0]), "+{v[96:103]}"(WP[1]), "+{v[104:111]}"(WP[2]));
    SCHED_FENCE();
  };
  lds_wait(lds, L_SY + 6, 8u);
  pace<T_LAST + PT_, 0>(clk, 0);
  u32x4 ra0, ra1;
  asm volatile(
      "v_mov_b32 v148, %[cw]\n\tv_mov_b32 v149, %[tag]\n\tv_mov_b32 v151, %[tag]\n\t"
      "v_mov_b32 v152, %[cw]\n\tv_mov_b32 v153, %[tag]\n\tv_mov_b32 v155, %[tag]\n\t"
      "s_mov_b64 exec, %[for0]\n\tglobal_load_dwordx4 v[148:151], %[p], off offset:-2304 sc1\n\t"
      "s_mov_b64 exec, %[for1]\n\tglobal_load_dwordx4 v[152:155], %[p], off offset:2304 sc1\n\t"
      "s_mov_b64 exec, %[m48]\n\t" SUM8("%[src]") SUM8B SUM8C
      "s_mov_b64 exec, %[own0]\n\tv_cvt_pk_bf16_f32 v148, v116, v117\n\tv_cvt_pk_bf16_f32 v150, v118, v119\n\t"
      "s_mov_b64 exec, %[own1]\n\tv_cvt_pk_bf16_f32 v152, v116, v117\n\tv_cvt_pk_bf16_f32 v154, v118, v119\n\t"
      "s_mov_b64 exec, -1"
      : "=&{v[148:151]}"(ra0), "=&{v[152:155]}"(ra1)
      : [cw] "v"(cw), [tag] "s"(tag), [for0] "s"(for0), [for1] "s"(for1), [p] "v"(pr), [own0] "s"(own0), [own1] "s"(own1),
        [m48] "s"(m48), [src] "v"(osrc)
      : CLOB_T, "memory");
  SCHED_FENCE();
  perm_wo();
  unsigned np;
  asm volatile(
      "s_mov_b32 %[n], 0\n\t"
      "s_mov_b64 exec, -1\n\tv_mov_b64 v[160:161], 0\n\tv_mov_b64 v[162:163], 0\n\tv_mov_b64 v[164:165], 0\n\t"
      "v_mov_b64 v[166:167], 0\n\ts_branch .Lrc%=\n"
      ".Lrp%=:\n\t"
      "s_add_u32 %[n], %[n], 1\n\ts_cmp_gt_u32 %[n], %[max]\n\ts_cbranch_scc1 .Lrd%=\n\t"
      "v_cmp_ne_u32 vcc, 0, v112\n\tv_cmp_ne_u32 s[98:99], 0, v113\n\ts_sleep " "1" "\n\t"
      "s_mov_b64 exec, vcc\n\tglobal_load_dwordx4 v[148:151], %[p], off offset:-2304 sc1\n\t"
      "s_mov_b64 exec, s[98:99]\n\tglobal_load_dwordx4 v[152:155], %[p], off offset:2304 sc1\n\t"
      "s_mov_b64 exec, -1\n"
      ".Lrc%=:\n\t"
      "s_waitcnt vmcnt(0)\n\t"
      "v_bitop3_b32 v112, v149, v151, %[tag] bitop3:0x7e\n\tv_bitop3_b32 v113, v153, v155, %[tag] bitop3:0x7e\n\t"
      "v_max_f32 v115, v148, v152\n\tv_or_b32 v114, v112, v113\n\tv_max_f32 v116, v148, v152\n\t"
      "v_cmp_ne_u32 vcc, 0, v114\n\tv_lshlrev_b32 v122, 16, v148\n\ts_cbranch_vccnz .Lrp%=\n"
      ".Lrd%=:\n\t"
      "v_permlane16_swap_b32 v115, v116\n\tv_max_f32 v117, v115, v116\n\tv_max_f32 v118, v115, v116\n\t"
      "v_and_b32 v123, 0xffff0000, v148\n\tv_lshlrev_b32 v124, 16, v150\n\t"
      "v_permlane32_swap_b32 v117, v118\n\tv_max_f32 v117, v117, v118\n\t"
      "v_sub_f32 v115, v148, v117\n\tv_sub_f32 v116, v152, v117\n\tv_exp_f32 v115, v115\n\tv_exp_f32 v116, v116\n\t"
      "v_and_b32 v125, 0xffff0000, v150\n\tv_lshlrev_b32 v126, 16, v152\n\t"
      "v_add_f32 v117, v115, v116\n\tv_add_f32 v118, v115, v116\n\t"
      "v_mul_f32_dpp v122, v115, v122" BC12 "v_mul_f32_dpp v123, v115, v123" BC12
      "v_permlane16_swap_b32 v117, v118\n\tv_add_f32 v119, v117, v118\n\tv_add_f32 v120, v117, v118\n\t"
      "v_mul_f32_dpp v124, v115, v124" BC12 "v_mul_f32_dpp v125, v115, v125" BC12
      "v_permlane32_swap_b32 v119, v120\n\tv_add_f32 v119, v119, v120\n\tv_lshlrev_b32 v128, 16, v154\n\t"
      "v_rcp_f32 v119, v119\n\tv_and_b32 v127, 0xffff0000, v152\n\tv_and_b32 v129, 0xffff0000, v154\n\t"
      "v_fmac_f32_dpp v122, v116, v126" BC12 "v_fmac_f32_dpp v124, v116, v128" BC12
      "v_fmac_f32_dpp v123, v116, v127" BC12 "v_fmac_f32_dpp v125, v116, v129" BC12
      "v_permlane32_swap_b32 v122, v124\n\tv_add_f32 v122, v122, v124\n\t"
      "v_permlane32_swap_b32 v123, v125\n\tv_add_f32 v123, v123, v125\n\t"
      "v_mov_b32_dpp v120, v119" BC12 "s_nop 0\n\t"
      "v_permlane16_swap_b32 v122, v123\n\tv_add_f32 v122, v122, v123\n\t"
      "v_mul_f32 v123, v122, v120\n\ts_cmp_lt_u32 %[sp], 6\n\t"
      "v_mul_f32_dpp v124, v122, v120 quad_perm:[1,0,3,2] row_mask:0xf bank_mask:0xf\n\t"
      "v_cvt_pk_bf16_f32 v124, v123, v124\n\t"
      "s_cbranch_scc1 .Lyf%=\n\ts_nop 0\n\t"
      DOT8(64, 65, 66, 67, 68, 69, 70, 71, 0) DOT8(72, 73, 74, 75, 76, 77, 78, 79, 2) DOT8(80, 81, 82, 83, 84, 85, 86, 87, 4)
      DOT8(88, 89, 90, 91, 92, 93, 94, 95, 6) DOT8(96, 97, 98, 99, 100, 101, 102, 103, 8)
      DOT8(104, 105, 106, 107, 108, 109, 110, 111, 10) YRED
      "v_pk_add_f32 v[160:161], v[160:161], v[162:163]\n\tds_write_b64 %[ydl], v[160:161]\n"
      ".Lys%=:\n\t"
      "s_mov_b64 exec, 1\n\tds_add_rtn_u32 v112, %[cnt], %[one]\n\ts_mov_b64 exec, -1\n\t"
      "ds_read2st64_b64 v[116:119], %[ysrc] offset0:0 offset1:1\n\tds_read2st64_b64 v[120:123], %[ysrc] offset0:2 offset1:3\n\t"
      "s_waitcnt lgkmcnt(2)\n\tv_readfirstlane_b32 s98, v112\n\ts_waitcnt lgkmcnt(1)\n\t"
      "v_pk_add_f32 v[116:117], v[116:117], v[118:119]\n\ts_cmp_eq_u32 s98, 3\n\ts_cselect_b64 exec, %[my], 0\n\t"
      "s_waitcnt lgkmcnt(0)\n\tv_pk_add_f32 v[120:121], v[120:121], v[122:123]\n\ts_nop 0\n\t"
      "v_add_f32 v156, v116, v120\n\tv_add_f32 v158, v117, v121\n\t"
      "global_store_dwordx4 %[dst], v[156:159], off sc1\n\ts_mov_b64 exec, -1\n\ts_branch .Lye%=\n"
      ".Lyf%=:\n\ts_nop 0\n\t"
      "v_mov_b32_dpp v125, v124 row_newbcast:0 row_mask:0xf bank_mask:0x3\n\t"
      "v_mov_b32_dpp v126, v124 row_newbcast:2 row_mask:0xf bank_mask:0x3\n\t"
      "v_mov_b32_dpp v127, v124 row_newbcast:4 row_mask:0xf bank_mask:0x3\n\t"
      "v_mov_b32_dpp v125, v124 row_newbcast:6 row_mask:0xf bank_mask:0xc\n\t"
      "v_mov_b32_dpp v126, v124 row_newbcast:8 row_mask:0xf bank_mask:0xc\n\t"
      "v_mov_b32_dpp v127, v124 row_newbcast:10 row_mask:0xf bank_mask:0xc\n\t"
      DOT8F(88, 89, 90, 91, 92, 93, 94, 95, 125) DOT8F(96, 97, 98, 99, 100, 101, 102, 103, 126)
      DOT8F(104, 105, 106, 107, 108, 109, 110, 111, 127) YRED
      "v_add_f32 v160, v160, v162\n\tv_add_f32 v161, v161, v163\n\ts_nop 0\n\t"
      "v_add_f32_dpp v160, v160, v160 row_ror:8 row_mask:0xf bank_mask:0x3\n\t"
      "v_add_f32_dpp v160, v161, v161 row_ror:8 row_mask:0xf bank_mask:0xc\n\t"
      "ds_write_b32 %[ydl], v160\n\ts_branch .Lys%=\n"
      ".Lye%=:"
      : [n] "=&s"(np), "+{v[156:159]}"(rq), "+{v[148:151]}"(ra0), "+{v[152:155]}"(ra1)
      : [tag] "s"(tag), [p] "v"(pr), [max] "n"(SPIN_MAX), [sp] "s"(sp), [ydl] "v"(ydl), [cnt] "v"(cnt1), [one] "v"(one),
        [ysrc] "v"(ysrc), [my] "s"(my), [dst] "v"(ydst), "{v[64:71]}"(XP[0]), "{v[72:79]}"(XP[1]), "{v[80:87]}"(XP[2]),
        "{v[88:95]}"(WP[0]), "{v[96:103]}"(WP[1]), "{v[104:111]}"(WP[2])
      : CLOB_T, "v160", "v161", "v162", "v163", "v164", "v165", "v166", "v167", "s98", "s99", "vcc", "scc", "memory");
  asm volatile("" ::"v"(dm0), "v"(dm1));
  asm volatile(".Lcode_last:");
}

__global__ void __launch_bounds__(NT, 2)
fused_decode(const bf16* __restrict__ wq, const bf16* __restrict__ kc, const bf16* __restrict__ w1, const bf16* __restrict__ w3,
             u64* __restrict__ gw, const bf16* __restrict__ x, const bf16* __restrict__ vc,
             const bf16* __restrict__ wo, const bf16* __restrict__ w2, bf16* __restrict__ out) {
  __shared__ __attribute__((aligned(16))) float lds[L_END];
  const int b = blockIdx.x;
  const int xcd = b & 7, jj = b >> 3;
  const int h = xcd * 4 + (jj >> 3), sp = jj & 7;
  const int w = __builtin_amdgcn_readfirstlane(threadIdx.x >> 6);
  unsigned cpf = threadIdx.x;
  {
    u64 pc;
    unsigned len;
    asm volatile(
        "s_cmp_lt_u32 %[w], 4\n\ts_cbranch_scc1 .Lnocpf\n\t"
        "s_getpc_b64 %[pc]\n.Lcode_here:\n\tv_xor_b32 %[c], %[hi], %[c]\n\tv_lshlrev_b32 %[c], 6, %[c]\n\t"
        "s_mov_b32 %[len], .Lcode_last-.Lcode_here-4\n\ts_max_i32 %[len], %[len], .Lcode_lastb-.Lcode_here+508\n\t"
        "v_min_u32 %[c], %[len], %[c]\n\tglobal_load_dword %[c], %[c], %[pc]\n.Lnocpf:"
        : [c] "+v"(cpf), [pc] "=&s"(pc), [len] "=&s"(len) : [w] "s"(w), [hi] "s"((b & 8) << 5) : "scc", "memory");
  }
  SCHED_FENCE();
  if (w >= 4)
    body<false>(lds, h, sp, w, x, kc, vc, wq, wo, w1, w3, w2, gw, out, cpf);
  else
    body<true>(lds, h, sp, w, x, kc, vc, wq, wo, w1, w3, w2, gw, out, cpf);
}

}

extern "C" void kernel_launch(void* const* d_in, const int* in_sizes, int n_in, void* d_out, int out_size, void* d_ws,
                              size_t ws_size, hipStream_t stream) {
  const bf16* x = (const bf16*)d_in[0];
  const bf16* kc = (const bf16*)d_in[1];
  const bf16* vc = (const bf16*)d_in[2];
  const bf16* wq = (const bf16*)d_in[3];
  const bf16* wo = (const bf16*)d_in[4];
  const bf16* w1 = (const bf16*)d_in[6];
  const bf16* w3 = (const bf16*)d_in[7];
  const bf16* w2 = (const bf16*)d_in[8];
  u64* gw = (u64*)d_ws;
  fused_decode<<<NWG, NT, 0, stream>>>(wq, kc, w1, w3, gw, x, vc, wo, w2, (bf16*)d_out);
}
```

```cpp
#define TEL 2
#include <hip/hip_runtime.h>
#include <hip/hip_bf16.h>
#include <stdint.h>

typedef __hip_bfloat16 bf16;
typedef unsigned long long u64;
typedef unsigned u32x4 __attribute__((ext_vector_type(4)));
typedef unsigned u32x8 __attribute__((ext_vector_type(8)));
typedef float f32x4 __attribute__((ext_vector_type(4)));
typedef float f32x2 __attribute__((ext_vector_type(2)));
typedef __bf16 bf16x2_t __attribute__((ext_vector_type(2)));

namespace {

constexpr int D = 640, HQ = 32, DH = 192, SK = 32768, WIN = 4096, FF = 1536;
constexpr int NSP = 8;
constexpr int NWG = HQ * NSP;
constexpr int NT = 512;
constexpr float SCALE = 0.07216878364870322f;
constexpr float LOG2E = 1.4426950408889634f;
constexpr unsigned SPIN_MAX = 1u << 16;
constexpr int SC1 = 16;
constexpr int NOWHERE = 1 << 30;

#define PV_ 81
#define PK_ 82
#define PX_ PK_
#define TS_ 358
#define SB_ 246
#define LOC 30
#define WT_ 98
#define W2T 132
#define W1N 3
#define PT_ 215
#define NFL 7
#define CE 7
#define CL 8
#define AR 3
#define GR 5
#define GRW 2
#define RSLEEP 1
#define MW 4
#define PM 0x1440
#define MEAS 1
#define ATHR 20
#define ASH 4
#define AGX 7
#define UTHR 8
#define UTH2 40
#define USH 4
#define UGX 9
#define ULIM 20
#define UMISS 60
#define PVL 1
#define LSP 16
#define GLP 1
#define E1P 1
#define UTA 5
#define USH2 3
#define CT 3
#define CPM 8
#define XS 6
#define LOCO 60
#define VT 10
#define CSH 30
#define HROT 16
constexpr int t_round(int r) { return r <= 2 ? TS_ : t_round(r - 1) + (r > 8 ? PV_ : r == 8 ? PX_ : PK_); }
constexpr int T_LAST = t_round(15);
constexpr int t_late(int i) { return t_round(i); }
constexpr int t_early(int i) { return t_round(i); }

constexpr int QS = 144;
constexpr int YB = 2 * (16 * 32 + 16);
constexpr int RS = 144;
constexpr size_t GQ = 0;
constexpr size_t G2 = GQ + (size_t)NWG * QS;
constexpr size_t GY = G2 + (size_t)NWG * RS;
constexpr size_t GZ = GY + 16;
constexpr size_t GEP = GY + (size_t)NSP * 5 * YB;
constexpr size_t GL = GEP + 16;

__device__ __forceinline__ unsigned fbits(float x) { return __builtin_bit_cast(unsigned, x); }
__device__ __forceinline__ u64 gld(const u64* p) {
  return __hip_atomic_load(p, __ATOMIC_RELAXED, __HIP_MEMORY_SCOPE_AGENT);
}
__device__ __forceinline__ float bf_lo(unsigned u) { return __builtin_bit_cast(float, u << 16); }
__device__ __forceinline__ float bf_hi(unsigned u) { return __builtin_bit_cast(float, u & 0xffff0000u); }
__device__ __forceinline__ float bfr(float x) { return __bfloat162float(__float2bfloat16(x)); }
__device__ __forceinline__ unsigned short bfbits(float x) {
  return __builtin_bit_cast(unsigned short, __float2bfloat16(x));
}
__device__ __forceinline__ float dot2(unsigned a, unsigned b, float c) {
  return __builtin_amdgcn_fdot2_f32_bf16(__builtin_bit_cast(bf16x2_t, a), __builtin_bit_cast(bf16x2_t, b), c, false);
}
template <int CTRL>
__device__ __forceinline__ float dpp(float x) {
  return __builtin_bit_cast(float, __builtin_amdgcn_mov_dpp(__builtin_bit_cast(int, x), CTRL, 0xf, 0xf, true));
}
__device__ __forceinline__ float grp8_sum(float x) {
  x += dpp<0xB1>(x);
  x += dpp<0x4E>(x);
  x += dpp<0x141>(x);
  return x;
}
__device__ __forceinline__ float sw16(float a, float b) {
  auto s = __builtin_amdgcn_permlane16_swap(fbits(a), fbits(b), false, false);
  return __builtin_bit_cast(float, (unsigned)s[0]) + __builtin_bit_cast(float, (unsigned)s[1]);
}
__device__ __forceinline__ float sw32(float a, float b) {
  auto s = __builtin_amdgcn_permlane32_swap(fbits(a), fbits(b), false, false);
  return __builtin_bit_cast(float, (unsigned)s[0]) + __builtin_bit_cast(float, (unsigned)s[1]);
}
__device__ __forceinline__ float xg_sum(float x) {
  x += dpp<0x128>(x);
  x = sw16(x, x);
  return sw32(x, x);
}
__device__ __forceinline__ float xg_max(float x) {
  x = fmaxf(x, dpp<0x128>(x));
  auto s = __builtin_amdgcn_permlane16_swap(fbits(x), fbits(x), false, false);
  x = fmaxf(__builtin_bit_cast(float, (unsigned)s[0]), __builtin_bit_cast(float, (unsigned)s[1]));
  auto t = __builtin_amdgcn_permlane32_swap(fbits(x), fbits(x), false, false);
  return fmaxf(__builtin_bit_cast(float, (unsigned)t[0]), __builtin_bit_cast(float, (unsigned)t[1]));
}
__device__ __forceinline__ float wave_sum(float x) { return xg_sum(grp8_sum(x)); }
__device__ __forceinline__ float ex2(float x) { return __builtin_amdgcn_exp2f(x); }
__device__ __forceinline__ float silu(float x) { return x / (1.0f + __expf(-x)); }
__device__ __forceinline__ float xg_reduce8(const float (&a)[8], bool g0) {
  float r[4];
#pragma unroll
  for (int i = 0; i < 4; ++i) r[i] = sw32(a[i], a[i + 4]);
  const float s0 = sw16(r[0], r[2]), s1 = sw16(r[1], r[3]);
  const float keep = g0 ? s1 : s0, send = g0 ? s0 : s1;
  return keep + dpp<0x128>(send);
}
__device__ __forceinline__ __amdgpu_buffer_rsrc_t rsrc(const void* p, unsigned bytes) {
  return __builtin_amdgcn_make_buffer_rsrc(const_cast<void*>(p), 0, bytes, 0x00020000);
}
__device__ __forceinline__ void gst16(u64* p, unsigned v0, unsigned v1, unsigned tag) {
  const u32x4 o = {v0, tag, v1, tag};
  asm volatile("global_store_dwordx4 %0, %1, off sc1\n\ts_nop 1" ::"v"(p), "v"(o) : "memory");
}
__device__ __forceinline__ unsigned bfpack(float a, float b) {
  return __builtin_bit_cast(unsigned, __builtin_convertvector(f32x2{a, b}, bf16x2_t));
}
__device__ __forceinline__ float2 rows_reduce(const float (&v)[8]) {
  float r[4];
#pragma unroll
  for (int i = 0; i < 4; ++i) r[i] = sw32(v[i], v[i + 4]);
  return float2{sw16(r[0], r[2]), sw16(r[1], r[3])};
}
template <int OFF>
__device__ __forceinline__ void xg_store(const float (&v)[8], float f, unsigned a) {
  float p[4], q[4], t, u;
#pragma unroll
  for (int i = 0; i < 4; ++i) {
    auto s = __builtin_amdgcn_permlane32_swap(fbits(v[i]), fbits(v[i + 4]), false, false);
    p[i] = __builtin_bit_cast(float, (unsigned)s[0]);
    q[i] = __builtin_bit_cast(float, (unsigned)s[1]);
  }
  const f32x2 r01 = f32x2{p[0], p[1]} + f32x2{q[0], q[1]}, r23 = f32x2{p[2], p[3]} + f32x2{q[2], q[3]};
  auto s0 = __builtin_amdgcn_permlane16_swap(fbits(r01[0]), fbits(r23[0]), false, false);
  auto s1 = __builtin_amdgcn_permlane16_swap(fbits(r01[1]), fbits(r23[1]), false, false);
  asm volatile("v_add_f32 %0, %2, %3\n\tv_add_f32 %1, %4, %5\n\ts_nop 0\n\t"
               "v_add_f32_dpp %0, %0, %0 row_ror:8 row_mask:0xf bank_mask:0x3\n\t"
               "v_add_f32_dpp %0, %1, %1 row_ror:8 row_mask:0xf bank_mask:0xc\n\t"
               "v_mul_f32 %0, %6, %0\n\tds_write_b32 %7, %0 offset:%8"
               : "=&v"(t), "=&v"(u)
               : "v"((unsigned)s0[0]), "v"((unsigned)s0[1]), "v"((unsigned)s1[0]), "v"((unsigned)s1[1]), "v"(f), "v"(a), "n"(OFF)
               : "memory");
}
#define SUM8(a)                                                                                                          \
  "ds_read_b128 v[116:119], " a "\n\tds_read_b128 v[120:123], " a " offset:768\n\t"                                      \
  "ds_read_b128 v[124:127], " a " offset:1536\n\tds_read_b128 v[128:131], " a " offset:2304\n\t"                         \
  "ds_read_b128 v[132:135], " a " offset:3072\n\tds_read_b128 v[136:139], " a " offset:3840\n\t"                         \
  "ds_read_b128 v[140:143], " a " offset:4608\n\tds_read_b128 v[144:147], " a " offset:5376\n\ts_waitcnt lgkmcnt(6)\n\t"
#define SUM8B                                                                                                            \
  "v_pk_add_f32 v[116:117], v[116:117], v[120:121]\n\tv_pk_add_f32 v[118:119], v[118:119], v[122:123]\n\t"               \
  "s_waitcnt lgkmcnt(4)\n\t"                                                                                             \
  "v_pk_add_f32 v[124:125], v[124:125], v[128:129]\n\tv_pk_add_f32 v[126:127], v[126:127], v[130:131]\n\t"               \
  "v_pk_add_f32 v[116:117], v[116:117], v[124:125]\n\tv_pk_add_f32 v[118:119], v[118:119], v[126:127]\n\t"
#define SUM8C                                                                                                            \
  "s_waitcnt lgkmcnt(2)\n\t"                                                                                             \
  "v_pk_add_f32 v[132:133], v[132:133], v[136:137]\n\tv_pk_add_f32 v[134:135], v[134:135], v[138:139]\n\t"               \
  "s_waitcnt lgkmcnt(0)\n\t"                                                                                             \
  "v_pk_add_f32 v[140:141], v[140:141], v[144:145]\n\tv_pk_add_f32 v[142:143], v[142:143], v[146:147]\n\t"               \
  "v_pk_add_f32 v[132:133], v[132:133], v[140:141]\n\tv_pk_add_f32 v[134:135], v[134:135], v[142:143]\n\t"               \
  "v_pk_add_f32 v[116:117], v[116:117], v[132:133]\n\tv_pk_add_f32 v[118:119], v[118:119], v[134:135]\n\t"
#define BC12 " row_newbcast:12 row_mask:0xf bank_mask:0xf\n\t"
#define DOTB(y, a, n) "v_dot2c_f32_bf16_dpp v" #y ", v124, v" #a " row_newbcast:" #n " row_mask:0xf bank_mask:0xf\n\t"
#define DOT8(a, b, c, d, e, f, g, h, n) \
  DOTB(160, a, n) DOTB(161, b, n) DOTB(162, c, n) DOTB(163, d, n) DOTB(164, e, n) DOTB(165, f, n) DOTB(166, g, n) DOTB(167, h, n)
#define DOTF(y, a, n) "v_dot2c_f32_bf16 v" #y ", v" #n ", v" #a "\n\t"
#define DOT8F(a, b, c, d, e, f, g, h, n) \
  DOTF(160, a, n) DOTF(161, b, n) DOTF(162, c, n) DOTF(163, d, n) DOTF(164, e, n) DOTF(165, f, n) DOTF(166, g, n) DOTF(167, h, n)
#define YRED                                                                                                             \
  "v_permlane32_swap_b32 v160, v164\n\tv_permlane32_swap_b32 v161, v165\n\tv_permlane32_swap_b32 v162, v166\n\t"         \
  "v_permlane32_swap_b32 v163, v167\n\tv_pk_add_f32 v[160:161], v[160:161], v[164:165]\n\t"                              \
  "v_pk_add_f32 v[162:163], v[162:163], v[166:167]\n\ts_nop 1\n\t"                                                       \
  "v_permlane16_swap_b32 v160, v162\n\tv_permlane16_swap_b32 v161, v163\n\t"
#define CLOB_T                                                                                                     \
  "v112", "v113", "v114", "v115", "v116", "v117", "v118", "v119", "v120", "v121", "v122", "v123", "v124", "v125",        \
      "v126", "v127", "v128", "v129", "v130", "v131", "v132", "v133", "v134", "v135", "v136", "v137", "v138", "v139",    \
      "v140", "v141", "v142", "v143", "v144", "v145", "v146", "v147"
#define SCHED_FENCE() __builtin_amdgcn_sched_barrier(0)
#define STR2(x) #x
#define STR(x) STR2(x)
#define THRN(n) do { SCHED_FENCE(); asm volatile("s_waitcnt vmcnt(%0)" ::"n"(n) : "memory"); SCHED_FENCE(); } while (0)
#define NOWT() ((unsigned)__builtin_amdgcn_s_memrealtime())
template <int N>
struct IC {
  static constexpr int v = N;
};
template <int I, int E, class F>
__device__ __forceinline__ void sfor(F&& f) {
  if constexpr (I < E) {
    f(IC<I>{});
    sfor<I + 1, E>(f);
  }
}
#define LDS_DRAIN() asm volatile("s_waitcnt lgkmcnt(0)" ::: "memory")
__device__ __forceinline__ unsigned lds_add(float* lds, int idx) {
  return __hip_atomic_fetch_add(reinterpret_cast<unsigned*>(lds + idx), 1u, __ATOMIC_RELAXED, __HIP_MEMORY_SCOPE_WORKGROUP);
}
__device__ __forceinline__ void lds_wait(float* lds, int idx, unsigned n) {
  SCHED_FENCE();
#pragma unroll 1
  for (unsigned spin = 0; spin < (SPIN_MAX << 4); ++spin) {
    if (__hip_atomic_load(reinterpret_cast<unsigned*>(lds + idx), __ATOMIC_RELAXED, __HIP_MEMORY_SCOPE_WORKGROUP) >= n) break;
    __builtin_amdgcn_s_sleep(1);
  }
  asm volatile("" ::: "memory");
  SCHED_FENCE();
}
template <int T, int R>
__device__ __forceinline__ void pace(unsigned clk, int ph) {
  unsigned o;
  SCHED_FENCE();
  asm volatile(
      "s_branch .Lpb%=\n"
      ".Lpa%=:\n\ts_sleep 1\n"
      ".Lpb%=:\n\tds_read_b32 %0, %1 offset:%4\n\ts_memrealtime s[98:99]\n\ts_waitcnt lgkmcnt(0)\n\tv_readfirstlane_b32 s99, %0\n\t"
      "s_sub_u32 s98, s98, %2\n\ts_sub_u32 s98, s98, s99\n\ts_cmp_lt_i32 s98, %3\n\ts_cbranch_scc1 .Lpa%="
      : "=&v"(o) : "v"(clk), "s"(ph), "n"(T), "n"(4 * R) : "s98", "s99", "scc", "memory");
  SCHED_FENCE();
}

template <int T, int R>
__device__ __forceinline__ unsigned pace_m(unsigned clk, int ph) {
  unsigned o, tl;
  SCHED_FENCE();
  asm volatile(
      "s_mov_b32 %1, 0\n\ts_branch .Lpb%=\n"
      ".Lpa%=:\n\ts_sleep 1\n"
      ".Lpb%=:\n\tds_read_b32 %0, %2 offset:%5\n\ts_getreg_b32 s97, hwreg(HW_REG_IB_STS, 0, 4)\n\ts_memrealtime s[98:99]\n\t"
      "s_or_b32 s97, s97, %1\n\ts_waitcnt lgkmcnt(0)\n\ts_cselect_b32 %1, %1, s98\n\tv_readfirstlane_b32 s99, %0\n\t"
      "s_sub_u32 s98, s98, %3\n\ts_sub_u32 s98, s98, s99\n\ts_cmp_lt_i32 s98, %4\n\ts_cbranch_scc1 .Lpa%="
      : "=&v"(o), "=&s"(tl) : "v"(clk), "s"(ph), "n"(T), "n"(4 * R) : "s97", "s98", "s99", "scc", "memory");
  SCHED_FENCE();
  return tl;
}

#define STAMP(i)
#define STAMP_FLUSH()
#define TT(i)
constexpr int L_QP = 0;
constexpr int L_QS = L_QP + 4 * 128;
constexpr int L_PART = L_QS + 96;
constexpr int L_ML = L_PART + 8 * DH;
constexpr int L_Z = L_ML + 16;
constexpr int L_Y = L_Z + 320;
constexpr int L_H = L_Y + 512;
constexpr int L_SY = L_H + 64;
constexpr int L_CK = L_SY + 8;
constexpr int L_TE = L_CK + 16;
constexpr int L_END = L_TE + 8;

#define PIN(x) asm volatile("" : "+v"(x))

__device__ __forceinline__ void wg_ml(const float* lds, float& M, float& L) {
  float mm[8], ll[8];
#pragma unroll
  for (int i = 0; i < 4; ++i) {
    const f32x4 t = *reinterpret_cast<const f32x4*>(lds + L_ML + 4 * i);
    mm[2 * i] = t[0];
    ll[2 * i] = t[1];
    mm[2 * i + 1] = t[2];
    ll[2 * i + 1] = t[3];
  }
  M = mm[0];
#pragma unroll
  for (int ww = 1; ww < 8; ++ww) M = fmaxf(M, mm[ww]);
  L = 0.f;
#pragma unroll
  for (int ww = 0; ww < 8; ++ww) L = fmaf(ex2((mm[ww] - M) * LOG2E), ll[ww], L);
}
__device__ __forceinline__ unsigned lds_addr(const float* p) {
  return (unsigned)(size_t)(const __attribute__((address_space(3))) float*)p;
}

template <bool early>
__device__ __forceinline__ void body(float* lds, const int h, const int sp, const int w, const bf16* __restrict__ x,
                                     const bf16* __restrict__ kc, const bf16* __restrict__ vc,
                                     const bf16* __restrict__ wq, const bf16* __restrict__ wo,
                                     const bf16* __restrict__ w1, const bf16* __restrict__ w3,
                                     const bf16* __restrict__ w2, u64* __restrict__ gw, bf16* __restrict__ out,
                                     unsigned cpf) {
  const int b = blockIdx.x;
  const int tid = threadIdx.x, lane = tid & 63;
  const int g = lane >> 3, c = lane & 7;
  const int rec = h * NSP + sp;

  STAMP(0);
  const bool isF = sp < 6;
  u32x4 PR[8], xa, xb, xq;
  unsigned epoch = 0;
  u64 tk = 0;
  int ra = 0, RA0 = 0;
  bool va = false;
  if constexpr (early) {
    int s0_ = 240, s1_ = 80, s2_ = 240 * (sp & 1), s3_ = 80 * sp, s4_ = 64 * (sp >> 1), s5_ = 64 * w, s6_ = 24 * w;
    asm("" : "+s"(s0_), "+s"(s1_), "+s"(s2_), "+s"(s3_), "+s"(s4_), "+s"(s5_), "+s"(s6_));
    const int rw0 = isF ? s5_ : s6_;
    int lim = (isF ? s0_ : s1_) - rw0, lim2 = isF ? 64 : 24;
    asm("" : "+s"(lim), "+s"(lim2));
    lim = lim < lim2 ? lim : lim2;
    ra = rw0 + 8 * g;
    va = 8 * g < lim;
    RA0 = isF ? s2_ : s3_;
    int aoff = (int)__umul24(g, 8 * HQ * DH * 2) + 16 * c;
    PIN(aoff);
    aoff = va ? aoff : NOWHERE;
    int sa = ((RA0 + rw0) * (HQ * DH) + h * DH + (isF ? s4_ : 64)) * 2;
    PIN(aoff);
    asm volatile("" : "+s"(sa));
    SCHED_FENCE();
    const __amdgpu_buffer_rsrc_t rw = rsrc(wq, NOWHERE);
    PR[0] = __builtin_amdgcn_raw_buffer_load_b128(rw, aoff, sa, 2);
    SCHED_FENCE();
    tk = __builtin_amdgcn_s_memrealtime();
    SCHED_FENCE();
    asm volatile("s_load_dword %0, %1, %2" : "=&s"(epoch) : "s"(gw), "s"(E1P && w == 1 ? (int)GL * 8 + (sp * 8 + (b & 7)) * LSP : (int)GEP * 8) : "memory");
    SCHED_FENCE();
#pragma unroll
    for (int i = 1; i < 8; ++i) PR[i] = __builtin_amdgcn_raw_buffer_load_b128(rw, aoff, sa + i * (HQ * DH * 2), 2);
    SCHED_FENCE();
    SCHED_FENCE();
  }
  const unsigned short* xs = reinterpret_cast<const unsigned short*>(x);
  const size_t wgb = ((size_t)h * SK + (SK - WIN)) * DH;
  const __amdgpu_buffer_rsrc_t rk = rsrc(kc + wgb, WIN * DH * 2);
  const __amdgpu_buffer_rsrc_t rv = rsrc(vc + wgb, WIN * DH * 2);
  const int voff = (g * DH + 8 * c) * 2;
  auto tile = [&](int t) { return (t * 64 + h * HROT + w * 8 + sp) & 511; };
  u32x4 K[8][3], V[8][3];
  auto kld = [&](int t, int j) { K[t][j] = __builtin_amdgcn_raw_buffer_load_b128(rk, voff + 128 * j, tile(t) * 3072, 2); };
  auto vld = [&](int t, int j) { V[t][j] = __builtin_amdgcn_raw_buffer_load_b128(rv, voff + 128 * j, tile(t) * 3072, 2); };
  unsigned tag = 0;
  const unsigned clk = lds_addr(lds + L_CK);
  const int ph = (w * PV_) >> 3;
  const int r4 = lane >> 4, c16 = lane & 15;
  const int ch = c16 < 10 ? c16 : 9;
  const int fi = (h & 3) * 6 + (isF ? sp : 5), gl = fi >> 3, gI = (h >> 2) * 3 + gl, fr = fi & 7;
  u32x4 WO[6], X[6], W2[4];
  unsigned tch = 0;
  const int wooff = ((48 * w + r4 + (isF ? 24 * (g & 1) : 24)) * D + 64 * sp + 8 * c + (isF ? 0 : 128 * (g & 1))) * 2;
  typedef unsigned u32x2 __attribute__((ext_vector_type(2)));
  const __amdgpu_buffer_rsrc_t rl = rsrc(gw + GL, 16 * 64 * LSP);
  const bool pub = !((sp + (b >> 6)) & 3);
  const int lw_ = lane == 0 && pub ? (sp * 8 + (b & 7)) * LSP : NOWHERE;
  u32x2 lat = {0u, 0u};
  auto fetch = [&](int off) {
    SCHED_FENCE();
    asm volatile("buffer_load_dwordx2 %0, %1, %2, %3 offen sc1" : "+v"(lat) : "v"(lane * LSP), "s"(rl), "s"(off) : "memory");
    SCHED_FENCE();
  };
  u64 own = 0, pre = 0;
  unsigned pro = 0, st1 = 0, old = 0;
  bool vslow = false, kslow = false;
  float sh = 0.f;
  if constexpr (early) {
    u32x4 PF[5];
    const int F0 = (isF ? 160 * (fr >> 1) : 480) + 40 * w;
    const int fs = isF ? FF : HQ * DH;
    int s6_ = 64 * gI, s7_ = h * DH + 128 * (sp & 1);
    asm("" : "+s"(s6_), "+s"(s7_));
    const int boff = ((int)__umul24(g, fs) + 8 * c) * 2;
    const int sb = (F0 * fs + (isF ? s6_ : s7_)) * 2;
    const bf16* pf_ = (fr & 1) ? w3 : w1;
    asm("" : "+s"(pf_));
    const __amdgpu_buffer_rsrc_t rf = rsrc(isF ? pf_ : wq, NOWHERE);
    unsigned short xr[5];
#pragma unroll
    for (int i = 0; i < 5; ++i) PF[i] = __builtin_amdgcn_raw_buffer_load_b128(rf, boff, sb + i * 16 * fs, 2);
    SCHED_FENCE();
    if (!isF) {
#pragma unroll
      for (int i = 0; i < XS; ++i)
        X[i] = __builtin_amdgcn_raw_buffer_load_b128(rsrc(wo + (size_t)h * DH * D, DH * D * 2), wooff - 24 * D * 2 + i * (4 * D * 2), 0, 2);
    } else {
#pragma unroll
      for (int i = 0; i < XS; ++i) X[i] = u32x4{0u, 0u, 0u, 0u};
    }
    SCHED_FENCE();
    xa = *reinterpret_cast<const u32x4*>(x + 8 * lane);
    xb = *reinterpret_cast<const u32x4*>(x + 512 + 8 * (lane & 15));
    xq = *reinterpret_cast<const u32x4*>(x + RA0 + (va ? ra : 0));
#pragma unroll
    for (int i = 0; i < 5; ++i) xr[i] = xs[F0 + 8 * i + g];
    SCHED_FENCE();
    const unsigned epv = __hip_atomic_load(reinterpret_cast<unsigned*>(gw + GEP), __ATOMIC_RELAXED, __HIP_MEMORY_SCOPE_AGENT);
    SCHED_FENCE();
    asm volatile("" : "+s"(tk));
    if (tid < 17) reinterpret_cast<unsigned*>(lds)[L_CK + tid] = (unsigned)tk;
    asm volatile("s_waitcnt lgkmcnt(0)\n\ts_barrier" : "+s"(epoch)::"memory");
    STAMP(6);
    SCHED_FENCE();
#pragma unroll
    for (int t = 0; t < 3; ++t)
#pragma unroll
      for (int j = 0; j < 3; ++j) kld(t, j);
    SCHED_FENCE();
    float mb, den;
    {
      float xf[8], xg[8];
#pragma unroll
      for (int i = 0; i < 4; ++i) {
        xf[2 * i] = bf_lo(xa[i]);
        xf[2 * i + 1] = bf_hi(xa[i]);
        xg[2 * i] = bf_lo(xb[i]);
        xg[2 * i + 1] = bf_hi(xb[i]);
      }
      const bool lo16 = lane < 16;
      float sum = 0.f;
#pragma unroll
      for (int i = 0; i < 8; ++i) sum += xf[i] + (lo16 ? xg[i] : 0.f);
      sum = wave_sum(sum);
      const float mean = sum * (1.0f / D);
      float sq = 0.f;
#pragma unroll
      for (int i = 0; i < 8; ++i) {
        const float d1 = xf[i] - mean, d2 = xg[i] - mean;
        sq += d1 * d1 + (lo16 ? d2 * d2 : 0.f);
      }
      sq = wave_sum(sq);
      mb = bfr(mean);
      const float vb = bfr(sq * (1.0f / D));
      den = bfr(sqrtf(bfr(vb + 1e-5f)));
    }
    {
      unsigned xn[13];
#pragma unroll
      for (int i = 0; i < 4; ++i) {
        xn[2 * i] = fbits(bfr(bfr(bf_lo(xq[i]) - mb) / den));
        xn[2 * i + 1] = fbits(bfr(bfr(bf_hi(xq[i]) - mb) / den));
      }
#pragma unroll
      for (int i = 0; i < 5; ++i) xn[8 + i] = fbits(bfr(bfr(bf_lo(xr[i]) - mb) / den));
#pragma unroll
      for (int i = 0; i < 13; ++i) PIN(xn[i]);
      SCHED_FENCE();
      pace<t_round(2), 2>(clk, ph);
#pragma unroll
      for (int j = 0; j < 3; ++j) kld(3, j);
      TT(2);
      SCHED_FENCE();
      float fa[8], fg[8];
#pragma unroll
      for (int k = 0; k < 8; ++k) fa[k] = fg[k] = 0.f;
#pragma unroll
      for (int i = 0; i < 8; ++i)
#pragma unroll
        for (int k = 0; k < 4; ++k) {
          fa[2 * k] = dot2(PR[i][k], xn[i] >> 16, fa[2 * k]);
          fa[2 * k + 1] = dot2(PR[i][k], xn[i], fa[2 * k + 1]);
        }
      lds[L_QP + w * 128 + 8 * c + g] = xg_reduce8(fa, g & 1);
#pragma unroll
      for (int i = 0; i < 5; ++i)
#pragma unroll
        for (int k = 0; k < 4; ++k) {
          fg[2 * k] = dot2(PF[i][k], xn[8 + i] >> 16, fg[2 * k]);
          fg[2 * k + 1] = dot2(PF[i][k], xn[8 + i], fg[2 * k + 1]);
        }
      lds[L_QP + w * 128 + 64 + 8 * c + g] = xg_reduce8(fg, g & 1);
    }
    SCHED_FENCE();
    tag = __builtin_amdgcn_readfirstlane(epv) + 1u;
    if (tid == 0) reinterpret_cast<unsigned*>(lds)[L_SY + 3] = tag;
    LDS_DRAIN();
    if (lane == 0) old = lds_add(lds, L_SY);
    old = __builtin_amdgcn_readfirstlane(old);
    if (old == 3) {
      const float2* qp = reinterpret_cast<const float2*>(lds + L_QP) + lane;
      const float2 p0_ = qp[0], p1_ = qp[64], p2_ = qp[128], p3_ = qp[192];
      gst16(gw + GQ + (size_t)rec * QS + 2 * lane, fbits((p0_.x + p1_.x) + (p2_.x + p3_.x)),
            fbits((p0_.y + p1_.y) + (p2_.y + p3_.y)), tag);
    }
    STAMP(11);
  } else {
    if (tid >= NT - 8) reinterpret_cast<unsigned*>(lds)[L_SY + tid - (NT - 8)] = 0u;
    unsigned st0 = 0;
    {
      const bf16* tp = w == 4 ? kc + wgb : w == 5 ? (!isF ? wo + (size_t)h * DH * D : (fr & 1) ? w3 : w1) : w == 6 ? ((sp & 1) ? x : wq) : vc + wgb;
      asm volatile("s_waitcnt lgkmcnt(0)\n\ts_load_dword %0, %1, 0x0" : "=&s"(st0) : "s"(tp) : "memory");
    }
    SCHED_FENCE();
#pragma unroll
    for (int j = 0; j < 3; ++j) kld(0, j);
    SCHED_FENCE();
    asm volatile("s_barrier" ::: "memory");
    STAMP(6);
    SCHED_FENCE();
    if (w < 6) {
      const int l = lane < 42 ? lane : 41, k = l & 15;
      const int yg = 4 * (k < 8 || isF ? sp : sp + 2) + ((k >> 1) & 3);
      const size_t yo = l < 32 || !isF ? GY + (size_t)yg * YB + 32 * h + 16 * (h >> 4) + 8 * (k & 1)
                                       : GZ + (size_t)(fr * 5 + (k >> 1)) * YB + 32 * gI + 16 * (gI >> 4) + 8 * (k & 1);
      unsigned o4 = (unsigned)(l < 16 ? G2 + (size_t)rec * RS + 8 * l : yo);
      unsigned o5 = (unsigned)(GQ + (size_t)rec * QS + 8 * (lane < 16 ? lane : 15));
      if (GLP && pub && lane >= 16 && lane < 32 && ((PM >> (lane - 16)) & 1))
        o5 = (unsigned)GL + (unsigned)(((lane - 16) * 64 + sp * 8 + (b & 7)) * (LSP / 8));
      PIN(o4);
      PIN(o5);
      pre = gld(gw + (w == 4 ? o4 : o5));
    }
    if (w == 7 && h < 5)
      pro = __hip_atomic_load(reinterpret_cast<const unsigned*>(out + 80 * sp + 16 * h) + (lane & 7), __ATOMIC_RELAXED,
                              __HIP_MEMORY_SCOPE_AGENT);
    SCHED_FENCE();
    asm volatile("s_waitcnt lgkmcnt(0)" ::"s"(st0) : "memory");
#pragma unroll
    for (int j = 0; j < 3; ++j) {
      kld(1, j);
      THRN(5);
    }
    THRN(3);
    TT(1);
    own = __builtin_amdgcn_s_memrealtime() - SB_;
    __builtin_amdgcn_raw_buffer_store_b64(u32x2{(unsigned)own, (unsigned)(own >> 32)}, rl, w == 6 ? lw_ : NOWHERE, 0, SC1);
    SCHED_FENCE();
    if (LOC && w == 6 && lane < 16) {
      const unsigned o = (unsigned)own - (isF ? LOC : LOCO);
      if ((int)(o - reinterpret_cast<unsigned*>(lds)[L_CK + lane]) > 0) reinterpret_cast<unsigned*>(lds)[L_CK + lane] = o;
    }
    SCHED_FENCE();
    {
      const bf16* tp = (w & 1) ? wo + (size_t)h * DH * D : w2 + (size_t)gI * 64 * D;
      asm volatile("s_load_dword %0, %1, 0x0" : "=&s"(st1) : "s"(tp) : "memory");
    }
    SCHED_FENCE();
  }
  u32x4 qf[3];
  float sc[8], mw, lw, acc[24];
  unsigned pb[8];
  auto score = [&](int t) {
    float d = 0.f;
#pragma unroll
    for (int j = 0; j < 3; ++j)
#pragma unroll
      for (int i = 0; i < 4; ++i) d = dot2(K[t][j][i], qf[j][i], d);
    d = grp8_sum(d);
    sc[t] = bfr(bfr(d) * SCALE);
  };
  auto pv = [&](int t) {
    const unsigned plo = pb[t], phi = plo << 16;
#pragma unroll
    for (int j = 0; j < 3; ++j)
#pragma unroll
      for (int i = 0; i < 4; ++i) {
        acc[8 * j + 2 * i] = dot2(V[t][j][i], plo, t ? acc[8 * j + 2 * i] : 0.f);
        acc[8 * j + 2 * i + 1] = dot2(V[t][j][i], phi, t ? acc[8 * j + 2 * i + 1] : 0.f);
        PIN(acc[8 * j + 2 * i]);
        PIN(acc[8 * j + 2 * i + 1]);
      }
  };
  const bool hf = lane >> 5;
  const __amdgpu_buffer_rsrc_t rq_ = rsrc(gw + GQ, NWG * QS * 8);
  int go[9];
  {
    const int l16 = (lane & 31) * 16;
    const int qa = l16 + (8 * h + (hf ? 4 : 0)) * (QS * 8);
    go[0] = qa;
    go[1] = qa + QS * 8;
    go[2] = l16 + (8 * h + (hf ? 7 : 6)) * (QS * 8) + 512;
    go[3] = qa + 2 * QS * 8;
    go[4] = qa + 3 * QS * 8;
#pragma unroll
    for (int k = 0; k < 4; ++k) {
      const int m = 8 * gl + 2 * k + (hf ? 1 : 0);
      go[5 + k] = isF ? l16 + (32 * (h >> 2) + m + (((m * 43) >> 7) & 6)) * (QS * 8) + 512 : NOWHERE;
    }
  }
  const bool gath = old == GRW;
  u32x4 pa[9];
  constexpr int C0 = early ? CE : CL;
  static_assert(C0 + 3 < 15 && C0 > GR + 1 && C0 >= 6, "schedule");
  sfor<early ? 3 : 2, 16>([&](auto R) {
    constexpr int r = R.v, t = r >= 8 ? r - 8 : r + early;
    unsigned tl = 0;
    if constexpr (!early && MEAS && ((PM >> r) & 1)) tl = pace_m<t_round(r), r>(clk, ph); else pace<t_round(r), r>(clk, ph);
    if constexpr (!early && ((PM >> (r - 1)) & 1)) {
      if (w == MW) fetch(64 * LSP * (r - 1));
    }
    if constexpr (r < 8 && t == 8) {
      const __amdgpu_buffer_rsrc_t rx = isF ? rsrc(w2 + ((size_t)gI * 64 + 16 * w) * D + 80 * fr, 16 * D * 2) : rsrc(wo + (size_t)h * DH * D, DH * D * 2);
#pragma unroll
      for (int i = 0; i < 4; ++i)
        W2[i] = __builtin_amdgcn_raw_buffer_load_b128(rx, isF ? (r4 * D + 8 * ch) * 2 : i < 6 - XS ? wooff - (24 - 4 * XS) * D * 2 : NOWHERE, i * (4 * D * 2), 2);
      tch = __builtin_amdgcn_raw_buffer_load_b32(rsrc(wo + (size_t)h * DH * D, DH * D * 2), tid ? NOWHERE : wooff, 0, 2);
    } else {
#pragma unroll
      for (int j = 0; j < 3; ++j) {
        if (r >= 8) vld(t, j); else kld(t, j);
        if (r == 15 && j == 2) break;
        if (early && r == GR + 1) continue;
        THRN(early && r == 8 ? NFL + 2 : NFL);
      }
    }
    TT(r);
    SCHED_FENCE();
    if constexpr (early) {
      if (r == GR && gath) {
#pragma unroll
        for (int s = 0; s < 9; ++s) pa[s] = __builtin_amdgcn_raw_buffer_load_b128(rq_, go[s], 0, SC1);
        SCHED_FENCE();
      }
      if (r == GR + 1) {
        if (gath) {
          const unsigned tf = isF ? tag : 0u;
          bool ok = true;
#pragma unroll
          for (int s = 0; s < 9; ++s) ok = ok & (pa[s][1] == (s < 5 ? tag : tf)) & (pa[s][3] == (s < 5 ? tag : tf));
          if (!__all(ok)) {
            for (unsigned spin = 0;; ++spin) {
              ok = true;
              asm volatile("" ::: "memory");
#pragma unroll
              for (int s = 0; s < 9; ++s) {
                pa[s] = __builtin_amdgcn_raw_buffer_load_b128(rq_, go[s], 0, SC1);
                ok = ok & (pa[s][1] == (s < 5 ? tag : tf)) & (pa[s][3] == (s < 5 ? tag : tf));
              }
              if (__all(ok) || spin >= SPIN_MAX) break;
              __builtin_amdgcn_s_sleep(1);
            }
          }
          float qo[2], qm[2], hu[2];
#pragma unroll
          for (int k = 0; k < 2; ++k) {
            auto val = [&](int s) { return __builtin_bit_cast(float, (unsigned)pa[s][2 * k]); };
            qo[k] = (val(0) + val(1)) + val(2);
            const float t_ = val(3) + val(4);
            qm[k] = sw32(t_, t_);
            const float u = bfr((val(5) + val(6)) + (val(7) + val(8)));
            auto sw = __builtin_amdgcn_permlane32_swap(fbits(u), fbits(u), false, false);
            hu[k] = bfr(bfr(silu(__builtin_bit_cast(float, (unsigned)sw[0]))) * __builtin_bit_cast(float, (unsigned)sw[1]));
          }
          reinterpret_cast<unsigned*>(lds + L_QS)[lane + (hf ? 32 : 0)] = bfpack(qo[0], qo[1]);
          if (hf) {
            reinterpret_cast<unsigned*>(lds + L_QS)[lane] = bfpack(qm[0], qm[1]);
          } else {
            *reinterpret_cast<float2*>(lds + L_H + 2 * lane) = float2{hu[0], hu[1]};
          }
          LDS_DRAIN();
          if (lane == 0) lds_add(lds, L_SY + 2);
        }
        STAMP(2);
      }
    } else {
      if (r == 2) asm volatile("" ::"s"(st1), "v"(cpf), "v"(pre), "v"(pro));
      if (r == AR && w == 6) fetch(0);
      if (r == AR + 1 && w == 6) {
        asm volatile("s_waitcnt vmcnt(3)" : "+v"(lat)::"memory");
        const u64 d = (((u64)lat[1] << 32) | lat[0]) - own + 200u;
        const bool ok = d <= 400u;
        const float s_ = wave_sum(ok ? (float)(unsigned)d : 0.f), n = (float)__builtin_popcountll(__ballot(ok));
        const unsigned o = (unsigned)own + (n > 0.f ? (unsigned)(s_ * __builtin_amdgcn_rcpf(n)) - 200u : 0u);
        if (w == 6 && lane < 16) reinterpret_cast<unsigned*>(lds)[L_CK + lane] = o;
        if (w == 6 && lane == 0) reinterpret_cast<unsigned*>(lds)[L_TE + 4] = o;
      }
      if (r == AR + 2) {
        lds_wait(lds, L_SY, 4u);
        tag = __builtin_amdgcn_readfirstlane(reinterpret_cast<const unsigned*>(lds)[L_SY + 3]);
      }
      static_assert(!(PM & ((1 << (AR + 2)) - 1)), "tag");
      if ((PM >> r) & 1) {
        const unsigned org = __builtin_amdgcn_readfirstlane(reinterpret_cast<const unsigned*>(lds)[L_CK + r]);
        if (w == MW && pub && !tl) THRN(3);
        const unsigned now = NOWT() - 9;
        const int l = (int)((tl ? tl : now) - org) - (t_round(r) + ph);
        __builtin_amdgcn_raw_buffer_store_b64(u32x2{(unsigned)(l < -64 ? 0 : l + 64), tag}, rl, w == MW && l < ULIM ? lw_ : NOWHERE,
                                              64 * LSP * r, SC1);
      }
      SCHED_FENCE();
    }
    if (r == C0) {
      lds_wait(lds, L_SY + 2, 1u);
      STAMP(3);
#pragma unroll
      for (int j = 0; j < 3; ++j) qf[j] = *reinterpret_cast<const u32x4*>(lds + L_QS + (c + 8 * j) * 4);
#pragma unroll
      for (int k = 0; k < 4; ++k) score(k);
    } else if (r == C0 + 1) {
#pragma unroll
      for (int k = 4; k < 7; ++k) score(k);
    } else if (r == C0 + 2) {
      score(7);
      mw = sc[0];
#pragma unroll
      for (int k = 1; k < 8; ++k) mw = fmaxf(mw, sc[k]);
      mw = xg_max(mw);
      lw = 0.f;
#pragma unroll
      for (int k = 0; k < 8; ++k) {
        const float p = ex2((sc[k] - mw) * LOG2E);
        lw += p;
        pb[k] = bfbits(p);
      }
      lw = xg_sum(lw);
      if (lane == 0) {
        lds[L_ML + 2 * w] = mw;
        lds[L_ML + 2 * w + 1] = lw;
      }
      if (early) {
        float z[8];
#pragma unroll
        for (int n = 0; n < 8; ++n) z[n] = 0.f;
#pragma unroll
        for (int r = 0; r < 4; ++r) {
          const unsigned chi = fbits(lds[L_H + 16 * w + 4 * r + r4]), clo = chi >> 16;
#pragma unroll
          for (int n = 0; n < 4; ++n) {
            z[2 * n] = dot2(W2[r][n], clo, z[2 * n]);
            z[2 * n + 1] = dot2(W2[r][n], chi, z[2 * n + 1]);
          }
        }
        const float2 zr = rows_reduce(z);
        if (c16 < 10) *reinterpret_cast<float2*>(lds + L_Z + 80 * w + 8 * c16 + 2 * r4) = zr;
      }
      LDS_DRAIN();
      if (lane == 0) lds_add(lds, L_SY + 4);
      STAMP(4);
    } else if (r == C0 + 3) {
#pragma unroll
      for (int k = 0; k < t + 1 - PVL; ++k) pv(k);
    } else if (r > C0 + 3 && r < 15) {
      pv(t - PVL);
    }
    if constexpr (!early && ((PM >> (r - 1)) & 1)) {
      SCHED_FENCE();
      if (w == MW) {
      const int lr = lane ? lane : 15;
      static_assert(!((PM >> r) & 1), "three requests behind the fetch");
      asm volatile("s_waitcnt vmcnt(3)" : "+v"(lat)::"memory");
      constexpr int p = r - 1;
      const float m = wave_sum((float)(lat[1] == tag && lat[0] < UMISS + 64 ? lat[0] : UMISS + 64)) * (1.f / 64) - 64.f;
      int d = 0;
      if (p <= 8) {
        kslow = m >= ATHR;
        if (kslow) d = (int)(m * (ASH / 4.f)) + AGX * (lr <= p ? 0 : lr < 8 ? lr - p : 8 - p);
      } else if (!vslow && m >= (p <= 10 ? (kslow ? UTA : UTHR) : UTH2)) {
        vslow = true;
        sh = fmaxf(m, 0.f) * ((p <= 10 ? USH : USH2) / 4.f);
        d = (int)sh + (lr > p ? UGX * (lr - p) : 0) + (lane ? 0 : VT);
      } else if (vslow && m < CT && sh < CSH) {
        d = (lr > p + 1 ? -UGX * (lr - p - 1) : 0) - (lane ? 0 : VT);
      }
      if (lane < 16) reinterpret_cast<unsigned*>(lds)[L_CK + lane] += d;
      if (lane == 0) reinterpret_cast<int*>(lds)[L_TE + (p <= 8 ? 1 : p <= 10 ? 2 : 3)] = (int)m;
      }
    }
  });
  asm volatile("" ::"v"(tch));
  SCHED_FENCE();
  const __amdgpu_buffer_rsrc_t rwo = rsrc(wo + (size_t)h * DH * D, DH * D * 2);
  lds_wait(lds, L_SY + 4, 8u);
  float f, M, L;
  wg_ml(lds, M, L);
  f = ex2((mw - M) * LOG2E) / L;
  const unsigned cw = fbits(fmaf(M, LOG2E, __builtin_amdgcn_logf(L)));
  PIN(f);
  SCHED_FENCE();
  const int l40 = lane < 40 ? lane : 39;
  u64* rdst = gw + G2 + (size_t)rec * RS + 32 * r4 + 2 * c16;
  u64* zdst = gw + GZ + (size_t)(fr * 5 + (l40 >> 3)) * YB + 32 * gI + 16 * (gI >> 4) + 2 * (l40 & 7);
  unsigned pdst = lds_addr(lds + L_PART + w * DH + 8 * c + g), psrc = lds_addr(lds + L_PART + 48 * r4 + 4 * c16);
  unsigned cnt6 = lds_addr(lds + L_SY + 6), one = 1u;
  u32x4 rq = {cw, tag, cw, tag};
  u64 m48 = 0x0fff0fff0fff0fffull, m52 = 0x1fff1fff1fff1fffull;
  asm volatile("" : "+s"(m48), "+s"(m52), "+{v[156:159]}"(rq));
  PIN(rdst);
  PIN(zdst);
  PIN(pdst);
  PIN(psrc);
  PIN(cnt6);
  PIN(one);
#pragma unroll
  for (int k = 7 - PVL; k < 7; ++k) pv(k);
  auto wo1 = [&]() {
    if (WT_) pace<T_LAST + WT_, 0>(clk, ph);
    SCHED_FENCE();
#pragma unroll
    for (int i = 0; i < W1N; ++i) WO[i] = __builtin_amdgcn_raw_buffer_load_b128(rwo, wooff, i * (4 * D * 2), 2);
    SCHED_FENCE();
  };
  {
    unsigned plo = pb[7], phi = plo << 16;
    PIN(plo);
    PIN(phi);
    sfor<0, 3>([&](auto J) {
      constexpr int j = J.v;
      SCHED_FENCE();
      float a[8];
#pragma unroll
      for (int i = 0; i < 4; ++i) {
        a[2 * i] = dot2(V[7][j][i], plo, acc[8 * j + 2 * i]);
        a[2 * i + 1] = dot2(V[7][j][i], phi, acc[8 * j + 2 * i + 1]);
      }
      xg_store<256 * j>(a, f, pdst);
    });
    if (early) wo1();
  }
  STAMP(5);
  unsigned arr;
  asm volatile(
      "s_mov_b64 exec, 1\n\tds_add_rtn_u32 v112, %[cnt], %[one]\n\ts_mov_b64 exec, %[m48]\n\t" SUM8("%[src]")
      "v_readfirstlane_b32 %[arr], v112\n\t" SUM8B
      "s_cmp_eq_u32 %[arr], 7\n\t" SUM8C
      "v_cvt_pk_bf16_f32 v156, v116, v117\n\tv_cvt_pk_bf16_f32 v158, v118, v119\n\t"
      "s_cselect_b64 exec, %[m52], 0\n\tglobal_store_dwordx4 %[dst], v[156:159], off sc1\n\ts_mov_b64 exec, -1\n\ts_nop 0"
      : [arr] "=&s"(arr), "+{v[156:159]}"(rq)
      : [cnt] "v"(cnt6), [one] "v"(one), [m48] "s"(m48), [m52] "s"(m52), [src] "v"(psrc), [dst] "v"(rdst)
      : CLOB_T, "scc", "memory");
  if (arr == 0 && isF) {
    const float2* zp = reinterpret_cast<const float2*>(lds + L_Z) + l40;
    const float2 z0 = zp[0], z1 = zp[40], z2 = zp[80], z3 = zp[120];
    gst16(zdst, fbits((z0.x + z1.x) + (z2.x + z3.x)), fbits((z0.y + z1.y) + (z2.y + z3.y)), tag);
  }
  if constexpr (!early) {
    if (w == 7 && h < 5) {
      const int col = 80 * sp + 16 * h + 2 * c + (lane >> 5);
      const float xv = bf_lo(xs[col]);
      const u64* py = gw + GZ + (size_t)(sp * 5 + h) * YB + 32 * g + 2 * c + 392;
      bf16* po = out + col;
      float gt = 0.f;
      u64 m3 = 0ull, ms = 0ull;
      PIN(po);
#pragma unroll 1
      for (int pass = 0; pass < 2; ++pass) {
        lds_wait(lds, L_SY + (pass ? 1 : 6), pass ? 4u : 8u);
        STAMP(8);
        asm volatile("" : "+s"(m3), "+s"(ms));
        float t;
        unsigned n;
        asm volatile(
            "v_mov_b32 v124, 0\n\tv_mov_b32 v125, %[tag]\n\tv_mov_b32 v126, 0\n\tv_mov_b32 v127, %[tag]\n\t"
            "s_mov_b64 vcc, -1\n\ts_mov_b32 %[n], 0\n"
            ".Lfp%=:\n\t"
            "s_add_u32 %[n], %[n], 1\n\ts_cmp_gt_u32 %[n], %[max]\n\ts_cbranch_scc1 .Lfd%=\n\t"
            "s_mov_b64 exec, vcc\n\t"
            "global_load_dwordx4 v[112:115], %[p], off offset:-3136 sc1\n\t"
            "global_load_dwordx4 v[116:119], %[p], off offset:-1088 sc1\n\t"
            "global_load_dwordx4 v[120:123], %[p], off offset:1088 sc1\n\t"
            "s_and_b64 exec, vcc, %[m3]\n\t"
            "global_load_dwordx4 v[124:127], %[p], off offset:3136 sc1\n\t"
            "s_mov_b64 exec, -1\n\ts_waitcnt vmcnt(0)\n\t"
            "v_bitop3_b32 v128, v113, v115, %[tag] bitop3:0x7e\n\tv_bitop3_b32 v129, v117, v119, %[tag] bitop3:0x7e\n\t"
            "v_bitop3_b32 v130, v121, v123, %[tag] bitop3:0x7e\n\tv_bitop3_b32 v131, v125, v127, %[tag] bitop3:0x7e\n\t"
            "v_or3_b32 v128, v128, v129, v130\n\tv_add_f32 v132, v112, v116\n\tv_or_b32 v128, v128, v131\n\t"
            "v_add_f32 v134, v120, v124\n\tv_cmp_ne_u32 vcc, 0, v128\n\tv_add_f32 v133, v114, v118\n\t"
            "s_cbranch_vccnz .Lfp%=\n"
            ".Lfd%=:\n\t"
            "v_add_f32 v135, v122, v126\n\tv_add_f32 v132, v132, v134\n\tv_add_f32 v133, v133, v135\n\ts_nop 1\n\t"
            "v_permlane32_swap_b32 v132, v133\n\tv_add_f32 v134, v132, v133\n\tv_add_f32 v135, v132, v133\n\ts_nop 1\n\t"
            "v_permlane16_swap_b32 v134, v135\n\tv_add_f32 v134, v134, v135\n\ts_nop 1\n\t"
            "v_add_f32_dpp v134, v134, v134 row_ror:8 row_mask:0xf bank_mask:0xf\n\t"
            "v_cvt_pk_bf16_f32 v134, v134, v134\n\tv_lshlrev_b32 %[t], 16, v134\n\t"
            "v_mul_f32 v134, %[t], %[gt]\n\tv_cvt_pk_bf16_f32 v134, v134, v134\n\tv_lshlrev_b32 v134, 16, v134\n\t"
            "v_add_f32 v134, %[xv], v134\n\tv_cvt_pk_bf16_f32 v134, v134, v134\n\t"
            "s_mov_b64 exec, %[ms]\n\tglobal_store_short %[po], v134, off sc1\n\ts_mov_b64 exec, -1"
            : [t] "=&v"(t), [n] "=&s"(n)
            : [tag] "s"(tag), [max] "n"(SPIN_MAX), [p] "v"(py), [m3] "s"(m3), [ms] "s"(ms), [gt] "v"(gt), [xv] "v"(xv), [po] "v"(po)
            : CLOB_T, "vcc", "scc", "memory");
        if (pass) break;
        gt = bfr(silu(t));
        py -= GZ - GY;
        m3 = ~0ull;
        ms = 0x000000ff000000ffull;
      }
      if (b == 0 && lane == 0)
        __hip_atomic_store(reinterpret_cast<unsigned*>(gw + GEP), tag, __ATOMIC_RELAXED, __HIP_MEMORY_SCOPE_AGENT);
      if (b == 0) {
        const volatile unsigned* lu = reinterpret_cast<const volatile unsigned*>(lds);
        const unsigned now = NOWT(), tk0 = lu[L_TE];
        auto cl = [](int v, int hi) { return v < 0 ? 0 : v > hi ? hi : v; };
        int code = 0;
        if (TEL == 1) code = cl((int)(now - lu[L_CK + 15] - T_LAST - 300) / 10, 19) + 20 * cl((int)(lu[L_CK + 15] - lu[L_TE + 4]) / 10, 19);
        if (TEL == 2) code = cl(((int)lu[L_TE + 3] + 20) / 8, 9) + 10 * cl(((int)lu[L_TE + 2] + 8) / 4, 9) + 100 * cl((int)lu[L_TE + 1] / 10, 6);
        while (NOWT() - tk0 < 10000u + 400u * code) __builtin_amdgcn_s_sleep(4);
      }
      STAMP(10);
    }
    STAMP_FLUSH();
    asm volatile(".Lcode_lastb:");
    return;
  }
  pace<T_LAST + W2T, 0>(clk, ph);
#pragma unroll
  for (int i = W1N; i < 6; ++i) WO[i] = __builtin_amdgcn_raw_buffer_load_b128(rwo, wooff, i * (4 * D * 2), 2);
  unsigned dm0 = __builtin_amdgcn_raw_buffer_load_b32(rwo, NOWHERE, 0, 2), dm1 = __builtin_amdgcn_raw_buffer_load_b32(rwo, NOWHERE, 4, 2);
  STAMP(3);
  SCHED_FENCE();
  const int ly = isF ? lane & 31 : lane;
  u64* ydst = gw + GY + (size_t)(4 * sp + (ly >> 3) + 4 * (ly >> 5)) * YB + 32 * h + 16 * (h >> 4) + 2 * (ly & 7);
  static_assert(RS * 8 * 4 == 4608, "record r4 + 4 is 4608 bytes behind record r4");
  const u64* pr = gw + G2 + (size_t)(h * NSP + r4 + 2) * RS + 32 * w + 2 * c16;
  const u64 ownm = 0xffffull << (16 * (sp & 3));
  u64 own0 = sp < 4 ? ownm : 0ull, own1 = sp < 4 ? 0ull : ownm, my = isF ? 0xffffffffull : ~0ull;
  u64 for0 = m52 & ~own0, for1 = m52 & ~own1;
  own0 &= m48;
  own1 &= m48;
  unsigned osrc = lds_addr(lds + L_PART + 48 * w + 4 * c16), ysrc = lds_addr(lds + L_Y + 2 * ly), cnt1 = lds_addr(lds + L_SY + 1);
  unsigned ydl = lds_addr(lds + L_Y + 128 * w + (isF ? 8 * c + g : 8 * c16 + 2 * r4));
  asm volatile("" : "+s"(own0), "+s"(own1), "+s"(for0), "+s"(for1), "+s"(my));
  PIN(ydst);
  PIN(pr);
  PIN(osrc);
  PIN(ysrc);
  PIN(cnt1);
  PIN(ydl);
#pragma unroll
  for (int i = XS; i < 6; ++i) X[i] = W2[i - XS];
  u32x8 WP[3], XP[3];
#pragma unroll
  for (int p = 0; p < 3; ++p)
#pragma unroll
    for (int k = 0; k < 4; ++k) {
      XP[p][2 * k] = __builtin_amdgcn_perm(X[2 * p + 1][k], X[2 * p][k], 0x05040100u);
      XP[p][2 * k + 1] = __builtin_amdgcn_perm(X[2 * p + 1][k], X[2 * p][k], 0x07060302u);
    }
  asm volatile("" : "+{v[64:71]}"(XP[0]), "+{v[72:79]}"(XP[1]), "+{v[80:87]}"(XP[2]));
  SCHED_FENCE();
  auto perm_wo = [&]() {
#pragma unroll
    for (int p = 0; p < 3; ++p)
#pragma unroll
      for (int k = 0; k < 4; ++k) {
        WP[p][2 * k] = __builtin_amdgcn_perm(WO[2 * p + 1][k], WO[2 * p][k], 0x05040100u);
        WP[p][2 * k + 1] = __builtin_amdgcn_perm(WO[2 * p + 1][k], WO[2 * p][k], 0x07060302u);
      }
    asm volatile("" : "+{v[88:95]}"(WP[0]), "+{v[96:103]}"(WP[1]), "+{v[104:111]}"(WP[2]));
    SCHED_FENCE();
    STAMP(1);
  };
  lds_wait(lds, L_SY + 6, 8u);
  if (PT_) pace<T_LAST + PT_, 0>(clk, 0);
  STAMP(4);
  u32x4 ra0, ra1;
  asm volatile(
      "v_mov_b32 v148, %[cw]\n\tv_mov_b32 v149, %[tag]\n\tv_mov_b32 v151, %[tag]\n\t"
      "v_mov_b32 v152, %[cw]\n\tv_mov_b32 v153, %[tag]\n\tv_mov_b32 v155, %[tag]\n\t"
      "s_mov_b64 exec, %[for0]\n\tglobal_load_dwordx4 v[148:151], %[p], off offset:-2304 sc1\n\t"
      "s_mov_b64 exec, %[for1]\n\tglobal_load_dwordx4 v[152:155], %[p], off offset:2304 sc1\n\t"
      "s_mov_b64 exec, %[m48]\n\t" SUM8("%[src]") SUM8B SUM8C
      "s_mov_b64 exec, %[own0]\n\tv_cvt_pk_bf16_f32 v148, v116, v117\n\tv_cvt_pk_bf16_f32 v150, v118, v119\n\t"
      "s_mov_b64 exec, %[own1]\n\tv_cvt_pk_bf16_f32 v152, v116, v117\n\tv_cvt_pk_bf16_f32 v154, v118, v119\n\t"
      "s_mov_b64 exec, -1"
      : "=&{v[148:151]}"(ra0), "=&{v[152:155]}"(ra1)
      : [cw] "v"(cw), [tag] "s"(tag), [for0] "s"(for0), [for1] "s"(for1), [p] "v"(pr), [own0] "s"(own0), [own1] "s"(own1),
        [m48] "s"(m48), [src] "v"(osrc)
      : CLOB_T, "memory");
  SCHED_FENCE();
  perm_wo();
  unsigned np;
  asm volatile(
      "s_mov_b32 %[n], 0\n\t"
      "s_mov_b64 exec, -1\n\tv_mov_b64 v[160:161], 0\n\tv_mov_b64 v[162:163], 0\n\tv_mov_b64 v[164:165], 0\n\t"
      "v_mov_b64 v[166:167], 0\n\ts_branch .Lrc%=\n"
      ".Lrp%=:\n\t"
      "s_add_u32 %[n], %[n], 1\n\ts_cmp_gt_u32 %[n], %[max]\n\ts_cbranch_scc1 .Lrd%=\n\t"
      "v_cmp_ne_u32 vcc, 0, v112\n\tv_cmp_ne_u32 s[98:99], 0, v113\n\ts_sleep " STR(RSLEEP) "\n\t"
      "s_mov_b64 exec, vcc\n\tglobal_load_dwordx4 v[148:151], %[p], off offset:-2304 sc1\n\t"
      "s_mov_b64 exec, s[98:99]\n\tglobal_load_dwordx4 v[152:155], %[p], off offset:2304 sc1\n\t"
      "s_mov_b64 exec, -1\n"
      ".Lrc%=:\n\t"
      "s_waitcnt vmcnt(0)\n\t"
      "v_bitop3_b32 v112, v149, v151, %[tag] bitop3:0x7e\n\tv_bitop3_b32 v113, v153, v155, %[tag] bitop3:0x7e\n\t"
      "v_max_f32 v115, v148, v152\n\tv_or_b32 v114, v112, v113\n\tv_max_f32 v116, v148, v152\n\t"
      "v_cmp_ne_u32 vcc, 0, v114\n\tv_lshlrev_b32 v122, 16, v148\n\ts_cbranch_vccnz .Lrp%=\n"
      ".Lrd%=:\n\t"
      "v_permlane16_swap_b32 v115, v116\n\tv_max_f32 v117, v115, v116\n\tv_max_f32 v118, v115, v116\n\t"
      "v_and_b32 v123, 0xffff0000, v148\n\tv_lshlrev_b32 v124, 16, v150\n\t"
      "v_permlane32_swap_b32 v117, v118\n\tv_max_f32 v117, v117, v118\n\t"
      "v_sub_f32 v115, v148, v117\n\tv_sub_f32 v116, v152, v117\n\tv_exp_f32 v115, v115\n\tv_exp_f32 v116, v116\n\t"
      "v_and_b32 v125, 0xffff0000, v150\n\tv_lshlrev_b32 v126, 16, v152\n\t"
      "v_add_f32 v117, v115, v116\n\tv_add_f32 v118, v115, v116\n\t"
      "v_mul_f32_dpp v122, v115, v122" BC12 "v_mul_f32_dpp v123, v115, v123" BC12
      "v_permlane16_swap_b32 v117, v118\n\tv_add_f32 v119, v117, v118\n\tv_add_f32 v120, v117, v118\n\t"
      "v_mul_f32_dpp v124, v115, v124" BC12 "v_mul_f32_dpp v125, v115, v125" BC12
      "v_permlane32_swap_b32 v119, v120\n\tv_add_f32 v119, v119, v120\n\tv_lshlrev_b32 v128, 16, v154\n\t"
      "v_rcp_f32 v119, v119\n\tv_and_b32 v127, 0xffff0000, v152\n\tv_and_b32 v129, 0xffff0000, v154\n\t"
      "v_fmac_f32_dpp v122, v116, v126" BC12 "v_fmac_f32_dpp v124, v116, v128" BC12
      "v_fmac_f32_dpp v123, v116, v127" BC12 "v_fmac_f32_dpp v125, v116, v129" BC12
      "v_permlane32_swap_b32 v122, v124\n\tv_add_f32 v122, v122, v124\n\t"
      "v_permlane32_swap_b32 v123, v125\n\tv_add_f32 v123, v123, v125\n\t"
      "v_mov_b32_dpp v120, v119" BC12 "s_nop 0\n\t"
      "v_permlane16_swap_b32 v122, v123\n\tv_add_f32 v122, v122, v123\n\t"
      "v_mul_f32 v123, v122, v120\n\ts_cmp_lt_u32 %[sp], 6\n\t"
      "v_mul_f32_dpp v124, v122, v120 quad_perm:[1,0,3,2] row_mask:0xf bank_mask:0xf\n\t"
      "v_cvt_pk_bf16_f32 v124, v123, v124\n\t"
      "s_cbranch_scc1 .Lyf%=\n\ts_nop 0\n\t"
      DOT8(64, 65, 66, 67, 68, 69, 70, 71, 0) DOT8(72, 73, 74, 75, 76, 77, 78, 79, 2) DOT8(80, 81, 82, 83, 84, 85, 86, 87, 4)
      DOT8(88, 89, 90, 91, 92, 93, 94, 95, 6) DOT8(96, 97, 98, 99, 100, 101, 102, 103, 8)
      DOT8(104, 105, 106, 107, 108, 109, 110, 111, 10) YRED
      "v_pk_add_f32 v[160:161], v[160:161], v[162:163]\n\tds_write_b64 %[ydl], v[160:161]\n"
      ".Lys%=:\n\t"
      "s_mov_b64 exec, 1\n\tds_add_rtn_u32 v112, %[cnt], %[one]\n\ts_mov_b64 exec, -1\n\t"
      "ds_read2st64_b64 v[116:119], %[ysrc] offset0:0 offset1:1\n\tds_read2st64_b64 v[120:123], %[ysrc] offset0:2 offset1:3\n\t"
      "s_waitcnt lgkmcnt(2)\n\tv_readfirstlane_b32 s98, v112\n\ts_waitcnt lgkmcnt(1)\n\t"
      "v_pk_add_f32 v[116:117], v[116:117], v[118:119]\n\ts_cmp_eq_u32 s98, 3\n\ts_cselect_b64 exec, %[my], 0\n\t"
      "s_waitcnt lgkmcnt(0)\n\tv_pk_add_f32 v[120:121], v[120:121], v[122:123]\n\ts_nop 0\n\t"
      "v_add_f32 v156, v116, v120\n\tv_add_f32 v158, v117, v121\n\t"
      "global_store_dwordx4 %[dst], v[156:159], off sc1\n\ts_mov_b64 exec, -1\n\ts_branch .Lye%=\n"
      ".Lyf%=:\n\ts_nop 0\n\t"
      "v_mov_b32_dpp v125, v124 row_newbcast:0 row_mask:0xf bank_mask:0x3\n\t"
      "v_mov_b32_dpp v126, v124 row_newbcast:2 row_mask:0xf bank_mask:0x3\n\t"
      "v_mov_b32_dpp v127, v124 row_newbcast:4 row_mask:0xf bank_mask:0x3\n\t"
      "v_mov_b32_dpp v125, v124 row_newbcast:6 row_mask:0xf bank_mask:0xc\n\t"
      "v_mov_b32_dpp v126, v124 row_newbcast:8 row_mask:0xf bank_mask:0xc\n\t"
      "v_mov_b32_dpp v127, v124 row_newbcast:10 row_mask:0xf bank_mask:0xc\n\t"
      DOT8F(88, 89, 90, 91, 92, 93, 94, 95, 125) DOT8F(96, 97, 98, 99, 100, 101, 102, 103, 126)
      DOT8F(104, 105, 106, 107, 108, 109, 110, 111, 127) YRED
      "v_add_f32 v160, v160, v162\n\tv_add_f32 v161, v161, v163\n\ts_nop 0\n\t"
      "v_add_f32_dpp v160, v160, v160 row_ror:8 row_mask:0xf bank_mask:0x3\n\t"
      "v_add_f32_dpp v160, v161, v161 row_ror:8 row_mask:0xf bank_mask:0xc\n\t"
      "ds_write_b32 %[ydl], v160\n\ts_branch .Lys%=\n"
      ".Lye%=:"
      : [n] "=&s"(np), "+{v[156:159]}"(rq), "+{v[148:151]}"(ra0), "+{v[152:155]}"(ra1)
      : [tag] "s"(tag), [p] "v"(pr), [max] "n"(SPIN_MAX), [sp] "s"(sp), [ydl] "v"(ydl), [cnt] "v"(cnt1), [one] "v"(one),
        [ysrc] "v"(ysrc), [my] "s"(my), [dst] "v"(ydst), "{v[64:71]}"(XP[0]), "{v[72:79]}"(XP[1]), "{v[80:87]}"(XP[2]),
        "{v[88:95]}"(WP[0]), "{v[96:103]}"(WP[1]), "{v[104:111]}"(WP[2])
      : CLOB_T, "v160", "v161", "v162", "v163", "v164", "v165", "v166", "v167", "s98", "s99", "vcc", "scc", "memory");
  STAMP(9);
  asm volatile("" ::"v"(dm0), "v"(dm1));
  STAMP_FLUSH();
  asm volatile(".Lcode_last:");
}

__global__ void __launch_bounds__(NT, 2)
fused_decode(const bf16* __restrict__ wq, const bf16* __restrict__ kc, const bf16* __restrict__ w1, const bf16* __restrict__ w3,
             u64* __restrict__ gw, const bf16* __restrict__ x, const bf16* __restrict__ vc,
             const bf16* __restrict__ wo, const bf16* __restrict__ w2, bf16* __restrict__ out) {
  __shared__ __attribute__((aligned(16))) float lds[L_END];
  const int b = blockIdx.x;
  const int xcd = b & 7, jj = b >> 3;
  const int h = xcd * 4 + (jj >> 3), sp = jj & 7;
  const int w = __builtin_amdgcn_readfirstlane(threadIdx.x >> 6);
  unsigned cpf = threadIdx.x;
  {
    u64 pc;
    unsigned len;
    asm volatile(
        "s_cmp_lt_u32 %[w], 4\n\ts_cbranch_scc1 .Lnocpf\n\t"
        "s_getpc_b64 %[pc]\n.Lcode_here:\n\tv_xor_b32 %[c], %[hi], %[c]\n\tv_lshlrev_b32 %[c], 6, %[c]\n\t"
        "s_mov_b32 %[len], .Lcode_last-.Lcode_here-4\n\ts_max_i32 %[len], %[len], .Lcode_lastb-.Lcode_here+1020\n\t"
        "v_min_u32 %[c], %[len], %[c]\n\tglobal_load_dword %[c], %[c], %[pc]\n.Lnocpf:"
        : [c] "+v"(cpf), [pc] "=&s"(pc), [len] "=&s"(len) : [w] "s"(w), [hi] "s"((b & CPM) << 5) : "scc", "memory");
  }
  SCHED_FENCE();
  if (w >= 4)
    body<false>(lds, h, sp, w, x, kc, vc, wq, wo, w1, w3, w2, gw, out, cpf);
  else
    body<true>(lds, h, sp, w, x, kc, vc, wq, wo, w1, w3, w2, gw, out, cpf);
}

}

extern "C" void kernel_launch(void* const* d_in, const int* in_sizes, int n_in, void* d_out, int out_size, void* d_ws,
                              size_t ws_size, hipStream_t stream) {
  const bf16* x = (const bf16*)d_in[0];
  const bf16* kc = (const bf16*)d_in[1];
  const bf16* vc = (const bf16*)d_in[2];
  const bf16* wq = (const bf16*)d_in[3];
  const bf16* wo = (const bf16*)d_in[4];
  const bf16* w1 = (const bf16*)d_in[6];
  const bf16* w3 = (const bf16*)d_in[7];
  const bf16* w2 = (const bf16*)d_in[8];
  u64* gw = (u64*)d_ws;
  fused_decode<<<NWG, NT, 0, stream>>>(wq, kc, w1, w3, gw, x, vc, wo, w2, (bf16*)d_out);
}
```

```cpp
#define FULLW 1
#include <hip/hip_runtime.h>
#include <hip/hip_bf16.h>
#include <stdint.h>

typedef __hip_bfloat16 bf16;
typedef unsigned long long u64;
typedef unsigned u32x4 __attribute__((ext_vector_type(4)));
typedef unsigned u32x8 __attribute__((ext_vector_type(8)));
typedef float f32x4 __attribute__((ext_vector_type(4)));
typedef float f32x2 __attribute__((ext_vector_type(2)));
typedef __bf16 bf16x2_t __attribute__((ext_vector_type(2)));

namespace {

constexpr int D = 640, HQ = 32, DH = 192, SK = 32768, WIN = 4096, FF = 1536;
constexpr int NSP = 8;
constexpr int NWG = HQ * NSP;
constexpr int NT = 512;
constexpr float SCALE = 0.07216878364870322f;
constexpr float LOG2E = 1.4426950408889634f;
constexpr unsigned SPIN_MAX = 1u << 16;
constexpr int SC1 = 16;
constexpr int NOWHERE = 1 << 30;

#define YP 0
#define RP 0
#define PV_ 81
#define PK_ 82
#define PX_ PK_
#define TS_ 358
#define SB_ 246
#define LOC 30
#define WT_ 98
#define W2T 132
#define W1N 3
#define PT_ 215
#define NFL 7
#define CE 7
#define CL 8
#define AR 3
#define GR 5
#define GRW 2
#define RSLEEP 1
#define MW 4
#define PM 0x1440
#define MEAS 1
#define ATHR 20
#define ASH 4
#define AGX 7
#define UTHR 8
#define UTH2 40
#define USH 4
#define UGX 9
#define ULIM 20
#define UMISS 60
#define PVL 1
#define LSP 16
#define GLP 1
#define E1P 1
#define UTA 5
#define USH2 3
#define CT 3
#define CPM 8
#define XS 6
#define LOCO 60
#define VT 10
#define CSH 30
#define X0 1
#define FW2 6
#define EBX 0
#define EBW 72
#define EBT 190
#define NW 0
#define NOST 0
#define OSC "sc1"
#define DB 8
#define DR0 5
#define DR1 8
#define FPX -1
#define PR_ 13
#define FT_ 0
#define PQ 0
#define FWM "-1"
#define HROT 16
constexpr int t_round(int r) { return r <= 2 ? TS_ : t_round(r - 1) + (r > 8 ? PV_ : r == 8 ? PX_ : PK_); }
constexpr int T_LAST = t_round(15);
constexpr int t_late(int i) { return t_round(i); }
constexpr int t_early(int i) { return t_round(i); }

constexpr int QS = 144;
constexpr int YB = 2048;
constexpr int RS = 128;
constexpr size_t GQ = 0;
constexpr size_t G2 = GQ + (size_t)NWG * QS;  static_assert(G2 % 2048 == 0, "16 KB");
constexpr size_t GY = G2 + (size_t)HQ * 2048;
constexpr size_t GZ = GY + 16;
constexpr size_t GEP = GY + (size_t)NSP * 5 * YB;
constexpr size_t GL = GEP + 16;

constexpr size_t GP = GL + 10 * 64 * (LSP / 8) + 1;
__device__ __forceinline__ unsigned fbits(float x) { return __builtin_bit_cast(unsigned, x); }
__device__ __forceinline__ u64 gld(const u64* p) {
  return __hip_atomic_load(p, __ATOMIC_RELAXED, __HIP_MEMORY_SCOPE_AGENT);
}
__device__ __forceinline__ float bf_lo(unsigned u) { return __builtin_bit_cast(float, u << 16); }
__device__ __forceinline__ float bf_hi(unsigned u) { return __builtin_bit_cast(float, u & 0xffff0000u); }
__device__ __forceinline__ float bfr(float x) { return __bfloat162float(__float2bfloat16(x)); }
__device__ __forceinline__ unsigned short bfbits(float x) {
  return __builtin_bit_cast(unsigned short, __float2bfloat16(x));
}
__device__ __forceinline__ float dot2(unsigned a, unsigned b, float c) {
  return __builtin_amdgcn_fdot2_f32_bf16(__builtin_bit_cast(bf16x2_t, a), __builtin_bit_cast(bf16x2_t, b), c, false);
}
template <int CTRL>
__device__ __forceinline__ float dpp(float x) {
  return __builtin_bit_cast(float, __builtin_amdgcn_mov_dpp(__builtin_bit_cast(int, x), CTRL, 0xf, 0xf, true));
}
__device__ __forceinline__ float grp8_sum(float x) {
  x += dpp<0xB1>(x);
  x += dpp<0x4E>(x);
  x += dpp<0x141>(x);
  return x;
}
__device__ __forceinline__ float sw16(float a, float b) {
  auto s = __builtin_amdgcn_permlane16_swap(fbits(a), fbits(b), false, false);
  return __builtin_bit_cast(float, (unsigned)s[0]) + __builtin_bit_cast(float, (unsigned)s[1]);
}
__device__ __forceinline__ float sw32(float a, float b) {
  auto s = __builtin_amdgcn_permlane32_swap(fbits(a), fbits(b), false, false);
  return __builtin_bit_cast(float, (unsigned)s[0]) + __builtin_bit_cast(float, (unsigned)s[1]);
}
__device__ __forceinline__ float xg_sum(float x) {
  x += dpp<0x128>(x);
  x = sw16(x, x);
  return sw32(x, x);
}
__device__ __forceinline__ float xg_max(float x) {
  x = fmaxf(x, dpp<0x128>(x));
  auto s = __builtin_amdgcn_permlane16_swap(fbits(x), fbits(x), false, false);
  x = fmaxf(__builtin_bit_cast(float, (unsigned)s[0]), __builtin_bit_cast(float, (unsigned)s[1]));
  auto t = __builtin_amdgcn_permlane32_swap(fbits(x), fbits(x), false, false);
  return fmaxf(__builtin_bit_cast(float, (unsigned)t[0]), __builtin_bit_cast(float, (unsigned)t[1]));
}
__device__ __forceinline__ float wave_sum(float x) { return xg_sum(grp8_sum(x)); }
__device__ __forceinline__ float ex2(float x) { return __builtin_amdgcn_exp2f(x); }
__device__ __forceinline__ float silu(float x) { return x / (1.0f + __expf(-x)); }
__device__ __forceinline__ float xg_reduce8(const float (&a)[8], bool g0) {
  float r[4];
#pragma unroll
  for (int i = 0; i < 4; ++i) r[i] = sw32(a[i], a[i + 4]);
  const float s0 = sw16(r[0], r[2]), s1 = sw16(r[1], r[3]);
  const float keep = g0 ? s1 : s0, send = g0 ? s0 : s1;
  return keep + dpp<0x128>(send);
}
__device__ __forceinline__ __amdgpu_buffer_rsrc_t rsrc(const void* p, unsigned bytes) {
  return __builtin_amdgcn_make_buffer_rsrc(const_cast<void*>(p), 0, bytes, 0x00020000);
}
__device__ __forceinline__ void gst16(u64* p, unsigned v0, unsigned v1, unsigned tag) {
  const u32x4 o = {v0, tag, v1, tag};
  asm volatile("global_store_dwordx4 %0, %1, off sc1\n\ts_nop 1" ::"v"(p), "v"(o) : "memory");
}
__device__ __forceinline__ unsigned bfpack(float a, float b) {
  return __builtin_bit_cast(unsigned, __builtin_convertvector(f32x2{a, b}, bf16x2_t));
}
__device__ __forceinline__ float2 rows_reduce(const float (&v)[8]) {
  float r[4];
#pragma unroll
  for (int i = 0; i < 4; ++i) r[i] = sw32(v[i], v[i + 4]);
  return float2{sw16(r[0], r[2]), sw16(r[1], r[3])};
}
template <int OFF>
__device__ __forceinline__ void xg_store(const float (&v)[8], float f, unsigned a) {
  float p[4], q[4], t, u;
#pragma unroll
  for (int i = 0; i < 4; ++i) {
    auto s = __builtin_amdgcn_permlane32_swap(fbits(v[i]), fbits(v[i + 4]), false, false);
    p[i] = __builtin_bit_cast(float, (unsigned)s[0]);
    q[i] = __builtin_bit_cast(float, (unsigned)s[1]);
  }
  const f32x2 r01 = f32x2{p[0], p[1]} + f32x2{q[0], q[1]}, r23 = f32x2{p[2], p[3]} + f32x2{q[2], q[3]};
  auto s0 = __builtin_amdgcn_permlane16_swap(fbits(r01[0]), fbits(r23[0]), false, false);
  auto s1 = __builtin_amdgcn_permlane16_swap(fbits(r01[1]), fbits(r23[1]), false, false);
  asm volatile("v_add_f32 %0, %2, %3\n\tv_add_f32 %1, %4, %5\n\ts_nop 0\n\t"
               "v_add_f32_dpp %0, %0, %0 row_ror:8 row_mask:0xf bank_mask:0x3\n\t"
               "v_add_f32_dpp %0, %1, %1 row_ror:8 row_mask:0xf bank_mask:0xc\n\t"
               "v_mul_f32 %0, %6, %0\n\tds_write_b32 %7, %0 offset:%8"
               : "=&v"(t), "=&v"(u)
               : "v"((unsigned)s0[0]), "v"((unsigned)s0[1]), "v"((unsigned)s1[0]), "v"((unsigned)s1[1]), "v"(f), "v"(a), "n"(OFF)
               : "memory");
}
#define SUM8(a)                                                                                                          \
  "ds_read_b128 v[116:119], " a "\n\tds_read_b128 v[120:123], " a " offset:768\n\t"                                      \
  "ds_read_b128 v[124:127], " a " offset:1536\n\tds_read_b128 v[128:131], " a " offset:2304\n\t"                         \
  "ds_read_b128 v[132:135], " a " offset:3072\n\tds_read_b128 v[136:139], " a " offset:3840\n\t"                         \
  "ds_read_b128 v[140:143], " a " offset:4608\n\tds_read_b128 v[144:147], " a " offset:5376\n\ts_waitcnt lgkmcnt(6)\n\t"
#define SUM8B                                                                                                            \
  "v_pk_add_f32 v[116:117], v[116:117], v[120:121]\n\tv_pk_add_f32 v[118:119], v[118:119], v[122:123]\n\t"               \
  "s_waitcnt lgkmcnt(4)\n\t"                                                                                             \
  "v_pk_add_f32 v[124:125], v[124:125], v[128:129]\n\tv_pk_add_f32 v[126:127], v[126:127], v[130:131]\n\t"               \
  "v_pk_add_f32 v[116:117], v[116:117], v[124:125]\n\tv_pk_add_f32 v[118:119], v[118:119], v[126:127]\n\t"
#define SUM8C                                                                                                            \
  "s_waitcnt lgkmcnt(2)\n\t"                                                                                             \
  "v_pk_add_f32 v[132:133], v[132:133], v[136:137]\n\tv_pk_add_f32 v[134:135], v[134:135], v[138:139]\n\t"               \
  "s_waitcnt lgkmcnt(0)\n\t"                                                                                             \
  "v_pk_add_f32 v[140:141], v[140:141], v[144:145]\n\tv_pk_add_f32 v[142:143], v[142:143], v[146:147]\n\t"               \
  "v_pk_add_f32 v[132:133], v[132:133], v[140:141]\n\tv_pk_add_f32 v[134:135], v[134:135], v[142:143]\n\t"               \
  "v_pk_add_f32 v[116:117], v[116:117], v[132:133]\n\tv_pk_add_f32 v[118:119], v[118:119], v[134:135]\n\t"
#define BC12 " row_newbcast:12 row_mask:0xf bank_mask:0xf\n\t"
#define DOTB(y, a, n) "v_dot2c_f32_bf16_dpp v" #y ", v124, v" #a " row_newbcast:" #n " row_mask:0xf bank_mask:0xf\n\t"
#define DOT8(a, b, c, d, e, f, g, h, n) \
  DOTB(160, a, n) DOTB(161, b, n) DOTB(162, c, n) DOTB(163, d, n) DOTB(164, e, n) DOTB(165, f, n) DOTB(166, g, n) DOTB(167, h, n)
#define DOTF(y, a, n) "v_dot2c_f32_bf16 v" #y ", v" #n ", v" #a "\n\t"
#define DOT8F(a, b, c, d, e, f, g, h, n) \
  DOTF(160, a, n) DOTF(161, b, n) DOTF(162, c, n) DOTF(163, d, n) DOTF(164, e, n) DOTF(165, f, n) DOTF(166, g, n) DOTF(167, h, n)
#define YRED                                                                                                             \
  "v_permlane32_swap_b32 v160, v164\n\tv_permlane32_swap_b32 v161, v165\n\tv_permlane32_swap_b32 v162, v166\n\t"         \
  "v_permlane32_swap_b32 v163, v167\n\tv_pk_add_f32 v[160:161], v[160:161], v[164:165]\n\t"                              \
  "v_pk_add_f32 v[162:163], v[162:163], v[166:167]\n\ts_nop 1\n\t"                                                       \
  "v_permlane16_swap_b32 v160, v162\n\tv_permlane16_swap_b32 v161, v163\n\t"
#define CLOB_T                                                                                                     \
  "v112", "v113", "v114", "v115", "v116", "v117", "v118", "v119", "v120", "v121", "v122", "v123", "v124", "v125",        \
      "v126", "v127", "v128", "v129", "v130", "v131", "v132", "v133", "v134", "v135", "v136", "v137", "v138", "v139",    \
      "v140", "v141", "v142", "v143", "v144", "v145", "v146", "v147"
#define SCHED_FENCE() __builtin_amdgcn_sched_barrier(0)
#define STR2(x) #x
#define STR(x) STR2(x)
#define THRN(n) do { SCHED_FENCE(); asm volatile("s_waitcnt vmcnt(%0)" ::"n"(n) : "memory"); SCHED_FENCE(); } while (0)
#define NOWT() ((unsigned)__builtin_amdgcn_s_memrealtime())
template <int N>
struct IC {
  static constexpr int v = N;
};
template <int I, int E, class F>
__device__ __forceinline__ void sfor(F&& f) {
  if constexpr (I < E) {
    f(IC<I>{});
    sfor<I + 1, E>(f);
  }
}
#define LDS_DRAIN() asm volatile("s_waitcnt lgkmcnt(0)" ::: "memory")
__device__ __forceinline__ unsigned lds_add(float* lds, int idx) {
  return __hip_atomic_fetch_add(reinterpret_cast<unsigned*>(lds + idx), 1u, __ATOMIC_RELAXED, __HIP_MEMORY_SCOPE_WORKGROUP);
}
__device__ __forceinline__ void lds_wait(float* lds, int idx, unsigned n) {
  SCHED_FENCE();
#pragma unroll 1
  for (unsigned spin = 0; spin < (SPIN_MAX << 4); ++spin) {
    if (__hip_atomic_load(reinterpret_cast<unsigned*>(lds + idx), __ATOMIC_RELAXED, __HIP_MEMORY_SCOPE_WORKGROUP) >= n) break;
    __builtin_amdgcn_s_sleep(1);
  }
  asm volatile("" ::: "memory");
  SCHED_FENCE();
}
template <int T, int R>
__device__ __forceinline__ void pace(unsigned clk, int ph) {
  unsigned o;
  SCHED_FENCE();
  asm volatile(
      "s_branch .Lpb%=\n"
      ".Lpa%=:\n\ts_sleep 1\n"
      ".Lpb%=:\n\tds_read_b32 %0, %1 offset:%4\n\ts_memrealtime s[98:99]\n\ts_waitcnt lgkmcnt(0)\n\tv_readfirstlane_b32 s99, %0\n\t"
      "s_sub_u32 s98, s98, %2\n\ts_sub_u32 s98, s98, s99\n\ts_cmp_lt_i32 s98, %3\n\ts_cbranch_scc1 .Lpa%="
      : "=&v"(o) : "v"(clk), "s"(ph), "n"(T), "n"(4 * R) : "s98", "s99", "scc", "memory");
  SCHED_FENCE();
}

template <int T, int R>
__device__ __forceinline__ unsigned pace_m(unsigned clk, int ph) {
  unsigned o, tl;
  SCHED_FENCE();
  asm volatile(
      "s_mov_b32 %1, 0\n\ts_branch .Lpb%=\n"
      ".Lpa%=:\n\ts_sleep 1\n"
      ".Lpb%=:\n\tds_read_b32 %0, %2 offset:%5\n\ts_getreg_b32 s97, hwreg(HW_REG_IB_STS, 0, 4)\n\ts_memrealtime s[98:99]\n\t"
      "s_or_b32 s97, s97, %1\n\ts_waitcnt lgkmcnt(0)\n\ts_cselect_b32 %1, %1, s98\n\tv_readfirstlane_b32 s99, %0\n\t"
      "s_sub_u32 s98, s98, %3\n\ts_sub_u32 s98, s98, s99\n\ts_cmp_lt_i32 s98, %4\n\ts_cbranch_scc1 .Lpa%="
      : "=&v"(o), "=&s"(tl) : "v"(clk), "s"(ph), "n"(T), "n"(4 * R) : "s97", "s98", "s99", "scc", "memory");
  SCHED_FENCE();
  return tl;
}

#define STAMP(i)
#define STAMP_FLUSH()
#define TT(i)
constexpr int L_QP = 0;
constexpr int L_QS = L_QP + 4 * 128;
constexpr int L_PART = L_QS + 96;
constexpr int L_ML = L_PART + 8 * DH;
constexpr int L_Z = L_ML + 16;
constexpr int L_Y = L_Z + 320;
constexpr int L_H = L_Y + 512;
constexpr int L_SY = L_H + 64;
constexpr int L_CK = L_SY + 8;
constexpr int L_TE = L_CK + 16;
constexpr int L_END = L_TE + 8;

#define PIN(x) asm volatile("" : "+v"(x))

__device__ __forceinline__ void wg_ml(const float* lds, float& M, float& L) {
  float mm[8], ll[8];
#pragma unroll
  for (int i = 0; i < 4; ++i) {
    const f32x4 t = *reinterpret_cast<const f32x4*>(lds + L_ML + 4 * i);
    mm[2 * i] = t[0];
    ll[2 * i] = t[1];
    mm[2 * i + 1] = t[2];
    ll[2 * i + 1] = t[3];
  }
  M = mm[0];
#pragma unroll
  for (int ww = 1; ww < 8; ++ww) M = fmaxf(M, mm[ww]);
  L = 0.f;
#pragma unroll
  for (int ww = 0; ww < 8; ++ww) L = fmaf(ex2((mm[ww] - M) * LOG2E), ll[ww], L);
}
__device__ __forceinline__ unsigned lds_addr(const float* p) {
  return (unsigned)(size_t)(const __attribute__((address_space(3))) float*)p;
}

template <bool early>
__device__ __forceinline__ void body(float* lds, const int h, const int sp, const int w, const bf16* __restrict__ x,
                                     const bf16* __restrict__ kc, const bf16* __restrict__ vc,
                                     const bf16* __restrict__ wq, const bf16* __restrict__ wo,
                                     const bf16* __restrict__ w1, const bf16* __restrict__ w3,
                                     const bf16* __restrict__ w2, u64* __restrict__ gw, bf16* __restrict__ out,
                                     unsigned cpf) {
  const int b = blockIdx.x;
  const int tid = threadIdx.x, lane = tid & 63;
  const int g = lane >> 3, c = lane & 7;
  const int rec = h * NSP + sp;

  STAMP(0);
  const bool isF = sp < 6;
  u32x4 PR[8], xa, xb, xq;
  unsigned epoch = 0;
  u64 tk = 0;
  int ra = 0, RA0 = 0;
  bool va = false;
  if constexpr (early) {
    int s0_ = 240, s1_ = 80, s2_ = 240 * (sp & 1), s3_ = 80 * sp, s4_ = 64 * (sp >> 1), s5_ = 64 * w, s6_ = 24 * w;
    asm("" : "+s"(s0_), "+s"(s1_), "+s"(s2_), "+s"(s3_), "+s"(s4_), "+s"(s5_), "+s"(s6_));
    const int rw0 = isF ? s5_ : s6_;
    int lim = (isF ? s0_ : s1_) - rw0, lim2 = isF ? 64 : 24;
    asm("" : "+s"(lim), "+s"(lim2));
    lim = lim < lim2 ? lim : lim2;
    ra = rw0 + 8 * g;
    va = 8 * g < lim;
    RA0 = isF ? s2_ : s3_;
    int aoff = (int)__umul24(g, 8 * HQ * DH * 2) + 16 * c;
    PIN(aoff);
    aoff = va ? aoff : NOWHERE;
    int sa = ((RA0 + rw0) * (HQ * DH) + h * DH + (isF ? s4_ : 64)) * 2;
    PIN(aoff);
    asm volatile("" : "+s"(sa));
    SCHED_FENCE();
    const __amdgpu_buffer_rsrc_t rw = rsrc(wq, NOWHERE);
    PR[0] = __builtin_amdgcn_raw_buffer_load_b128(rw, aoff, sa, 2);
    SCHED_FENCE();
    tk = __builtin_amdgcn_s_memrealtime();
    SCHED_FENCE();
    asm volatile("s_load_dword %0, %1, %2" : "=&s"(epoch) : "s"(gw), "s"(E1P && w == 1 ? (int)GL * 8 + (sp * 8 + (b & 7)) * LSP : (int)GEP * 8) : "memory");
    SCHED_FENCE();
#pragma unroll
    for (int i = 1; i < 8; ++i) PR[i] = __builtin_amdgcn_raw_buffer_load_b128(rw, aoff, sa + i * (HQ * DH * 2), 2);
    SCHED_FENCE();
    SCHED_FENCE();
  }
  const unsigned short* xs = reinterpret_cast<const unsigned short*>(x);
  const size_t wgb = ((size_t)h * SK + (SK - WIN)) * DH;
  const __amdgpu_buffer_rsrc_t rk = rsrc(kc + wgb, WIN * DH * 2);
  const __amdgpu_buffer_rsrc_t rv = rsrc(vc + wgb, WIN * DH * 2);
  const int voff = (g * DH + 8 * c) * 2;
  auto tile = [&](int t) { return (t * 64 + h * HROT + w * 8 + sp) & 511; };
  u32x4 K[8][3], V[8][3];
  auto kld = [&](int t, int j) { K[t][j] = __builtin_amdgcn_raw_buffer_load_b128(rk, voff + 128 * j, tile(t) * 3072, 2); };
  auto vld = [&](int t, int j) { V[t][j] = __builtin_amdgcn_raw_buffer_load_b128(rv, voff + 128 * j, tile(t) * 3072, 2); };
  unsigned tag = 0;
  const unsigned clk = lds_addr(lds + L_CK);
  const int ph = (w * PV_) >> 3;
  const int r4 = lane >> 4, c16 = lane & 15;
  const int ch = c16 < 10 ? c16 : 9;
  const int fi = (h & 3) * 6 + (isF ? sp : 5), gl = fi >> 3, gI = (h >> 2) * 3 + gl, fr = fi & 7;
  u32x4 WO[6], X[6], W2[4];
  unsigned tch = 0;
  const int wooff = ((48 * w + r4 + (isF ? 24 * (g & 1) : 24)) * D + 64 * sp + 8 * c + (isF ? 0 : 128 * (g & 1))) * 2;
  typedef unsigned u32x2 __attribute__((ext_vector_type(2)));
  const __amdgpu_buffer_rsrc_t rl = rsrc(gw + GL, 16 * 64 * LSP);
  const bool pub = !((sp + (b >> 6)) & 3);
  const int lw_ = lane == 0 && pub ? (sp * 8 + (b & 7)) * LSP : NOWHERE;
  u32x4 lat = {0u, 0u, 0u, 0u};
  auto fetch = [&](int off) {
    SCHED_FENCE();
    asm volatile("buffer_load_dwordx4 %0, %1, %2, %3 offen sc1" : "+v"(lat) : "v"(lane * LSP), "s"(rl), "s"(off) : "memory");
    SCHED_FENCE();
  };
  u64 own = 0, pre = 0;
  unsigned pro = 0, st1 = 0, old = 0;
  bool vslow = false, kslow = false;
  float sh = 0.f;
  if constexpr (early) {
    u32x4 PF[5];
    const int F0 = (isF ? 160 * (fr >> 1) : 480) + 40 * w;
    const int fs = isF ? FF : HQ * DH;
    int s6_ = 64 * gI, s7_ = h * DH + 128 * (sp & 1);
    asm("" : "+s"(s6_), "+s"(s7_));
    const int boff = ((int)__umul24(g, fs) + 8 * c) * 2;
    const int sb = (F0 * fs + (isF ? s6_ : s7_)) * 2;
    const bf16* pf_ = (fr & 1) ? w3 : w1;
    asm("" : "+s"(pf_));
    const __amdgpu_buffer_rsrc_t rf = rsrc(isF ? pf_ : wq, NOWHERE);
    unsigned short xr[5];
#pragma unroll
    for (int i = 0; i < 5; ++i) PF[i] = __builtin_amdgcn_raw_buffer_load_b128(rf, boff, sb + i * 16 * fs, 2);
    SCHED_FENCE();
    if (!isF) {
#pragma unroll
      for (int i = 0; i < XS; ++i)
        X[i] = __builtin_amdgcn_raw_buffer_load_b128(rsrc(wo + (size_t)h * DH * D, DH * D * 2), wooff - 24 * D * 2 + i * (4 * D * 2), 0, 2);
    } else {
#pragma unroll
      for (int i = 0; i < XS; ++i) X[i] = u32x4{0u, 0u, 0u, 0u};
    }
    SCHED_FENCE();
    xa = *reinterpret_cast<const u32x4*>(x + 8 * lane);
    xb = *reinterpret_cast<const u32x4*>(x + 512 + 8 * (lane & 15));
    xq = *reinterpret_cast<const u32x4*>(x + RA0 + (va ? ra : 0));
#pragma unroll
    for (int i = 0; i < 5; ++i) xr[i] = xs[F0 + 8 * i + g];
    SCHED_FENCE();
    const unsigned epv = __hip_atomic_load(reinterpret_cast<unsigned*>(gw + GEP), __ATOMIC_RELAXED, __HIP_MEMORY_SCOPE_AGENT);
    SCHED_FENCE();
    asm volatile("" : "+s"(tk));
    if (tid < 17) reinterpret_cast<unsigned*>(lds)[L_CK + tid] = (unsigned)tk;
    asm volatile("s_waitcnt lgkmcnt(0)\n\ts_barrier" : "+s"(epoch)::"memory");
    STAMP(6);
    SCHED_FENCE();
#pragma unroll
    for (int t = 0; t < 3; ++t)
#pragma unroll
      for (int j = 0; j < 3; ++j) kld(t, j);
    SCHED_FENCE();
    float mb, den;
    {
      float xf[8], xg[8];
#pragma unroll
      for (int i = 0; i < 4; ++i) {
        xf[2 * i] = bf_lo(xa[i]);
        xf[2 * i + 1] = bf_hi(xa[i]);
        xg[2 * i] = bf_lo(xb[i]);
        xg[2 * i + 1] = bf_hi(xb[i]);
      }
      const bool lo16 = lane < 16;
      float sum = 0.f;
#pragma unroll
      for (int i = 0; i < 8; ++i) sum += xf[i] + (lo16 ? xg[i] : 0.f);
      sum = wave_sum(sum);
      const float mean = sum * (1.0f / D);
      float sq = 0.f;
#pragma unroll
      for (int i = 0; i < 8; ++i) {
        const float d1 = xf[i] - mean, d2 = xg[i] - mean;
        sq += d1 * d1 + (lo16 ? d2 * d2 : 0.f);
      }
      sq = wave_sum(sq);
      mb = bfr(mean);
      const float vb = bfr(sq * (1.0f / D));
      den = bfr(sqrtf(bfr(vb + 1e-5f)));
    }
    {
      unsigned xn[13];
#pragma unroll
      for (int i = 0; i < 4; ++i) {
        xn[2 * i] = fbits(bfr(bfr(bf_lo(xq[i]) - mb) / den));
        xn[2 * i + 1] = fbits(bfr(bfr(bf_hi(xq[i]) - mb) / den));
      }
#pragma unroll
      for (int i = 0; i < 5; ++i) xn[8 + i] = fbits(bfr(bfr(bf_lo(xr[i]) - mb) / den));
#pragma unroll
      for (int i = 0; i < 13; ++i) PIN(xn[i]);
      SCHED_FENCE();
      pace<t_round(2), 2>(clk, ph);
#pragma unroll
      for (int j = 0; j < 3; ++j) kld(3, j);
      TT(2);
      SCHED_FENCE();
      float fa[8], fg[8];
#pragma unroll
      for (int k = 0; k < 8; ++k) fa[k] = fg[k] = 0.f;
#pragma unroll
      for (int i = 0; i < 8; ++i)
#pragma unroll
        for (int k = 0; k < 4; ++k) {
          fa[2 * k] = dot2(PR[i][k], xn[i] >> 16, fa[2 * k]);
          fa[2 * k + 1] = dot2(PR[i][k], xn[i], fa[2 * k + 1]);
        }
      lds[L_QP + w * 128 + 8 * c + g] = xg_reduce8(fa, g & 1);
#pragma unroll
      for (int i = 0; i < 5; ++i)
#pragma unroll
        for (int k = 0; k < 4; ++k) {
          fg[2 * k] = dot2(PF[i][k], xn[8 + i] >> 16, fg[2 * k]);
          fg[2 * k + 1] = dot2(PF[i][k], xn[8 + i], fg[2 * k + 1]);
        }
      lds[L_QP + w * 128 + 64 + 8 * c + g] = xg_reduce8(fg, g & 1);
    }
    SCHED_FENCE();
    tag = __builtin_amdgcn_readfirstlane(epv) + 1u;
    if (tid == 0) reinterpret_cast<unsigned*>(lds)[L_SY + 3] = tag;
    LDS_DRAIN();
    if (lane == 0) old = lds_add(lds, L_SY);
    old = __builtin_amdgcn_readfirstlane(old);
    if (old == 3) {
      const float2* qp = reinterpret_cast<const float2*>(lds + L_QP) + lane;
      const float2 p0_ = qp[0], p1_ = qp[64], p2_ = qp[128], p3_ = qp[192];
      gst16(gw + GQ + (size_t)rec * QS + 2 * lane, fbits((p0_.x + p1_.x) + (p2_.x + p3_.x)),
            fbits((p0_.y + p1_.y) + (p2_.y + p3_.y)), tag);
    }
    STAMP(11);
  } else {
    if (tid >= NT - 8) reinterpret_cast<unsigned*>(lds)[L_SY + tid - (NT - 8)] = 0u;
    unsigned st0 = 0;
    {
      const bf16* tp = w == 4 ? kc + wgb : w == 5 ? (!isF ? wo + (size_t)h * DH * D : (fr & 1) ? w3 : w1) : w == 6 ? ((sp & 1) ? x : wq) : vc + wgb;
      asm volatile("s_waitcnt lgkmcnt(0)\n\ts_load_dword %0, %1, 0x0" : "=&s"(st0) : "s"(tp) : "memory");
    }
    SCHED_FENCE();
#pragma unroll
    for (int j = 0; j < 3; ++j) kld(0, j);
    SCHED_FENCE();
    asm volatile("s_barrier" ::: "memory");
    STAMP(6);
    SCHED_FENCE();
    if (FULLW ? w == 5 && pub : w < 6) {
      const int k = lane & 7;
      const int yg = 4 * (k < 4 || isF ? sp : sp + 2) + (k & 3);
      unsigned o4 = (unsigned)(lane & 8 ? GY + (size_t)yg * YB + 32 * h : G2 + (size_t)(16 * h + sp) * RS + 16 * k) + 64 * (lane & 16);
      unsigned o5 = (unsigned)(GQ + (size_t)rec * QS + (PQ ? 16 * k : 0));
      if (GLP && pub && lane >= 16 && lane < 32 && ((PM >> (lane - 16)) & 1))
        o5 = (unsigned)GL + (unsigned)(((lane - 16) * 64 + sp * 8 + (b & 7)) * (LSP / 8));
      PIN(o4);
      PIN(o5);
      pre = gld(gw + (w == 4 ? o4 : o5));
    }
    if (w == 7 && (h < 4 || b == DB + 1)) {
      const unsigned* pp = h < 4 ? reinterpret_cast<const unsigned*>(out + 80 * sp + 16 * (lane >= 8 ? 4 : h)) + (lane & 7)
                                 : reinterpret_cast<const unsigned*>(gw + GZ + (size_t)((lane >> 3) & 3) * YB + 32 * (24 + c) + 1024 * (lane >> 5));
      pro = __hip_atomic_load(pp, __ATOMIC_RELAXED, __HIP_MEMORY_SCOPE_AGENT);
    }
    SCHED_FENCE();
    asm volatile("s_waitcnt lgkmcnt(0)" ::"s"(st0) : "memory");
#pragma unroll
    for (int j = 0; j < 3; ++j) {
      kld(1, j);
      THRN(5);
    }
    THRN(3);
    TT(1);
    own = __builtin_amdgcn_s_memrealtime() - SB_;
    __builtin_amdgcn_raw_buffer_store_b64(u32x2{(unsigned)own, (unsigned)(own >> 32)}, rl, w == 6 ? lw_ : NOWHERE, 0, SC1);
    SCHED_FENCE();
    if (LOC && w == 6 && lane < 16) {
      const unsigned o = (unsigned)own - (isF ? LOC : LOCO);
      if ((int)(o - reinterpret_cast<unsigned*>(lds)[L_CK + lane]) > 0) reinterpret_cast<unsigned*>(lds)[L_CK + lane] = o;
    }
    SCHED_FENCE();
    {
      const bf16* tp = (w & 1) ? wo + (size_t)h * DH * D : w2 + (size_t)gI * 64 * D;
      asm volatile("s_load_dword %0, %1, 0x0" : "=&s"(st1) : "s"(tp) : "memory");
    }
    SCHED_FENCE();
  }
  u32x4 qf[3];
  float sc[8], mw, lw, acc[24];
  unsigned pb[8];
  auto score = [&](int t) {
    float d = 0.f;
#pragma unroll
    for (int j = 0; j < 3; ++j)
#pragma unroll
      for (int i = 0; i < 4; ++i) d = dot2(K[t][j][i], qf[j][i], d);
    d = grp8_sum(d);
    sc[t] = bfr(bfr(d) * SCALE);
  };
  auto pv = [&](int t) {
    const unsigned plo = pb[t], phi = plo << 16;
#pragma unroll
    for (int j = 0; j < 3; ++j)
#pragma unroll
      for (int i = 0; i < 4; ++i) {
        acc[8 * j + 2 * i] = dot2(V[t][j][i], plo, t ? acc[8 * j + 2 * i] : 0.f);
        acc[8 * j + 2 * i + 1] = dot2(V[t][j][i], phi, t ? acc[8 * j + 2 * i + 1] : 0.f);
        PIN(acc[8 * j + 2 * i]);
        PIN(acc[8 * j + 2 * i + 1]);
      }
  };
  const bool hf = lane >> 5;
  const __amdgpu_buffer_rsrc_t rq_ = rsrc(gw + GQ, NWG * QS * 8);
  int go[9];
  {
    const int l16 = (lane & 31) * 16;
    const int qa = l16 + (8 * h + (hf ? 4 : 0)) * (QS * 8);
    go[0] = qa;
    go[1] = qa + QS * 8;
    go[2] = l16 + (8 * h + (hf ? 7 : 6)) * (QS * 8) + 512;
    go[3] = qa + 2 * QS * 8;
    go[4] = qa + 3 * QS * 8;
#pragma unroll
    for (int k = 0; k < 4; ++k) {
      const int m = 8 * gl + 2 * k + (hf ? 1 : 0);
      go[5 + k] = isF ? l16 + (32 * (h >> 2) + m + (((m * 43) >> 7) & 6)) * (QS * 8) + 512 : NOWHERE;
    }
  }
  const bool gath = old == GRW;
  u32x4 pa[9];
  float f = 0.f;
  unsigned cw = 0;
  int yp = 0, r0 = 0;
  const int l40 = lane < 40 ? lane : 39;
  u64 *rdst = gw, *zdst = gw;
  auto prep = [&]() {
    lds_wait(lds, L_SY + 4, 8u);
    float M, L;
    wg_ml(lds, M, L);
    f = ex2((mw - M) * LOG2E) / L;
    cw = fbits(fmaf(M, LOG2E, __builtin_amdgcn_logf(L)));
    PIN(f);
    PIN(cw);
    lds_wait(lds, L_SY + 5, 1u);
    yp = __builtin_amdgcn_readfirstlane(reinterpret_cast<const unsigned*>(lds)[L_SY + 5]) >> 1;
    r0 = h * 16 + 8 * (((h >> 4) ^ yp) & 1);
    rdst = gw + G2 + (size_t)(r0 + sp) * RS + 32 * r4 + 2 * c16;
    zdst = gw + GZ + (size_t)(fr * 5 + (l40 >> 3)) * YB + 32 * gI + 2 * (l40 & 7);
    PIN(rdst);
    PIN(zdst);
    SCHED_FENCE();
  };
  constexpr int C0 = early ? CE : CL;
  static_assert(C0 + 3 < 15 && C0 > GR + 1 && C0 >= 6, "schedule");
  sfor<early ? 3 : 2, 16>([&](auto R) {
    constexpr int r = R.v, t = r >= 8 ? r - 8 : r + early;
    unsigned tl = 0;
    if constexpr (!early && MEAS && ((PM >> r) & 1)) tl = pace_m<t_round(r), r>(clk, ph); else pace<t_round(r), r>(clk, ph);
    if constexpr (!early && ((PM >> (r - 1)) & 1)) {
      if (w == MW) fetch(64 * LSP * (r - 1));
    }
    if constexpr (r < 8 && t == 8) {
      const __amdgpu_buffer_rsrc_t rx = isF ? rsrc(w2 + ((size_t)gI * 64 + 16 * w) * D + 80 * fr, 16 * D * 2) : rsrc(wo + (size_t)h * DH * D, DH * D * 2);
#pragma unroll
      for (int i = 0; i < 4; ++i)
        W2[i] = __builtin_amdgcn_raw_buffer_load_b128(rx, isF ? (r4 * D + 8 * ch) * 2 : i < 6 - XS ? wooff - (24 - 4 * XS) * D * 2 : NOWHERE, i * (4 * D * 2), 2);
      tch = __builtin_amdgcn_raw_buffer_load_b32(rsrc(wo + (size_t)h * DH * D, DH * D * 2), tid ? NOWHERE : wooff, 0, 2);
    } else {
#pragma unroll
      for (int j = 0; j < 3; ++j) {
        if (r >= 8) vld(t, j); else kld(t, j);
        if (r == 15 && j == 2) break;
        if (early && r == GR + 1) continue;
        THRN(early && r == 8 ? NFL + 2 : NFL);
      }
    }
    TT(r);
    SCHED_FENCE();
    if constexpr (early) {
      if (r == GR && gath) {
#pragma unroll
        for (int s = 0; s < 9; ++s) pa[s] = __builtin_amdgcn_raw_buffer_load_b128(rq_, go[s], 0, SC1);
        SCHED_FENCE();
      }
      if (r == GR + 1) {
        if (gath) {
          const unsigned tf = isF ? tag : 0u;
          bool ok = true;
#pragma unroll
          for (int s = 0; s < 9; ++s) ok = ok & (pa[s][1] == (s < 5 ? tag : tf)) & (pa[s][3] == (s < 5 ? tag : tf));
          if (!__all(ok)) {
            for (unsigned spin = 0;; ++spin) {
              ok = true;
              asm volatile("" ::: "memory");
#pragma unroll
              for (int s = 0; s < 9; ++s) {
                pa[s] = __builtin_amdgcn_raw_buffer_load_b128(rq_, go[s], 0, SC1);
                ok = ok & (pa[s][1] == (s < 5 ? tag : tf)) & (pa[s][3] == (s < 5 ? tag : tf));
              }
              if (__all(ok) || spin >= SPIN_MAX) break;
              __builtin_amdgcn_s_sleep(1);
            }
          }
          float qo[2], qm[2], hu[2];
#pragma unroll
          for (int k = 0; k < 2; ++k) {
            auto val = [&](int s) { return __builtin_bit_cast(float, (unsigned)pa[s][2 * k]); };
            qo[k] = (val(0) + val(1)) + val(2);
            const float t_ = val(3) + val(4);
            qm[k] = sw32(t_, t_);
            const float u = bfr((val(5) + val(6)) + (val(7) + val(8)));
            auto sw = __builtin_amdgcn_permlane32_swap(fbits(u), fbits(u), false, false);
            hu[k] = bfr(bfr(silu(__builtin_bit_cast(float, (unsigned)sw[0]))) * __builtin_bit_cast(float, (unsigned)sw[1]));
          }
          reinterpret_cast<unsigned*>(lds + L_QS)[lane + (hf ? 32 : 0)] = bfpack(qo[0], qo[1]);
          if (hf) {
            reinterpret_cast<unsigned*>(lds + L_QS)[lane] = bfpack(qm[0], qm[1]);
          } else {
            *reinterpret_cast<float2*>(lds + L_H + 2 * lane) = float2{hu[0], hu[1]};
          }
          LDS_DRAIN();
          if (lane == 0) lds_add(lds, L_SY + 2);
        }
        STAMP(2);
      }
    } else {
      if (r == 2) asm volatile("" ::"s"(st1), "v"(cpf), "v"(pre), "v"(pro));
      if (r == AR && w == 6) fetch(0);
      if (r == AR + 1 && w == 6) {
        asm volatile("s_waitcnt vmcnt(3)" : "+v"(lat)::"memory");
        const u64 d = (((u64)lat[1] << 32) | lat[0]) - own + 200u;
        const bool ok = d <= 400u;
        const float s_ = wave_sum(ok ? (float)(unsigned)d : 0.f), n = (float)__builtin_popcountll(__ballot(ok));
        const unsigned o = (unsigned)own + (n > 0.f ? (unsigned)(s_ * __builtin_amdgcn_rcpf(n)) - 200u : 0u);
        if (w == 6 && lane < 16) reinterpret_cast<unsigned*>(lds)[L_CK + lane] = o;
        if (w == 6 && lane == 0) reinterpret_cast<unsigned*>(lds)[L_TE + 4] = o;
      }
      if (r == AR + 2) {
        lds_wait(lds, L_SY, 4u);
        tag = __builtin_amdgcn_readfirstlane(reinterpret_cast<const unsigned*>(lds)[L_SY + 3]);
      }
      if (r >= DR0 && r < DR1 && b == DB && w > 4) {
        const int n = (r - DR0) * 3 + w - 5, oa = ((int)GZ + (n >> 3) * YB + 32 * (24 + (n & 7)) + 1024 * (r & 1)) * 8;
        u64 ta, tb, tc;
        unsigned dd;
        asm volatile(
            "s_memtime %0\n\ts_load_dword %3, %4, %5\n\ts_waitcnt lgkmcnt(0)\n\t"
            "s_memtime %1\n\ts_load_dword %3, %4, %6\n\ts_waitcnt lgkmcnt(0)\n\ts_memtime %2\n\ts_waitcnt lgkmcnt(0)"
            : "=&s"(ta), "=&s"(tb), "=&s"(tc), "=&s"(dd) : "s"(gw), "s"(oa), "s"(oa ^ 8192) : "memory");
        const int d1 = (int)(tb - ta), d2 = (int)(tc - tb);
        if (lane == 0)
          __hip_atomic_fetch_add(reinterpret_cast<unsigned*>(lds + L_SY + 7), 256u + ((d1 < d2) != (r & 1)), __ATOMIC_RELAXED,
                                 __HIP_MEMORY_SCOPE_WORKGROUP);
      }
      if (r == DR1 && b == DB && w == 7) {
        const unsigned v = __hip_atomic_load(reinterpret_cast<unsigned*>(lds + L_SY + 7), __ATOMIC_RELAXED, __HIP_MEMORY_SCOPE_WORKGROUP);
        const u64 gr = ((u64)tag << 32) | (FPX >= 0 ? FPX : 2 * (v & 255u) > (v >> 8) ? 0u : 1u);
        asm volatile("s_mov_b64 exec, 1\n\tglobal_store_dwordx2 %0, %1, off sc1\n\ts_mov_b64 exec, -1\n\ts_nop 0" ::"v"(gw + GP), "v"(gr) : "memory");
      }
      static_assert(!(PM & ((1 << (AR + 2)) - 1)), "tag");
      if ((PM >> r) & 1) {
        const unsigned org = __builtin_amdgcn_readfirstlane(reinterpret_cast<const unsigned*>(lds)[L_CK + r]);
        if (w == MW && pub && !tl) THRN(3);
        const unsigned now = NOWT() - 9;
        const int l = (int)((tl ? tl : now) - org) - (t_round(r) + ph);
        __builtin_amdgcn_raw_buffer_store_b64(u32x2{(unsigned)(l < -64 ? 0 : l + 64), tag}, rl, w == MW && l < ULIM ? lw_ : NOWHERE,
                                              64 * LSP * r, SC1);
      }
      SCHED_FENCE();
    }
    if (r == PR_ && r < 15) prep();
    if (r == C0) {
      lds_wait(lds, L_SY + 2, 1u);
      STAMP(3);
#pragma unroll
      for (int j = 0; j < 3; ++j) qf[j] = *reinterpret_cast<const u32x4*>(lds + L_QS + (c + 8 * j) * 4);
#pragma unroll
      for (int k = 0; k < 4; ++k) score(k);
    } else if (r == C0 + 1) {
#pragma unroll
      for (int k = 4; k < 7; ++k) score(k);
    } else if (r == C0 + 2) {
      score(7);
      mw = sc[0];
#pragma unroll
      for (int k = 1; k < 8; ++k) mw = fmaxf(mw, sc[k]);
      mw = xg_max(mw);
      lw = 0.f;
#pragma unroll
      for (int k = 0; k < 8; ++k) {
        const float p = ex2((sc[k] - mw) * LOG2E);
        lw += p;
        pb[k] = bfbits(p);
      }
      lw = xg_sum(lw);
      if (lane == 0) {
        lds[L_ML + 2 * w] = mw;
        lds[L_ML + 2 * w + 1] = lw;
      }
      if (early) {
        float z[8];
#pragma unroll
        for (int n = 0; n < 8; ++n) z[n] = 0.f;
#pragma unroll
        for (int r = 0; r < 4; ++r) {
          const unsigned chi = fbits(lds[L_H + 16 * w + 4 * r + r4]), clo = chi >> 16;
#pragma unroll
          for (int n = 0; n < 4; ++n) {
            z[2 * n] = dot2(W2[r][n], clo, z[2 * n]);
            z[2 * n + 1] = dot2(W2[r][n], chi, z[2 * n + 1]);
          }
        }
        const float2 zr = rows_reduce(z);
        if (c16 < 10) *reinterpret_cast<float2*>(lds + L_Z + 80 * w + 8 * c16 + 2 * r4) = zr;
      }
      LDS_DRAIN();
      if (lane == 0) lds_add(lds, L_SY + 4);
      STAMP(4);
    } else if (r == C0 + 3) {
#pragma unroll
      for (int k = 0; k < t + 1 - PVL; ++k) pv(k);
    } else if (r > C0 + 3 && r < 15) {
      pv(t - PVL);
    }
    if constexpr (!early && ((PM >> (r - 1)) & 1)) {
      SCHED_FENCE();
      if (w == MW) {
      const int lr = lane ? lane : 15;
      static_assert(!((PM >> r) & 1), "three requests behind the fetch");
      asm volatile("s_waitcnt vmcnt(3)" : "+v"(lat)::"memory");
      constexpr int p = r - 1;
      const float m = wave_sum((float)(lat[1] == tag && lat[0] < UMISS + 64 ? lat[0] : UMISS + 64)) * (1.f / 64) - 64.f;
      int d = 0;
      if (p <= 8) {
        kslow = m >= ATHR;
        if (kslow) d = (int)(m * (ASH / 4.f)) + AGX * (lr <= p ? 0 : lr < 8 ? lr - p : 8 - p);
      } else if (!vslow && m >= (p <= 10 ? (kslow ? UTA : UTHR) : UTH2)) {
        vslow = true;
        sh = fmaxf(m, 0.f) * ((p <= 10 ? USH : USH2) / 4.f);
        d = (int)sh + (lr > p ? UGX * (lr - p) : 0) + (lane ? 0 : VT);
      } else if (vslow && m < CT && sh < CSH) {
        d = (lr > p + 1 ? -UGX * (lr - p - 1) : 0) - (lane ? 0 : VT);
      }
      if (lane < 16) reinterpret_cast<unsigned*>(lds)[L_CK + lane] += d;
      if (p == 10) {
#pragma unroll 1
        for (unsigned spin = 0; spin < SPIN_MAX && __builtin_amdgcn_readfirstlane(lat[3]) != tag; ++spin) {
          __builtin_amdgcn_s_sleep(4);
          fetch(64 * LSP * p);
          asm volatile("s_waitcnt vmcnt(0)" : "+v"(lat)::"memory");
        }
        if (lane == 0) reinterpret_cast<unsigned*>(lds)[L_SY + 5] = 1u + (lat[2] & 1u);
      }
      }
    }
  });
  asm volatile("" ::"v"(tch));
  SCHED_FENCE();
  const __amdgpu_buffer_rsrc_t rwo = rsrc(wo + (size_t)h * DH * D, DH * D * 2);
  if (PR_ == 15) prep();
  unsigned pdst = lds_addr(lds + L_PART + w * DH + 8 * c + g), psrc = lds_addr(lds + L_PART + 48 * r4 + 4 * c16);
  unsigned cnt6 = lds_addr(lds + L_SY + 6), one = 1u;
  u32x4 rq = {cw, tag, cw, tag};
  u64 m48 = 0x0fff0fff0fff0fffull, m52 = 0x1fff1fff1fff1fffull;
  asm volatile("" : "+s"(m48), "+s"(m52), "+{v[156:159]}"(rq));
  PIN(pdst);
  PIN(psrc);
  PIN(cnt6);
  PIN(one);
#pragma unroll
  for (int k = 7 - PVL; k < 7; ++k) pv(k);
  auto wo1 = [&]() {
    if (WT_) pace<T_LAST + WT_, 0>(clk, ph);
    SCHED_FENCE();
#pragma unroll
    for (int i = 0; i < W1N; ++i) WO[i] = __builtin_amdgcn_raw_buffer_load_b128(rwo, wooff, i * (4 * D * 2), 2);
    SCHED_FENCE();
  };
  {
    unsigned plo = pb[7], phi = plo << 16;
    PIN(plo);
    PIN(phi);
    sfor<0, 3>([&](auto J) {
      constexpr int j = J.v;
      SCHED_FENCE();
      float a[8];
#pragma unroll
      for (int i = 0; i < 4; ++i) {
        a[2 * i] = dot2(V[7][j][i], plo, acc[8 * j + 2 * i]);
        a[2 * i + 1] = dot2(V[7][j][i], phi, acc[8 * j + 2 * i + 1]);
      }
      xg_store<256 * j>(a, f, pdst);
    });
    if (early) wo1();
  }
  STAMP(5);
  unsigned arr;
  asm volatile(
      "s_mov_b64 exec, 1\n\tds_add_rtn_u32 v112, %[cnt], %[one]\n\ts_mov_b64 exec, %[m48]\n\t" SUM8("%[src]")
      "v_readfirstlane_b32 %[arr], v112\n\t" SUM8B
      "s_cmp_eq_u32 %[arr], 7\n\t" SUM8C
      "v_cvt_pk_bf16_f32 v156, v116, v117\n\tv_cvt_pk_bf16_f32 v158, v118, v119\n\t"
      "s_cselect_b64 exec, " FWM ", 0\n\tglobal_store_dwordx4 %[dst], v[156:159], off sc1\n\ts_mov_b64 exec, -1\n\ts_nop 0"
      : [arr] "=&s"(arr), "+{v[156:159]}"(rq)
      : [cnt] "v"(cnt6), [one] "v"(one), [m48] "s"(m48), [m52] "s"(m52), [src] "v"(psrc), [dst] "v"(rdst)
      : CLOB_T, "scc", "memory");
  if (arr == 0 && isF) {
    const float2* zp = reinterpret_cast<const float2*>(lds + L_Z) + l40;
    const float2 z0 = zp[0], z1 = zp[40], z2 = zp[80], z3 = zp[120];
    gst16(zdst, fbits((z0.x + z1.x) + (z2.x + z3.x)), fbits((z0.y + z1.y) + (z2.y + z3.y)), tag);
  }
  if constexpr (!early) {
    const int fh = X0 && w != 7 ? 4 : h;
    if (X0 ? h < 4 && (w == 7 || (w == FW2 && h == (sp & 3))) : w == 7 && h < 5) {
      const int col = 80 * sp + 16 * fh + 2 * c + (lane >> 5);
      const float xv = bf_lo(xs[col]);
      const u64* py = gw + GZ + (size_t)(sp * 5 + fh) * YB + 32 * g + 2 * c + 384;
      bf16* po = out + col;
      float gt = 0.f;
      u64 m3 = 0ull, ms = 0ull;
      PIN(po);
#pragma unroll 1
      for (int pass = 0; pass < 2; ++pass) {
        lds_wait(lds, L_SY + (pass ? 1 : 6), pass ? 4u : 8u);
        if (FT_ && pass) pace<T_LAST + FT_, 0>(clk, 0);
        STAMP(8);
        asm volatile("" : "+s"(m3), "+s"(ms));
        float t;
        unsigned n;
        asm volatile(
            "v_mov_b32 v124, 0\n\tv_mov_b32 v125, %[tag]\n\tv_mov_b32 v126, 0\n\tv_mov_b32 v127, %[tag]\n\t"
            "s_mov_b64 vcc, -1\n\ts_mov_b32 %[n], 0\n"
            ".Lfp%=:\n\t"
            "s_add_u32 %[n], %[n], 1\n\ts_cmp_gt_u32 %[n], %[max]\n\ts_cbranch_scc1 .Lfd%=\n\t"
            "s_mov_b64 exec, vcc\n\t"
            "global_load_dwordx4 v[112:115], %[p], off offset:-3072 sc1\n\t"
            "global_load_dwordx4 v[116:119], %[p], off offset:-1024 sc1\n\t"
            "global_load_dwordx4 v[120:123], %[p], off offset:1024 sc1\n\t"
            "s_and_b64 exec, vcc, %[m3]\n\t"
            "global_load_dwordx4 v[124:127], %[p], off offset:3072 sc1\n\t"
            "s_mov_b64 exec, -1\n\ts_waitcnt vmcnt(0)\n\t"
            "v_bitop3_b32 v128, v113, v115, %[tag] bitop3:0x7e\n\tv_bitop3_b32 v129, v117, v119, %[tag] bitop3:0x7e\n\t"
            "v_bitop3_b32 v130, v121, v123, %[tag] bitop3:0x7e\n\tv_bitop3_b32 v131, v125, v127, %[tag] bitop3:0x7e\n\t"
            "v_or3_b32 v128, v128, v129, v130\n\tv_add_f32 v132, v112, v116\n\tv_or_b32 v128, v128, v131\n\t"
            "v_add_f32 v134, v120, v124\n\tv_cmp_ne_u32 vcc, 0, v128\n\tv_add_f32 v133, v114, v118\n\t"
            "s_cbranch_vccnz .Lfp%=\n"
            ".Lfd%=:\n\t"
            "v_add_f32 v135, v122, v126\n\tv_add_f32 v132, v132, v134\n\tv_add_f32 v133, v133, v135\n\ts_nop 1\n\t"
            "v_permlane32_swap_b32 v132, v133\n\tv_add_f32 v134, v132, v133\n\tv_add_f32 v135, v132, v133\n\ts_nop 1\n\t"
            "v_permlane16_swap_b32 v134, v135\n\tv_add_f32 v134, v134, v135\n\ts_nop 1\n\t"
            "v_add_f32_dpp v134, v134, v134 row_ror:8 row_mask:0xf bank_mask:0xf\n\t"
            "v_cvt_pk_bf16_f32 v134, v134, v134\n\tv_lshlrev_b32 %[t], 16, v134\n\t"
            "v_mul_f32 v134, %[t], %[gt]\n\tv_cvt_pk_bf16_f32 v134, v134, v134\n\tv_lshlrev_b32 v134, 16, v134\n\t"
            "v_add_f32 v134, %[xv], v134\n\tv_cvt_pk_bf16_f32 v134, v134, v134\n\t"
            "s_mov_b64 exec, %[ms]\n\tglobal_store_short %[po], v134, off " OSC "\n\ts_mov_b64 exec, -1"
            : [t] "=&v"(t), [n] "=&s"(n)
            : [tag] "s"(tag), [max] "n"(SPIN_MAX), [p] "v"(py), [m3] "s"(m3), [ms] "s"(ms), [gt] "v"(gt), [xv] "v"(xv), [po] "v"(po)
            : CLOB_T, "vcc", "scc", "memory");
        if (pass) break;
        gt = bfr(silu(t));
        py += 1024 * yp - (int)(GZ - GY);
        m3 = ~0ull;
        ms = NOST ? 0ull : 0x000000ff000000ffull;
      }
      if (!EBX && b == 0 && tid == NT - 64)
        __hip_atomic_store(reinterpret_cast<unsigned*>(gw + GEP), tag, __ATOMIC_RELAXED, __HIP_MEMORY_SCOPE_AGENT);
      {
        const unsigned now = NOWT();
        auto lu = [&](int i) { return __hip_atomic_load(reinterpret_cast<unsigned*>(lds + i), __ATOMIC_RELAXED, __HIP_MEMORY_SCOPE_WORKGROUP); };
        const unsigned o0 = lu(L_TE + 4);
        int code = (int)(now - lu(L_CK) - T_LAST - 300) / 2;
        code = code < 0 ? 0 : code > 150 ? 150 : code;
        while (NOWT() - o0 < 10000u + 400u * code) __builtin_amdgcn_s_sleep(4);
      }
      STAMP(10);
    }
    if (EBX && w == 5 && b == EBW) {
      const int hk = 6 * (lane & 3), gz = 3 * ((lane & 31) >> 2) + (hk >> 3);
      const u64* pz = gw + GZ + (size_t)((hk & 7) * 5) * YB + 32 * gz;
      pace<T_LAST + EBT, 0>(clk, 0);
#pragma unroll 1
      for (unsigned spin = 0; spin < SPIN_MAX; ++spin) {
        if (__all((unsigned)(gld(pz) >> 32) == tag)) break;
        __builtin_amdgcn_s_sleep(16);
      }
      if (lane == 0)
        __hip_atomic_store(reinterpret_cast<unsigned*>(gw + GEP), tag, __ATOMIC_RELAXED, __HIP_MEMORY_SCOPE_AGENT);
    }
    STAMP_FLUSH();
    asm volatile(".Lcode_lastb:");
    return;
  }
  pace<T_LAST + W2T, 0>(clk, ph);
#pragma unroll
  for (int i = W1N; i < 6; ++i) WO[i] = __builtin_amdgcn_raw_buffer_load_b128(rwo, wooff, i * (4 * D * 2), 2);
  unsigned dm0 = __builtin_amdgcn_raw_buffer_load_b32(rwo, NOWHERE, 0, 2), dm1 = __builtin_amdgcn_raw_buffer_load_b32(rwo, NOWHERE, 4, 2);
  STAMP(3);
  SCHED_FENCE();
  const int ly = isF ? lane & 31 : lane;
  u64* ydst = gw + GY + (size_t)(4 * sp + (ly >> 3) + 4 * (ly >> 5)) * YB + 32 * h + 2 * (ly & 7) + 1024 * yp;

  const u64* pr = gw + G2 + (size_t)(r0 + r4 + 2) * RS + 32 * w + 2 * c16;
  const u64 ownm = 0xffffull << (16 * (sp & 3));
  u64 own0 = sp < 4 ? ownm : 0ull, own1 = sp < 4 ? 0ull : ownm, my = isF ? 0xffffffffull : ~0ull;
  u64 for0 = m52 & ~own0, for1 = m52 & ~own1;
  own0 &= m48;
  own1 &= m48;
  unsigned osrc = lds_addr(lds + L_PART + 48 * w + 4 * c16), ysrc = lds_addr(lds + L_Y + 2 * ly), cnt1 = lds_addr(lds + L_SY + 1);
  unsigned ydl = lds_addr(lds + L_Y + 128 * w + (isF ? 8 * c + g : 8 * c16 + 2 * r4));
  asm volatile("" : "+s"(own0), "+s"(own1), "+s"(for0), "+s"(for1), "+s"(my));
  PIN(ydst);
  PIN(pr);
  PIN(osrc);
  PIN(ysrc);
  PIN(cnt1);
  PIN(ydl);
#pragma unroll
  for (int i = XS; i < 6; ++i) X[i] = W2[i - XS];
  u32x8 WP[3], XP[3];
#pragma unroll
  for (int p = 0; p < 3; ++p)
#pragma unroll
    for (int k = 0; k < 4; ++k) {
      XP[p][2 * k] = __builtin_amdgcn_perm(X[2 * p + 1][k], X[2 * p][k], 0x05040100u);
      XP[p][2 * k + 1] = __builtin_amdgcn_perm(X[2 * p + 1][k], X[2 * p][k], 0x07060302u);
    }
  asm volatile("" : "+{v[64:71]}"(XP[0]), "+{v[72:79]}"(XP[1]), "+{v[80:87]}"(XP[2]));
  SCHED_FENCE();
  auto perm_wo = [&]() {
#pragma unroll
    for (int p = 0; p < 3; ++p)
#pragma unroll
      for (int k = 0; k < 4; ++k) {
        WP[p][2 * k] = __builtin_amdgcn_perm(WO[2 * p + 1][k], WO[2 * p][k], 0x05040100u);
        WP[p][2 * k + 1] = __builtin_amdgcn_perm(WO[2 * p + 1][k], WO[2 * p][k], 0x07060302u);
      }
    asm volatile("" : "+{v[88:95]}"(WP[0]), "+{v[96:103]}"(WP[1]), "+{v[104:111]}"(WP[2]));
    SCHED_FENCE();
    STAMP(1);
  };
  lds_wait(lds, L_SY + 6, 8u);
  if (PT_) pace<T_LAST + PT_, 0>(clk, 0);
  STAMP(4);
  u32x4 ra0, ra1;
  asm volatile(
      "v_mov_b32 v148, %[cw]\n\tv_mov_b32 v149, %[tag]\n\tv_mov_b32 v151, %[tag]\n\t"
      "v_mov_b32 v152, %[cw]\n\tv_mov_b32 v153, %[tag]\n\tv_mov_b32 v155, %[tag]\n\t"
      "s_mov_b64 exec, %[for0]\n\tglobal_load_dwordx4 v[148:151], %[p], off offset:-2048 sc1\n\t"
      "s_mov_b64 exec, %[for1]\n\tglobal_load_dwordx4 v[152:155], %[p], off offset:2048 sc1\n\t"
      "s_mov_b64 exec, %[m48]\n\t" SUM8("%[src]") SUM8B SUM8C
      "s_mov_b64 exec, %[own0]\n\tv_cvt_pk_bf16_f32 v148, v116, v117\n\tv_cvt_pk_bf16_f32 v150, v118, v119\n\t"
      "s_mov_b64 exec, %[own1]\n\tv_cvt_pk_bf16_f32 v152, v116, v117\n\tv_cvt_pk_bf16_f32 v154, v118, v119\n\t"
      "s_mov_b64 exec, -1"
      : "=&{v[148:151]}"(ra0), "=&{v[152:155]}"(ra1)
      : [cw] "v"(cw), [tag] "s"(tag), [for0] "s"(for0), [for1] "s"(for1), [p] "v"(pr), [own0] "s"(own0), [own1] "s"(own1),
        [m48] "s"(m48), [src] "v"(osrc)
      : CLOB_T, "memory");
  SCHED_FENCE();
  perm_wo();
  unsigned np;
  asm volatile(
      "s_mov_b32 %[n], 0\n\t"
      "s_mov_b64 exec, -1\n\tv_mov_b64 v[160:161], 0\n\tv_mov_b64 v[162:163], 0\n\tv_mov_b64 v[164:165], 0\n\t"
      "v_mov_b64 v[166:167], 0\n\ts_branch .Lrc%=\n"
      ".Lrp%=:\n\t"
      "s_add_u32 %[n], %[n], 1\n\ts_cmp_gt_u32 %[n], %[max]\n\ts_cbranch_scc1 .Lrd%=\n\t"
      "v_cmp_ne_u32 vcc, 0, v112\n\tv_cmp_ne_u32 s[98:99], 0, v113\n\ts_sleep " STR(RSLEEP) "\n\t"
      "s_mov_b64 exec, vcc\n\tglobal_load_dwordx4 v[148:151], %[p], off offset:-2048 sc1\n\t"
      "s_mov_b64 exec, s[98:99]\n\tglobal_load_dwordx4 v[152:155], %[p], off offset:2048 sc1\n\t"
      "s_mov_b64 exec, -1\n"
      ".Lrc%=:\n\t"
      "s_waitcnt vmcnt(0)\n\t"
      "v_bitop3_b32 v112, v149, v151, %[tag] bitop3:0x7e\n\tv_bitop3_b32 v113, v153, v155, %[tag] bitop3:0x7e\n\t"
      "v_max_f32 v115, v148, v152\n\tv_or_b32 v114, v112, v113\n\tv_max_f32 v116, v148, v152\n\t"
      "v_cmp_ne_u32 vcc, 0, v114\n\tv_lshlrev_b32 v122, 16, v148\n\ts_cbranch_vccnz .Lrp%=\n"
      ".Lrd%=:\n\t"
      "v_permlane16_swap_b32 v115, v116\n\tv_max_f32 v117, v115, v116\n\tv_max_f32 v118, v115, v116\n\t"
      "v_and_b32 v123, 0xffff0000, v148\n\tv_lshlrev_b32 v124, 16, v150\n\t"
      "v_permlane32_swap_b32 v117, v118\n\tv_max_f32 v117, v117, v118\n\t"
      "v_sub_f32 v115, v148, v117\n\tv_sub_f32 v116, v152, v117\n\tv_exp_f32 v115, v115\n\tv_exp_f32 v116, v116\n\t"
      "v_and_b32 v125, 0xffff0000, v150\n\tv_lshlrev_b32 v126, 16, v152\n\t"
      "v_add_f32 v117, v115, v116\n\tv_add_f32 v118, v115, v116\n\t"
      "v_mul_f32_dpp v122, v115, v122" BC12 "v_mul_f32_dpp v123, v115, v123" BC12
      "v_permlane16_swap_b32 v117, v118\n\tv_add_f32 v119, v117, v118\n\tv_add_f32 v120, v117, v118\n\t"
      "v_mul_f32_dpp v124, v115, v124" BC12 "v_mul_f32_dpp v125, v115, v125" BC12
      "v_permlane32_swap_b32 v119, v120\n\tv_add_f32 v119, v119, v120\n\tv_lshlrev_b32 v128, 16, v154\n\t"
      "v_rcp_f32 v119, v119\n\tv_and_b32 v127, 0xffff0000, v152\n\tv_and_b32 v129, 0xffff0000, v154\n\t"
      "v_fmac_f32_dpp v122, v116, v126" BC12 "v_fmac_f32_dpp v124, v116, v128" BC12
      "v_fmac_f32_dpp v123, v116, v127" BC12 "v_fmac_f32_dpp v125, v116, v129" BC12
      "v_permlane32_swap_b32 v122, v124\n\tv_add_f32 v122, v122, v124\n\t"
      "v_permlane32_swap_b32 v123, v125\n\tv_add_f32 v123, v123, v125\n\t"
      "v_mov_b32_dpp v120, v119" BC12 "s_nop 0\n\t"
      "v_permlane16_swap_b32 v122, v123\n\tv_add_f32 v122, v122, v123\n\t"
      "v_mul_f32 v123, v122, v120\n\ts_cmp_lt_u32 %[sp], 6\n\t"
      "v_mul_f32_dpp v124, v122, v120 quad_perm:[1,0,3,2] row_mask:0xf bank_mask:0xf\n\t"
      "v_cvt_pk_bf16_f32 v124, v123, v124\n\t"
      "s_cbranch_scc1 .Lyf%=\n\ts_nop 0\n\t"
      DOT8(64, 65, 66, 67, 68, 69, 70, 71, 0) DOT8(72, 73, 74, 75, 76, 77, 78, 79, 2) DOT8(80, 81, 82, 83, 84, 85, 86, 87, 4)
      DOT8(88, 89, 90, 91, 92, 93, 94, 95, 6) DOT8(96, 97, 98, 99, 100, 101, 102, 103, 8)
      DOT8(104, 105, 106, 107, 108, 109, 110, 111, 10) YRED
      "v_pk_add_f32 v[160:161], v[160:161], v[162:163]\n\tds_write_b64 %[ydl], v[160:161]\n"
      ".Lys%=:\n\t"
      "s_mov_b64 exec, 1\n\tds_add_rtn_u32 v112, %[cnt], %[one]\n\ts_mov_b64 exec, -1\n\t"
      "ds_read2st64_b64 v[116:119], %[ysrc] offset0:0 offset1:1\n\tds_read2st64_b64 v[120:123], %[ysrc] offset0:2 offset1:3\n\t"
      "s_waitcnt lgkmcnt(2)\n\tv_readfirstlane_b32 s98, v112\n\ts_waitcnt lgkmcnt(1)\n\t"
      "v_pk_add_f32 v[116:117], v[116:117], v[118:119]\n\ts_cmp_eq_u32 s98, 3\n\ts_cselect_b64 exec, %[my], 0\n\t"
      "s_waitcnt lgkmcnt(0)\n\tv_pk_add_f32 v[120:121], v[120:121], v[122:123]\n\ts_nop 0\n\t"
      "v_add_f32 v156, v116, v120\n\tv_add_f32 v158, v117, v121\n\t"
      "global_store_dwordx4 %[dst], v[156:159], off sc1\n\ts_mov_b64 exec, -1\n\ts_branch .Lye%=\n"
      ".Lyf%=:\n\ts_nop 0\n\t"
      "v_mov_b32_dpp v125, v124 row_newbcast:0 row_mask:0xf bank_mask:0x3\n\t"
      "v_mov_b32_dpp v126, v124 row_newbcast:2 row_mask:0xf bank_mask:0x3\n\t"
      "v_mov_b32_dpp v127, v124 row_newbcast:4 row_mask:0xf bank_mask:0x3\n\t"
      "v_mov_b32_dpp v125, v124 row_newbcast:6 row_mask:0xf bank_mask:0xc\n\t"
      "v_mov_b32_dpp v126, v124 row_newbcast:8 row_mask:0xf bank_mask:0xc\n\t"
      "v_mov_b32_dpp v127, v124 row_newbcast:10 row_mask:0xf bank_mask:0xc\n\t"
      DOT8F(88, 89, 90, 91, 92, 93, 94, 95, 125) DOT8F(96, 97, 98, 99, 100, 101, 102, 103, 126)
      DOT8F(104, 105, 106, 107, 108, 109, 110, 111, 127) YRED
      "v_add_f32 v160, v160, v162\n\tv_add_f32 v161, v161, v163\n\ts_nop 0\n\t"
      "v_add_f32_dpp v160, v160, v160 row_ror:8 row_mask:0xf bank_mask:0x3\n\t"
      "v_add_f32_dpp v160, v161, v161 row_ror:8 row_mask:0xf bank_mask:0xc\n\t"
      "ds_write_b32 %[ydl], v160\n\ts_branch .Lys%=\n"
      ".Lye%=:"
      : [n] "=&s"(np), "+{v[156:159]}"(rq), "+{v[148:151]}"(ra0), "+{v[152:155]}"(ra1)
      : [tag] "s"(tag), [p] "v"(pr), [max] "n"(SPIN_MAX), [sp] "s"(sp), [ydl] "v"(ydl), [cnt] "v"(cnt1), [one] "v"(one),
        [ysrc] "v"(ysrc), [my] "s"(my), [dst] "v"(ydst), "{v[64:71]}"(XP[0]), "{v[72:79]}"(XP[1]), "{v[80:87]}"(XP[2]),
        "{v[88:95]}"(WP[0]), "{v[96:103]}"(WP[1]), "{v[104:111]}"(WP[2])
      : CLOB_T, "v160", "v161", "v162", "v163", "v164", "v165", "v166", "v167", "s98", "s99", "vcc", "scc", "memory");
  STAMP(9);
  asm volatile("" ::"v"(dm0), "v"(dm1));
  STAMP_FLUSH();
  asm volatile(".Lcode_last:");
}

__global__ void __launch_bounds__(NT, 2)
fused_decode(const bf16* __restrict__ wq, const bf16* __restrict__ kc, const bf16* __restrict__ w1, const bf16* __restrict__ w3,
             u64* __restrict__ gw, const bf16* __restrict__ x, const bf16* __restrict__ vc,
             const bf16* __restrict__ wo, const bf16* __restrict__ w2, bf16* __restrict__ out) {
  __shared__ __attribute__((aligned(16))) float lds[L_END];
  const int b = blockIdx.x;
  const int xcd = b & 7, jj = b >> 3;
  const int h = xcd * 4 + (jj >> 3), sp = jj & 7;
  const int w = __builtin_amdgcn_readfirstlane(threadIdx.x >> 6);
  unsigned cpf = threadIdx.x;
  {
    u64 pc;
    unsigned len;
    asm volatile(
        "s_cmp_lt_u32 %[w], 4\n\ts_cbranch_scc1 .Lnocpf\n\t"
        "s_getpc_b64 %[pc]\n.Lcode_here:\n\tv_xor_b32 %[c], %[hi], %[c]\n\tv_lshlrev_b32 %[c], 6, %[c]\n\t"
        "s_mov_b32 %[len], .Lcode_last-.Lcode_here-4\n\ts_max_i32 %[len], %[len], .Lcode_lastb-.Lcode_here+1020\n\t"
        "v_min_u32 %[c], %[len], %[c]\n\tglobal_load_dword %[c], %[c], %[pc]\n.Lnocpf:"
        : [c] "+v"(cpf), [pc] "=&s"(pc), [len] "=&s"(len) : [w] "s"(w), [hi] "s"((b & CPM) << 5) : "scc", "memory");
  }
  SCHED_FENCE();
  if (w >= 4)
    body<false>(lds, h, sp, w, x, kc, vc, wq, wo, w1, w3, w2, gw, out, cpf);
  else
    body<true>(lds, h, sp, w, x, kc, vc, wq, wo, w1, w3, w2, gw, out, cpf);
}

}

extern "C" void kernel_launch(void* const* d_in, const int* in_sizes, int n_in, void* d_out, int out_size, void* d_ws,
                              size_t ws_size, hipStream_t stream) {
  const bf16* x = (const bf16*)d_in[0];
  const bf16* kc = (const bf16*)d_in[1];
  const bf16* vc = (const bf16*)d_in[2];
  const bf16* wq = (const bf16*)d_in[3];
  const bf16* wo = (const bf16*)d_in[4];
  const bf16* w1 = (const bf16*)d_in[6];
  const bf16* w3 = (const bf16*)d_in[7];
  const bf16* w2 = (const bf16*)d_in[8];
#define WOFF 0
  u64* gw = (u64*)((char*)d_ws + WOFF);
  fused_decode<<<NWG, NT, 0, stream>>>(wq, kc, w1, w3, gw, x, vc, wo, w2, (bf16*)d_out);
}
```

```cpp
#include <hip/hip_runtime.h>
#include <hip/hip_bf16.h>
#include <stdint.h>

typedef __hip_bfloat16 bf16;
typedef unsigned long long u64;
typedef unsigned u32x4 __attribute__((ext_vector_type(4)));
typedef unsigned u32x8 __attribute__((ext_vector_type(8)));
typedef float f32x4 __attribute__((ext_vector_type(4)));
typedef float f32x2 __attribute__((ext_vector_type(2)));
typedef __bf16 bf16x2_t __attribute__((ext_vector_type(2)));

namespace {

constexpr int D = 640, HQ = 32, DH = 192, SK = 32768, WIN = 4096, FF = 1536;
constexpr int NSP = 8;
constexpr int NWG = HQ * NSP;
constexpr int NT = 512;
constexpr float SCALE = 0.07216878364870322f;
constexpr float LOG2E = 1.4426950408889634f;
constexpr unsigned SPIN_MAX = 1u << 16;
constexpr int SC1 = 16;
constexpr int NOWHERE = 1 << 30;

constexpr int PK_ = 82, PV_ = 81;
constexpr int TS_ = 358;
constexpr int SB_ = 246;
constexpr int LOC = 30, LOCO = 60;
constexpr int WT_ = 98, W2T = 132;
constexpr int PT_ = 215;
constexpr int NFL = 7;
constexpr int CE = 7, CL = 8;
constexpr int AR = 3;
constexpr int GR = 5, GRW = 2;
constexpr int MW = 4;
constexpr int PM = 0x1440;
constexpr int ULIM = 20, UMISS = 60;
constexpr int ATHR = 20, AGX = 7;
constexpr int UTHR = 8, UTA = 5, UTH2 = 40, UGX = 9, VT = 10;
constexpr int CT = 3, CSH = 30;
constexpr int LSP = 16;
constexpr int t_round(int r) { return r <= 2 ? TS_ : t_round(r - 1) + (r > 8 ? PV_ : PK_); }
constexpr int T_LAST = t_round(15);

constexpr int QS = 144;
constexpr int YB = 2 * (16 * 32 + 16);
constexpr int RS = 144;
constexpr size_t GQ = 0;
constexpr size_t G2 = GQ + (size_t)NWG * QS;
constexpr size_t GY = G2 + (size_t)NWG * RS;
constexpr size_t GZ = GY + 16;
constexpr size_t GEP = GY + (size_t)NSP * 5 * YB;
constexpr size_t GL = GEP + 16;

__device__ __forceinline__ unsigned fbits(float x) { return __builtin_bit_cast(unsigned, x); }
__device__ __forceinline__ u64 gld(const u64* p) {
  return __hip_atomic_load(p, __ATOMIC_RELAXED, __HIP_MEMORY_SCOPE_AGENT);
}
__device__ __forceinline__ float bf_lo(unsigned u) { return __builtin_bit_cast(float, u << 16); }
__device__ __forceinline__ float bf_hi(unsigned u) { return __builtin_bit_cast(float, u & 0xffff0000u); }
__device__ __forceinline__ float bfr(float x) { return __bfloat162float(__float2bfloat16(x)); }
__device__ __forceinline__ unsigned short bfbits(float x) {
  return __builtin_bit_cast(unsigned short, __float2bfloat16(x));
}
__device__ __forceinline__ float dot2(unsigned a, unsigned b, float c) {
  return __builtin_amdgcn_fdot2_f32_bf16(__builtin_bit_cast(bf16x2_t, a), __builtin_bit_cast(bf16x2_t, b), c, false);
}
template <int CTRL>
__device__ __forceinline__ float dpp(float x) {
  return __builtin_bit_cast(float, __builtin_amdgcn_mov_dpp(__builtin_bit_cast(int, x), CTRL, 0xf, 0xf, true));
}
__device__ __forceinline__ float grp8_sum(float x) {
  x += dpp<0xB1>(x);
  x += dpp<0x4E>(x);
  x += dpp<0x141>(x);
  return x;
}
__device__ __forceinline__ float sw16(float a, float b) {
  auto s = __builtin_amdgcn_permlane16_swap(fbits(a), fbits(b), false, false);
  return __builtin_bit_cast(float, (unsigned)s[0]) + __builtin_bit_cast(float, (unsigned)s[1]);
}
__device__ __forceinline__ float sw32(float a, float b) {
  auto s = __builtin_amdgcn_permlane32_swap(fbits(a), fbits(b), false, false);
  return __builtin_bit_cast(float, (unsigned)s[0]) + __builtin_bit_cast(float, (unsigned)s[1]);
}
__device__ __forceinline__ float xg_sum(float x) {
  x += dpp<0x128>(x);
  x = sw16(x, x);
  return sw32(x, x);
}
__device__ __forceinline__ float xg_max(float x) {
  x = fmaxf(x, dpp<0x128>(x));
  auto s = __builtin_amdgcn_permlane16_swap(fbits(x), fbits(x), false, false);
  x = fmaxf(__builtin_bit_cast(float, (unsigned)s[0]), __builtin_bit_cast(float, (unsigned)s[1]));
  auto t = __builtin_amdgcn_permlane32_swap(fbits(x), fbits(x), false, false);
  return fmaxf(__builtin_bit_cast(float, (unsigned)t[0]), __builtin_bit_cast(float, (unsigned)t[1]));
}
__device__ __forceinline__ float wave_sum(float x) { return xg_sum(grp8_sum(x)); }
__device__ __forceinline__ float ex2(float x) { return __builtin_amdgcn_exp2f(x); }
__device__ __forceinline__ float silu(float x) { return x / (1.0f + __expf(-x)); }
__device__ __forceinline__ float xg_reduce8(const float (&a)[8], bool g0) {
  float r[4];
#pragma unroll
  for (int i = 0; i < 4; ++i) r[i] = sw32(a[i], a[i + 4]);
  const float s0 = sw16(r[0], r[2]), s1 = sw16(r[1], r[3]);
  const float keep = g0 ? s1 : s0, send = g0 ? s0 : s1;
  return keep + dpp<0x128>(send);
}
__device__ __forceinline__ __amdgpu_buffer_rsrc_t rsrc(const void* p, unsigned bytes) {
  return __builtin_amdgcn_make_buffer_rsrc(const_cast<void*>(p), 0, bytes, 0x00020000);
}
__device__ __forceinline__ void gst16(u64* p, unsigned v0, unsigned v1, unsigned tag) {
  const u32x4 o = {v0, tag, v1, tag};
  asm volatile("global_store_dwordx4 %0, %1, off sc1\n\ts_nop 1" ::"v"(p), "v"(o) : "memory");
}
__device__ __forceinline__ unsigned bfpack(float a, float b) {
  return __builtin_bit_cast(unsigned, __builtin_convertvector(f32x2{a, b}, bf16x2_t));
}
__device__ __forceinline__ float2 rows_reduce(const float (&v)[8]) {
  float r[4];
#pragma unroll
  for (int i = 0; i < 4; ++i) r[i] = sw32(v[i], v[i + 4]);
  return float2{sw16(r[0], r[2]), sw16(r[1], r[3])};
}
template <int OFF>
__device__ __forceinline__ void xg_store(const float (&v)[8], float f, unsigned a) {
  float p[4], q[4], t, u;
#pragma unroll
  for (int i = 0; i < 4; ++i) {
    auto s = __builtin_amdgcn_permlane32_swap(fbits(v[i]), fbits(v[i + 4]), false, false);
    p[i] = __builtin_bit_cast(float, (unsigned)s[0]);
    q[i] = __builtin_bit_cast(float, (unsigned)s[1]);
  }
  const f32x2 r01 = f32x2{p[0], p[1]} + f32x2{q[0], q[1]}, r23 = f32x2{p[2], p[3]} + f32x2{q[2], q[3]};
  auto s0 = __builtin_amdgcn_permlane16_swap(fbits(r01[0]), fbits(r23[0]), false, false);
  auto s1 = __builtin_amdgcn_permlane16_swap(fbits(r01[1]), fbits(r23[1]), false, false);
  asm volatile("v_add_f32 %0, %2, %3\n\tv_add_f32 %1, %4, %5\n\ts_nop 0\n\t"
               "v_add_f32_dpp %0, %0, %0 row_ror:8 row_mask:0xf bank_mask:0x3\n\t"
               "v_add_f32_dpp %0, %1, %1 row_ror:8 row_mask:0xf bank_mask:0xc\n\t"
               "v_mul_f32 %0, %6, %0\n\tds_write_b32 %7, %0 offset:%8"
               : "=&v"(t), "=&v"(u)
               : "v"((unsigned)s0[0]), "v"((unsigned)s0[1]), "v"((unsigned)s1[0]), "v"((unsigned)s1[1]), "v"(f), "v"(a), "n"(OFF)
               : "memory");
}
#define SUM8(a)                                                                                                          \
  "ds_read_b128 v[116:119], " a "\n\tds_read_b128 v[120:123], " a " offset:768\n\t"                                      \
  "ds_read_b128 v[124:127], " a " offset:1536\n\tds_read_b128 v[128:131], " a " offset:2304\n\t"                         \
  "ds_read_b128 v[132:135], " a " offset:3072\n\tds_read_b128 v[136:139], " a " offset:3840\n\t"                         \
  "ds_read_b128 v[140:143], " a " offset:4608\n\tds_read_b128 v[144:147], " a " offset:5376\n\ts_waitcnt lgkmcnt(6)\n\t"
#define SUM8B                                                                                                            \
  "v_pk_add_f32 v[116:117], v[116:117], v[120:121]\n\tv_pk_add_f32 v[118:119], v[118:119], v[122:123]\n\t"               \
  "s_waitcnt lgkmcnt(4)\n\t"                                                                                             \
  "v_pk_add_f32 v[124:125], v[124:125], v[128:129]\n\tv_pk_add_f32 v[126:127], v[126:127], v[130:131]\n\t"               \
  "v_pk_add_f32 v[116:117], v[116:117], v[124:125]\n\tv_pk_add_f32 v[118:119], v[118:119], v[126:127]\n\t"
#define SUM8C                                                                                                            \
  "s_waitcnt lgkmcnt(2)\n\t"                                                                                             \
  "v_pk_add_f32 v[132:133], v[132:133], v[136:137]\n\tv_pk_add_f32 v[134:135], v[134:135], v[138:139]\n\t"               \
  "s_waitcnt lgkmcnt(0)\n\t"                                                                                             \
  "v_pk_add_f32 v[140:141], v[140:141], v[144:145]\n\tv_pk_add_f32 v[142:143], v[142:143], v[146:147]\n\t"               \
  "v_pk_add_f32 v[132:133], v[132:133], v[140:141]\n\tv_pk_add_f32 v[134:135], v[134:135], v[142:143]\n\t"               \
  "v_pk_add_f32 v[116:117], v[116:117], v[132:133]\n\tv_pk_add_f32 v[118:119], v[118:119], v[134:135]\n\t"
#define BC12 " row_newbcast:12 row_mask:0xf bank_mask:0xf\n\t"
#define DOTB(y, a, n) "v_dot2c_f32_bf16_dpp v" #y ", v124, v" #a " row_newbcast:" #n " row_mask:0xf bank_mask:0xf\n\t"
#define DOT8(a, b, c, d, e, f, g, h, n) \
  DOTB(160, a, n) DOTB(161, b, n) DOTB(162, c, n) DOTB(163, d, n) DOTB(164, e, n) DOTB(165, f, n) DOTB(166, g, n) DOTB(167, h, n)
#define DOTF(y, a, n) "v_dot2c_f32_bf16 v" #y ", v" #n ", v" #a "\n\t"
#define DOT8F(a, b, c, d, e, f, g, h, n) \
  DOTF(160, a, n) DOTF(161, b, n) DOTF(162, c, n) DOTF(163, d, n) DOTF(164, e, n) DOTF(165, f, n) DOTF(166, g, n) DOTF(167, h, n)
#define YRED                                                                                                             \
  "v_permlane32_swap_b32 v160, v164\n\tv_permlane32_swap_b32 v161, v165\n\tv_permlane32_swap_b32 v162, v166\n\t"         \
  "v_permlane32_swap_b32 v163, v167\n\tv_pk_add_f32 v[160:161], v[160:161], v[164:165]\n\t"                              \
  "v_pk_add_f32 v[162:163], v[162:163], v[166:167]\n\ts_nop 1\n\t"                                                       \
  "v_permlane16_swap_b32 v160, v162\n\tv_permlane16_swap_b32 v161, v163\n\t"
#define CLOB_T                                                                                                     \
  "v112", "v113", "v114", "v115", "v116", "v117", "v118", "v119", "v120", "v121", "v122", "v123", "v124", "v125",        \
      "v126", "v127", "v128", "v129", "v130", "v131", "v132", "v133", "v134", "v135", "v136", "v137", "v138", "v139",    \
      "v140", "v141", "v142", "v143", "v144", "v145", "v146", "v147"
#define SCHED_FENCE() __builtin_amdgcn_sched_barrier(0)
#define THRN(n) do { SCHED_FENCE(); asm volatile("s_waitcnt vmcnt(%0)" ::"n"(n) : "memory"); SCHED_FENCE(); } while (0)
#define NOWT() ((unsigned)__builtin_amdgcn_s_memrealtime())
template <int N>
struct IC {
  static constexpr int v = N;
};
template <int I, int E, class F>
__device__ __forceinline__ void sfor(F&& f) {
  if constexpr (I < E) {
    f(IC<I>{});
    sfor<I + 1, E>(f);
  }
}
#define LDS_DRAIN() asm volatile("s_waitcnt lgkmcnt(0)" ::: "memory")
__device__ __forceinline__ unsigned lds_add(float* lds, int idx) {
  return __hip_atomic_fetch_add(reinterpret_cast<unsigned*>(lds + idx), 1u, __ATOMIC_RELAXED, __HIP_MEMORY_SCOPE_WORKGROUP);
}
__device__ __forceinline__ void lds_wait(float* lds, int idx, unsigned n) {
  SCHED_FENCE();
#pragma unroll 1
  for (unsigned spin = 0; spin < (SPIN_MAX << 4); ++spin) {
    if (__hip_atomic_load(reinterpret_cast<unsigned*>(lds + idx), __ATOMIC_RELAXED, __HIP_MEMORY_SCOPE_WORKGROUP) >= n) break;
    __builtin_amdgcn_s_sleep(1);
  }
  asm volatile("" ::: "memory");
  SCHED_FENCE();
}
template <int T, int R>
__device__ __forceinline__ void pace(unsigned clk, int ph) {
  unsigned o;
  SCHED_FENCE();
  asm volatile(
      "s_branch .Lpb%=\n"
      ".Lpa%=:\n\ts_sleep 1\n"
      ".Lpb%=:\n\tds_read_b32 %0, %1 offset:%4\n\ts_memrealtime s[98:99]\n\ts_waitcnt lgkmcnt(0)\n\tv_readfirstlane_b32 s99, %0\n\t"
      "s_sub_u32 s98, s98, %2\n\ts_sub_u32 s98, s98, s99\n\ts_cmp_lt_i32 s98, %3\n\ts_cbranch_scc1 .Lpa%="
      : "=&v"(o) : "v"(clk), "s"(ph), "n"(T), "n"(4 * R) : "s98", "s99", "scc", "memory");
  SCHED_FENCE();
}

template <int T, int R>
__device__ __forceinline__ unsigned pace_m(unsigned clk, int ph) {
  unsigned o, tl;
  SCHED_FENCE();
  asm volatile(
      "s_mov_b32 %1, 0\n\ts_branch .Lpb%=\n"
      ".Lpa%=:\n\ts_sleep 1\n"
      ".Lpb%=:\n\tds_read_b32 %0, %2 offset:%5\n\ts_getreg_b32 s97, hwreg(HW_REG_IB_STS, 0, 4)\n\ts_memrealtime s[98:99]\n\t"
      "s_or_b32 s97, s97, %1\n\ts_waitcnt lgkmcnt(0)\n\ts_cselect_b32 %1, %1, s98\n\tv_readfirstlane_b32 s99, %0\n\t"
      "s_sub_u32 s98, s98, %3\n\ts_sub_u32 s98, s98, s99\n\ts_cmp_lt_i32 s98, %4\n\ts_cbranch_scc1 .Lpa%="
      : "=&v"(o), "=&s"(tl) : "v"(clk), "s"(ph), "n"(T), "n"(4 * R) : "s97", "s98", "s99", "scc", "memory");
  SCHED_FENCE();
  return tl;
}

constexpr int L_QP = 0;
constexpr int L_QS = L_QP + 4 * 128;
constexpr int L_PART = L_QS + 96;
constexpr int L_ML = L_PART + 8 * DH;
constexpr int L_Z = L_ML + 16;
constexpr int L_Y = L_Z + 320;
constexpr int L_H = L_Y + 512;
constexpr int L_SY = L_H + 64;
constexpr int L_CK = L_SY + 8;
constexpr int L_END = L_CK + 16;

#define PIN(x) asm volatile("" : "+v"(x))

__device__ __forceinline__ void wg_ml(const float* lds, float& M, float& L) {
  float mm[8], ll[8];
#pragma unroll
  for (int i = 0; i < 4; ++i) {
    const f32x4 t = *reinterpret_cast<const f32x4*>(lds + L_ML + 4 * i);
    mm[2 * i] = t[0];
    ll[2 * i] = t[1];
    mm[2 * i + 1] = t[2];
    ll[2 * i + 1] = t[3];
  }
  M = mm[0];
#pragma unroll
  for (int ww = 1; ww < 8; ++ww) M = fmaxf(M, mm[ww]);
  L = 0.f;
#pragma unroll
  for (int ww = 0; ww < 8; ++ww) L = fmaf(ex2((mm[ww] - M) * LOG2E), ll[ww], L);
}
__device__ __forceinline__ unsigned lds_addr(const float* p) {
  return (unsigned)(size_t)(const __attribute__((address_space(3))) float*)p;
}

template <bool early>
__device__ __forceinline__ void body(float* lds, const int h, const int sp, const int w, const bf16* __restrict__ x,
                                     const bf16* __restrict__ kc, const bf16* __restrict__ vc,
                                     const bf16* __restrict__ wq, const bf16* __restrict__ wo,
                                     const bf16* __restrict__ w1, const bf16* __restrict__ w3,
                                     const bf16* __restrict__ w2, u64* __restrict__ gw, bf16* __restrict__ out,
                                     unsigned cpf) {
  const int b = blockIdx.x;
  const int tid = threadIdx.x, lane = tid & 63;
  const int g = lane >> 3, c = lane & 7;
  const int rec = h * NSP + sp;

  const bool isF = sp < 6;
  u32x4 PR[8], xa, xb, xq;
  unsigned epoch = 0;
  u64 tk = 0;
  int ra = 0, RA0 = 0;
  bool va = false;
  if constexpr (early) {
    int s0_ = 240, s1_ = 80, s2_ = 240 * (sp & 1), s3_ = 80 * sp, s4_ = 64 * (sp >> 1), s5_ = 64 * w, s6_ = 24 * w;
    asm("" : "+s"(s0_), "+s"(s1_), "+s"(s2_), "+s"(s3_), "+s"(s4_), "+s"(s5_), "+s"(s6_));
    const int rw0 = isF ? s5_ : s6_;
    int lim = (isF ? s0_ : s1_) - rw0, lim2 = isF ? 64 : 24;
    asm("" : "+s"(lim), "+s"(lim2));
    lim = lim < lim2 ? lim : lim2;
    ra = rw0 + 8 * g;
    va = 8 * g < lim;
    RA0 = isF ? s2_ : s3_;
    int aoff = (int)__umul24(g, 8 * HQ * DH * 2) + 16 * c;
    PIN(aoff);
    aoff = va ? aoff : NOWHERE;
    int sa = ((RA0 + rw0) * (HQ * DH) + h * DH + (isF ? s4_ : 64)) * 2;
    PIN(aoff);
    asm volatile("" : "+s"(sa));
    SCHED_FENCE();
    const __amdgpu_buffer_rsrc_t rw = rsrc(wq, NOWHERE);
    PR[0] = __builtin_amdgcn_raw_buffer_load_b128(rw, aoff, sa, 2);
    SCHED_FENCE();
    tk = __builtin_amdgcn_s_memrealtime();
    SCHED_FENCE();
    asm volatile("s_load_dword %0, %1, %2" : "=&s"(epoch) : "s"(gw), "s"(w == 1 ? (int)GL * 8 + (sp * 8 + (b & 7)) * LSP : (int)GEP * 8) : "memory");
    SCHED_FENCE();
#pragma unroll
    for (int i = 1; i < 8; ++i) PR[i] = __builtin_amdgcn_raw_buffer_load_b128(rw, aoff, sa + i * (HQ * DH * 2), 2);
    SCHED_FENCE();
    SCHED_FENCE();
  }
  const unsigned short* xs = reinterpret_cast<const unsigned short*>(x);
  const size_t wgb = ((size_t)h * SK + (SK - WIN)) * DH;
  const __amdgpu_buffer_rsrc_t rk = rsrc(kc + wgb, WIN * DH * 2);
  const __amdgpu_buffer_rsrc_t rv = rsrc(vc + wgb, WIN * DH * 2);
  const int voff = (g * DH + 8 * c) * 2;
  auto tile = [&](int t) { return (t * 64 + h * 16 + w * 8 + sp) & 511; };
  u32x4 K[8][3], V[8][3];
  auto kld = [&](int t, int j) { K[t][j] = __builtin_amdgcn_raw_buffer_load_b128(rk, voff + 128 * j, tile(t) * 3072, 2); };
  auto vld = [&](int t, int j) { V[t][j] = __builtin_amdgcn_raw_buffer_load_b128(rv, voff + 128 * j, tile(t) * 3072, 2); };
  unsigned tag = 0;
  const unsigned clk = lds_addr(lds + L_CK);
  const int ph = (w * PV_) >> 3;
  const int r4 = lane >> 4, c16 = lane & 15;
  const int ch = c16 < 10 ? c16 : 9;
  const int fi = (h & 3) * 6 + (isF ? sp : 5), gl = fi >> 3, gI = (h >> 2) * 3 + gl, fr = fi & 7;
  u32x4 WO[6], X[6], W2[4];
  unsigned tch = 0;
  const int wooff = ((48 * w + r4 + (isF ? 24 * (g & 1) : 24)) * D + 64 * sp + 8 * c + (isF ? 0 : 128 * (g & 1))) * 2;
  typedef unsigned u32x2 __attribute__((ext_vector_type(2)));
  const __amdgpu_buffer_rsrc_t rl = rsrc(gw + GL, 16 * 64 * LSP);
  const bool pub = !((sp + (b >> 6)) & 3);
  const int lw_ = lane == 0 && pub ? (sp * 8 + (b & 7)) * LSP : NOWHERE;
  u32x2 lat = {0u, 0u};
  auto fetch = [&](int off) {
    SCHED_FENCE();
    asm volatile("buffer_load_dwordx2 %0, %1, %2, %3 offen sc1" : "+v"(lat) : "v"(lane * LSP), "s"(rl), "s"(off) : "memory");
    SCHED_FENCE();
  };
  u64 own = 0, pre = 0;
  unsigned pro = 0, st1 = 0, old = 0;
  bool vslow = false, kslow = false;
  float sh = 0.f;
  if constexpr (early) {
    u32x4 PF[5];
    const int F0 = (isF ? 160 * (fr >> 1) : 480) + 40 * w;
    const int fs = isF ? FF : HQ * DH;
    int s6_ = 64 * gI, s7_ = h * DH + 128 * (sp & 1);
    asm("" : "+s"(s6_), "+s"(s7_));
    const int boff = ((int)__umul24(g, fs) + 8 * c) * 2;
    const int sb = (F0 * fs + (isF ? s6_ : s7_)) * 2;
    const bf16* pf_ = (fr & 1) ? w3 : w1;
    asm("" : "+s"(pf_));
    const __amdgpu_buffer_rsrc_t rf = rsrc(isF ? pf_ : wq, NOWHERE);
    unsigned short xr[5];
#pragma unroll
    for (int i = 0; i < 5; ++i) PF[i] = __builtin_amdgcn_raw_buffer_load_b128(rf, boff, sb + i * 16 * fs, 2);
    SCHED_FENCE();
    if (!isF) {
#pragma unroll
      for (int i = 0; i < 6; ++i)
        X[i] = __builtin_amdgcn_raw_buffer_load_b128(rsrc(wo + (size_t)h * DH * D, DH * D * 2), wooff - 24 * D * 2 + i * (4 * D * 2), 0, 2);
    } else {
#pragma unroll
      for (int i = 0; i < 6; ++i) X[i] = u32x4{0u, 0u, 0u, 0u};
    }
    SCHED_FENCE();
    xa = *reinterpret_cast<const u32x4*>(x + 8 * lane);
    xb = *reinterpret_cast<const u32x4*>(x + 512 + 8 * (lane & 15));
    xq = *reinterpret_cast<const u32x4*>(x + RA0 + (va ? ra : 0));
#pragma unroll
    for (int i = 0; i < 5; ++i) xr[i] = xs[F0 + 8 * i + g];
    SCHED_FENCE();
    const unsigned epv = __hip_atomic_load(reinterpret_cast<unsigned*>(gw + GEP), __ATOMIC_RELAXED, __HIP_MEMORY_SCOPE_AGENT);
    SCHED_FENCE();
    asm volatile("" : "+s"(tk));
    if (tid < 16) reinterpret_cast<unsigned*>(lds)[L_CK + tid] = (unsigned)tk;
    asm volatile("s_waitcnt lgkmcnt(0)\n\ts_barrier" : "+s"(epoch)::"memory");
    SCHED_FENCE();
#pragma unroll
    for (int t = 0; t < 3; ++t)
#pragma unroll
      for (int j = 0; j < 3; ++j) kld(t, j);
    SCHED_FENCE();
    float mb, den;
    {
      float xf[8], xg[8];
#pragma unroll
      for (int i = 0; i < 4; ++i) {
        xf[2 * i] = bf_lo(xa[i]);
        xf[2 * i + 1] = bf_hi(xa[i]);
        xg[2 * i] = bf_lo(xb[i]);
        xg[2 * i + 1] = bf_hi(xb[i]);
      }
      const bool lo16 = lane < 16;
      float sum = 0.f;
#pragma unroll
      for (int i = 0; i < 8; ++i) sum += xf[i] + (lo16 ? xg[i] : 0.f);
      sum = wave_sum(sum);
      const float mean = sum * (1.0f / D);
      float sq = 0.f;
#pragma unroll
      for (int i = 0; i < 8; ++i) {
        const float d1 = xf[i] - mean, d2 = xg[i] - mean;
        sq += d1 * d1 + (lo16 ? d2 * d2 : 0.f);
      }
      sq = wave_sum(sq);
      mb = bfr(mean);
      const float vb = bfr(sq * (1.0f / D));
      den = bfr(sqrtf(bfr(vb + 1e-5f)));
    }
    {
      unsigned xn[13];
#pragma unroll
      for (int i = 0; i < 4; ++i) {
        xn[2 * i] = fbits(bfr(bfr(bf_lo(xq[i]) - mb) / den));
        xn[2 * i + 1] = fbits(bfr(bfr(bf_hi(xq[i]) - mb) / den));
      }
#pragma unroll
      for (int i = 0; i < 5; ++i) xn[8 + i] = fbits(bfr(bfr(bf_lo(xr[i]) - mb) / den));
#pragma unroll
      for (int i = 0; i < 13; ++i) PIN(xn[i]);
      SCHED_FENCE();
      pace<t_round(2), 2>(clk, ph);
#pragma unroll
      for (int j = 0; j < 3; ++j) kld(3, j);
      SCHED_FENCE();
      float fa[8], fg[8];
#pragma unroll
      for (int k = 0; k < 8; ++k) fa[k] = fg[k] = 0.f;
#pragma unroll
      for (int i = 0; i < 8; ++i)
#pragma unroll
        for (int k = 0; k < 4; ++k) {
          fa[2 * k] = dot2(PR[i][k], xn[i] >> 16, fa[2 * k]);
          fa[2 * k + 1] = dot2(PR[i][k], xn[i], fa[2 * k + 1]);
        }
      lds[L_QP + w * 128 + 8 * c + g] = xg_reduce8(fa, g & 1);
#pragma unroll
      for (int i = 0; i < 5; ++i)
#pragma unroll
        for (int k = 0; k < 4; ++k) {
          fg[2 * k] = dot2(PF[i][k], xn[8 + i] >> 16, fg[2 * k]);
          fg[2 * k + 1] = dot2(PF[i][k], xn[8 + i], fg[2 * k + 1]);
        }
      lds[L_QP + w * 128 + 64 + 8 * c + g] = xg_reduce8(fg, g & 1);
    }
    SCHED_FENCE();
    tag = __builtin_amdgcn_readfirstlane(epv) + 1u;
    if (tid == 0) reinterpret_cast<unsigned*>(lds)[L_SY + 3] = tag;
    LDS_DRAIN();
    if (lane == 0) old = lds_add(lds, L_SY);
    old = __builtin_amdgcn_readfirstlane(old);
    if (old == 3) {
      const float2* qp = reinterpret_cast<const float2*>(lds + L_QP) + lane;
      const float2 p0_ = qp[0], p1_ = qp[64], p2_ = qp[128], p3_ = qp[192];
      gst16(gw + GQ + (size_t)rec * QS + 2 * lane, fbits((p0_.x + p1_.x) + (p2_.x + p3_.x)),
            fbits((p0_.y + p1_.y) + (p2_.y + p3_.y)), tag);
    }
  } else {
    if (tid >= NT - 8) reinterpret_cast<unsigned*>(lds)[L_SY + tid - (NT - 8)] = 0u;
    unsigned st0 = 0;
    {
      const bf16* tp = w == 4 ? kc + wgb : w == 5 ? (!isF ? wo + (size_t)h * DH * D : (fr & 1) ? w3 : w1) : w == 6 ? ((sp & 1) ? x : wq) : vc + wgb;
      asm volatile("s_waitcnt lgkmcnt(0)\n\ts_load_dword %0, %1, 0x0" : "=&s"(st0) : "s"(tp) : "memory");
    }
    SCHED_FENCE();
#pragma unroll
    for (int j = 0; j < 3; ++j) kld(0, j);
    SCHED_FENCE();
    asm volatile("s_barrier" ::: "memory");
    SCHED_FENCE();
    if (w < 6) {
      const int l = lane < 42 ? lane : 41, k = l & 15;
      const int yg = 4 * (k < 8 || isF ? sp : sp + 2) + ((k >> 1) & 3);
      const size_t yo = l < 32 || !isF ? GY + (size_t)yg * YB + 32 * h + 16 * (h >> 4) + 8 * (k & 1)
                                       : GZ + (size_t)(fr * 5 + (k >> 1)) * YB + 32 * gI + 16 * (gI >> 4) + 8 * (k & 1);
      unsigned o4 = (unsigned)(l < 16 ? G2 + (size_t)rec * RS + 8 * l : yo);
      unsigned o5 = (unsigned)(GQ + (size_t)rec * QS + 8 * (lane < 16 ? lane : 15));
      if (pub && lane >= 16 && lane < 32 && ((PM >> (lane - 16)) & 1))
        o5 = (unsigned)GL + (unsigned)(((lane - 16) * 64 + sp * 8 + (b & 7)) * (LSP / 8));
      PIN(o4);
      PIN(o5);
      pre = gld(gw + (w == 4 ? o4 : o5));
    }
    if (w == 7 && h < 4)
      pro = __hip_atomic_load(reinterpret_cast<const unsigned*>(out + 80 * sp + 16 * (lane >= 8 ? 4 : h)) + (lane & 7), __ATOMIC_RELAXED,
                              __HIP_MEMORY_SCOPE_AGENT);
    SCHED_FENCE();
    asm volatile("s_waitcnt lgkmcnt(0)" ::"s"(st0) : "memory");
#pragma unroll
    for (int j = 0; j < 3; ++j) {
      kld(1, j);
      THRN(5);
    }
    THRN(3);
    own = __builtin_amdgcn_s_memrealtime() - SB_;
    __builtin_amdgcn_raw_buffer_store_b64(u32x2{(unsigned)own, (unsigned)(own >> 32)}, rl, w == 6 ? lw_ : NOWHERE, 0, SC1);
    SCHED_FENCE();
    if (w == 6 && lane < 16) {
      const unsigned o = (unsigned)own - (isF ? LOC : LOCO);
      if ((int)(o - reinterpret_cast<unsigned*>(lds)[L_CK + lane]) > 0) reinterpret_cast<unsigned*>(lds)[L_CK + lane] = o;
    }
    SCHED_FENCE();
    {
      const bf16* tp = (w & 1) ? wo + (size_t)h * DH * D : w2 + (size_t)gI * 64 * D;
      asm volatile("s_load_dword %0, %1, 0x0" : "=&s"(st1) : "s"(tp) : "memory");
    }
    SCHED_FENCE();
  }
  u32x4 qf[3];
  float sc[8], mw, lw, acc[24];
  unsigned pb[8];
  auto score = [&](int t) {
    float d = 0.f;
#pragma unroll
    for (int j = 0; j < 3; ++j)
#pragma unroll
      for (int i = 0; i < 4; ++i) d = dot2(K[t][j][i], qf[j][i], d);
    d = grp8_sum(d);
    sc[t] = bfr(bfr(d) * SCALE);
  };
  auto pv = [&](int t) {
    const unsigned plo = pb[t], phi = plo << 16;
#pragma unroll
    for (int j = 0; j < 3; ++j)
#pragma unroll
      for (int i = 0; i < 4; ++i) {
        acc[8 * j + 2 * i] = dot2(V[t][j][i], plo, t ? acc[8 * j + 2 * i] : 0.f);
        acc[8 * j + 2 * i + 1] = dot2(V[t][j][i], phi, t ? acc[8 * j + 2 * i + 1] : 0.f);
        PIN(acc[8 * j + 2 * i]);
        PIN(acc[8 * j + 2 * i + 1]);
      }
  };
  const bool hf = lane >> 5;
  const __amdgpu_buffer_rsrc_t rq_ = rsrc(gw + GQ, NWG * QS * 8);
  int go[9];
  {
    const int l16 = (lane & 31) * 16;
    const int qa = l16 + (8 * h + (hf ? 4 : 0)) * (QS * 8);
    go[0] = qa;
    go[1] = qa + QS * 8;
    go[2] = l16 + (8 * h + (hf ? 7 : 6)) * (QS * 8) + 512;
    go[3] = qa + 2 * QS * 8;
    go[4] = qa + 3 * QS * 8;
#pragma unroll
    for (int k = 0; k < 4; ++k) {
      const int m = 8 * gl + 2 * k + (hf ? 1 : 0);
      go[5 + k] = isF ? l16 + (32 * (h >> 2) + m + (((m * 43) >> 7) & 6)) * (QS * 8) + 512 : NOWHERE;
    }
  }
  const bool gath = old == GRW;
  u32x4 pa[9];
  constexpr int C0 = early ? CE : CL;
  static_assert(C0 + 3 < 15 && C0 > GR + 1 && C0 >= 6, "schedule");
  sfor<early ? 3 : 2, 16>([&](auto R) {
    constexpr int r = R.v, t = r >= 8 ? r - 8 : r + early;
    unsigned tl = 0;
    if constexpr (!early && ((PM >> r) & 1)) tl = pace_m<t_round(r), r>(clk, ph); else pace<t_round(r), r>(clk, ph);
    if constexpr (!early && ((PM >> (r - 1)) & 1)) {
      if (w == MW) fetch(64 * LSP * (r - 1));
    }
    if constexpr (r < 8 && t == 8) {
      const __amdgpu_buffer_rsrc_t rx = isF ? rsrc(w2 + ((size_t)gI * 64 + 16 * w) * D + 80 * fr, 16 * D * 2) : rsrc(wo + (size_t)h * DH * D, DH * D * 2);
#pragma unroll
      for (int i = 0; i < 4; ++i)
        W2[i] = __builtin_amdgcn_raw_buffer_load_b128(rx, isF ? (r4 * D + 8 * ch) * 2 : NOWHERE, i * (4 * D * 2), 2);
      tch = __builtin_amdgcn_raw_buffer_load_b32(rsrc(wo + (size_t)h * DH * D, DH * D * 2), tid ? NOWHERE : wooff, 0, 2);
    } else {
#pragma unroll
      for (int j = 0; j < 3; ++j) {
        if (r >= 8) vld(t, j); else kld(t, j);
        if (r == 15 && j == 2) break;
        if (early && r == GR + 1) continue;
        THRN(early && r == 8 ? NFL + 2 : NFL);
      }
    }
    SCHED_FENCE();
    if constexpr (early) {
      if (r == GR && gath) {
#pragma unroll
        for (int s = 0; s < 9; ++s) pa[s] = __builtin_amdgcn_raw_buffer_load_b128(rq_, go[s], 0, SC1);
        SCHED_FENCE();
      }
      if (r == GR + 1) {
        if (gath) {
          const unsigned tf = isF ? tag : 0u;
          bool ok = true;
#pragma unroll
          for (int s = 0; s < 9; ++s) ok = ok & (pa[s][1] == (s < 5 ? tag : tf)) & (pa[s][3] == (s < 5 ? tag : tf));
          if (!__all(ok)) {
            for (unsigned spin = 0;; ++spin) {
              ok = true;
              asm volatile("" ::: "memory");
#pragma unroll
              for (int s = 0; s < 9; ++s) {
                pa[s] = __builtin_amdgcn_raw_buffer_load_b128(rq_, go[s], 0, SC1);
                ok = ok & (pa[s][1] == (s < 5 ? tag : tf)) & (pa[s][3] == (s < 5 ? tag : tf));
              }
              if (__all(ok) || spin >= SPIN_MAX) break;
              __builtin_amdgcn_s_sleep(1);
            }
          }
          float qo[2], qm[2], hu[2];
#pragma unroll
          for (int k = 0; k < 2; ++k) {
            auto val = [&](int s) { return __builtin_bit_cast(float, (unsigned)pa[s][2 * k]); };
            qo[k] = (val(0) + val(1)) + val(2);
            const float t_ = val(3) + val(4);
            qm[k] = sw32(t_, t_);
            const float u = bfr((val(5) + val(6)) + (val(7) + val(8)));
            auto sw = __builtin_amdgcn_permlane32_swap(fbits(u), fbits(u), false, false);
            hu[k] = bfr(bfr(silu(__builtin_bit_cast(float, (unsigned)sw[0]))) * __builtin_bit_cast(float, (unsigned)sw[1]));
          }
          reinterpret_cast<unsigned*>(lds + L_QS)[lane + (hf ? 32 : 0)] = bfpack(qo[0], qo[1]);
          if (hf) {
            reinterpret_cast<unsigned*>(lds + L_QS)[lane] = bfpack(qm[0], qm[1]);
          } else {
            *reinterpret_cast<float2*>(lds + L_H + 2 * lane) = float2{hu[0], hu[1]};
          }
          LDS_DRAIN();
          if (lane == 0) lds_add(lds, L_SY + 2);
        }
      }
    } else {
      if (r == 2) asm volatile("" ::"s"(st1), "v"(cpf), "v"(pre), "v"(pro));
      if (r == AR && w == 6) fetch(0);
      if (r == AR + 1 && w == 6) {
        asm volatile("s_waitcnt vmcnt(3)" : "+v"(lat)::"memory");
        const u64 d = (((u64)lat[1] << 32) | lat[0]) - own + 200u;
        const bool ok = d <= 400u;
        const float s_ = wave_sum(ok ? (float)(unsigned)d : 0.f), n = (float)__builtin_popcountll(__ballot(ok));
        const unsigned o = (unsigned)own + (n > 0.f ? (unsigned)(s_ * __builtin_amdgcn_rcpf(n)) - 200u : 0u);
        if (w == 6 && lane < 16) reinterpret_cast<unsigned*>(lds)[L_CK + lane] = o;
      }
      if (r == AR + 2) {
        lds_wait(lds, L_SY, 4u);
        tag = __builtin_amdgcn_readfirstlane(reinterpret_cast<const unsigned*>(lds)[L_SY + 3]);
      }
      static_assert(!(PM & ((1 << (AR + 2)) - 1)), "tag");
      if ((PM >> r) & 1) {
        const unsigned org = __builtin_amdgcn_readfirstlane(reinterpret_cast<const unsigned*>(lds)[L_CK + r]);
        if (w == MW && pub && !tl) THRN(3);
        const unsigned now = NOWT() - 9;
        const int l = (int)((tl ? tl : now) - org) - (t_round(r) + ph);
        __builtin_amdgcn_raw_buffer_store_b64(u32x2{(unsigned)(l < -64 ? 0 : l + 64), tag}, rl, w == MW && l < ULIM ? lw_ : NOWHERE,
                                              64 * LSP * r, SC1);
      }
      SCHED_FENCE();
    }
    if (r == C0) {
      lds_wait(lds, L_SY + 2, 1u);
#pragma unroll
      for (int j = 0; j < 3; ++j) qf[j] = *reinterpret_cast<const u32x4*>(lds + L_QS + (c + 8 * j) * 4);
#pragma unroll
      for (int k = 0; k < 4; ++k) score(k);
    } else if (r == C0 + 1) {
#pragma unroll
      for (int k = 4; k < 7; ++k) score(k);
    } else if (r == C0 + 2) {
      score(7);
      mw = sc[0];
#pragma unroll
      for (int k = 1; k < 8; ++k) mw = fmaxf(mw, sc[k]);
      mw = xg_max(mw);
      lw = 0.f;
#pragma unroll
      for (int k = 0; k < 8; ++k) {
        const float p = ex2((sc[k] - mw) * LOG2E);
        lw += p;
        pb[k] = bfbits(p);
      }
      lw = xg_sum(lw);
      if (lane == 0) {
        lds[L_ML + 2 * w] = mw;
        lds[L_ML + 2 * w + 1] = lw;
      }
      if (early) {
        float z[8];
#pragma unroll
        for (int n = 0; n < 8; ++n) z[n] = 0.f;
#pragma unroll
        for (int r = 0; r < 4; ++r) {
          const unsigned chi = fbits(lds[L_H + 16 * w + 4 * r + r4]), clo = chi >> 16;
#pragma unroll
          for (int n = 0; n < 4; ++n) {
            z[2 * n] = dot2(W2[r][n], clo, z[2 * n]);
            z[2 * n + 1] = dot2(W2[r][n], chi, z[2 * n + 1]);
          }
        }
        const float2 zr = rows_reduce(z);
        if (c16 < 10) *reinterpret_cast<float2*>(lds + L_Z + 80 * w + 8 * c16 + 2 * r4) = zr;
      }
      LDS_DRAIN();
      if (lane == 0) lds_add(lds, L_SY + 4);
    } else if (r == C0 + 3) {
#pragma unroll
      for (int k = 0; k < t; ++k) pv(k);
    } else if (r > C0 + 3 && r < 15) {
      pv(t - 1);
    }
    if constexpr (!early && ((PM >> (r - 1)) & 1)) {
      SCHED_FENCE();
      if (w == MW) {
      const int lr = lane ? lane : 15;
      static_assert(!((PM >> r) & 1), "three requests behind the fetch");
      asm volatile("s_waitcnt vmcnt(3)" : "+v"(lat)::"memory");
      constexpr int p = r - 1;
      const float m = wave_sum((float)(lat[1] == tag && lat[0] < UMISS + 64 ? lat[0] : UMISS + 64)) * (1.f / 64) - 64.f;
      int d = 0;
      if (p <= 8) {
        kslow = m >= ATHR;
        if (kslow) d = (int)m + AGX * (lr <= p ? 0 : lr < 8 ? lr - p : 8 - p);
      } else if (!vslow && m >= (p <= 10 ? (kslow ? UTA : UTHR) : UTH2)) {
        vslow = true;
        sh = fmaxf(m, 0.f) * (p <= 10 ? 1.f : 0.75f);
        d = (int)sh + (lr > p ? UGX * (lr - p) : 0) + (lane ? 0 : VT);
      } else if (vslow && m < CT && sh < CSH) {
        d = (lr > p + 1 ? -UGX * (lr - p - 1) : 0) - (lane ? 0 : VT);
      }
      if (lane < 16) reinterpret_cast<unsigned*>(lds)[L_CK + lane] += d;
      }
    }
  });
  asm volatile("" ::"v"(tch));
  SCHED_FENCE();
  const __amdgpu_buffer_rsrc_t rwo = rsrc(wo + (size_t)h * DH * D, DH * D * 2);
  lds_wait(lds, L_SY + 4, 8u);
  float f, M, L;
  wg_ml(lds, M, L);
  f = ex2((mw - M) * LOG2E) / L;
  const unsigned cw = fbits(fmaf(M, LOG2E, __builtin_amdgcn_logf(L)));
  PIN(f);
  SCHED_FENCE();
  const int l40 = lane < 40 ? lane : 39;
  u64* rdst = gw + G2 + (size_t)rec * RS + 32 * r4 + 2 * c16;
  u64* zdst = gw + GZ + (size_t)(fr * 5 + (l40 >> 3)) * YB + 32 * gI + 16 * (gI >> 4) + 2 * (l40 & 7);
  unsigned pdst = lds_addr(lds + L_PART + w * DH + 8 * c + g), psrc = lds_addr(lds + L_PART + 48 * r4 + 4 * c16);
  unsigned cnt6 = lds_addr(lds + L_SY + 6), one = 1u;
  u32x4 rq = {cw, tag, cw, tag};
  u64 m48 = 0x0fff0fff0fff0fffull, m52 = 0x1fff1fff1fff1fffull;
  asm volatile("" : "+s"(m48), "+s"(m52), "+{v[156:159]}"(rq));
  PIN(rdst);
  PIN(zdst);
  PIN(pdst);
  PIN(psrc);
  PIN(cnt6);
  PIN(one);
  pv(6);
  auto wo1 = [&]() {
    pace<T_LAST + WT_, 0>(clk, ph);
    SCHED_FENCE();
#pragma unroll
    for (int i = 0; i < 3; ++i) WO[i] = __builtin_amdgcn_raw_buffer_load_b128(rwo, wooff, i * (4 * D * 2), 2);
    SCHED_FENCE();
  };
  {
    unsigned plo = pb[7], phi = plo << 16;
    PIN(plo);
    PIN(phi);
    sfor<0, 3>([&](auto J) {
      constexpr int j = J.v;
      SCHED_FENCE();
      float a[8];
#pragma unroll
      for (int i = 0; i < 4; ++i) {
        a[2 * i] = dot2(V[7][j][i], plo, acc[8 * j + 2 * i]);
        a[2 * i + 1] = dot2(V[7][j][i], phi, acc[8 * j + 2 * i + 1]);
      }
      xg_store<256 * j>(a, f, pdst);
    });
    if (early) wo1();
  }
  unsigned arr;
  asm volatile(
      "s_mov_b64 exec, 1\n\tds_add_rtn_u32 v112, %[cnt], %[one]\n\ts_mov_b64 exec, %[m48]\n\t" SUM8("%[src]")
      "v_readfirstlane_b32 %[arr], v112\n\t" SUM8B
      "s_cmp_eq_u32 %[arr], 7\n\t" SUM8C
      "v_cvt_pk_bf16_f32 v156, v116, v117\n\tv_cvt_pk_bf16_f32 v158, v118, v119\n\t"
      "s_cselect_b64 exec, %[m52], 0\n\tglobal_store_dwordx4 %[dst], v[156:159], off sc1\n\ts_mov_b64 exec, -1\n\ts_nop 0"
      : [arr] "=&s"(arr), "+{v[156:159]}"(rq)
      : [cnt] "v"(cnt6), [one] "v"(one), [m48] "s"(m48), [m52] "s"(m52), [src] "v"(psrc), [dst] "v"(rdst)
      : CLOB_T, "scc", "memory");
  if (arr == 0 && isF) {
    const float2* zp = reinterpret_cast<const float2*>(lds + L_Z) + l40;
    const float2 z0 = zp[0], z1 = zp[40], z2 = zp[80], z3 = zp[120];
    gst16(zdst, fbits((z0.x + z1.x) + (z2.x + z3.x)), fbits((z0.y + z1.y) + (z2.y + z3.y)), tag);
  }
  if constexpr (!early) {
    const int fh = w != 7 ? 4 : h;
    if (h < 4 && (w == 7 || (w == 6 && h == (sp & 3)))) {
      const int col = 80 * sp + 16 * fh + 2 * c + (lane >> 5);
      const float xv = bf_lo(xs[col]);
      const u64* py = gw + GZ + (size_t)(sp * 5 + fh) * YB + 32 * g + 2 * c + 392;
      bf16* po = out + col;
      float gt = 0.f;
      u64 m3 = 0ull, ms = 0ull;
      PIN(po);
#pragma unroll 1
      for (int pass = 0; pass < 2; ++pass) {
        lds_wait(lds, L_SY + (pass ? 1 : 6), pass ? 4u : 8u);
        asm volatile("" : "+s"(m3), "+s"(ms));
        float t;
        unsigned n;
        asm volatile(
            "v_mov_b32 v124, 0\n\tv_mov_b32 v125, %[tag]\n\tv_mov_b32 v126, 0\n\tv_mov_b32 v127, %[tag]\n\t"
            "s_mov_b64 vcc, -1\n\ts_mov_b32 %[n], 0\n"
            ".Lfp%=:\n\t"
            "s_add_u32 %[n], %[n], 1\n\ts_cmp_gt_u32 %[n], %[max]\n\ts_cbranch_scc1 .Lfd%=\n\t"
            "s_mov_b64 exec, vcc\n\t"
            "global_load_dwordx4 v[112:115], %[p], off offset:-3136 sc1\n\t"
            "global_load_dwordx4 v[116:119], %[p], off offset:-1088 sc1\n\t"
            "global_load_dwordx4 v[120:123], %[p], off offset:1088 sc1\n\t"
            "s_and_b64 exec, vcc, %[m3]\n\t"
            "global_load_dwordx4 v[124:127], %[p], off offset:3136 sc1\n\t"
            "s_mov_b64 exec, -1\n\ts_waitcnt vmcnt(0)\n\t"
            "v_bitop3_b32 v128, v113, v115, %[tag] bitop3:0x7e\n\tv_bitop3_b32 v129, v117, v119, %[tag] bitop3:0x7e\n\t"
            "v_bitop3_b32 v130, v121, v123, %[tag] bitop3:0x7e\n\tv_bitop3_b32 v131, v125, v127, %[tag] bitop3:0x7e\n\t"
            "v_or3_b32 v128, v128, v129, v130\n\tv_add_f32 v132, v112, v116\n\tv_or_b32 v128, v128, v131\n\t"
            "v_add_f32 v134, v120, v124\n\tv_cmp_ne_u32 vcc, 0, v128\n\tv_add_f32 v133, v114, v118\n\t"
            "s_cbranch_vccnz .Lfp%=\n"
            ".Lfd%=:\n\t"
            "v_add_f32 v135, v122, v126\n\tv_add_f32 v132, v132, v134\n\tv_add_f32 v133, v133, v135\n\ts_nop 1\n\t"
            "v_permlane32_swap_b32 v132, v133\n\tv_add_f32 v134, v132, v133\n\tv_add_f32 v135, v132, v133\n\ts_nop 1\n\t"
            "v_permlane16_swap_b32 v134, v135\n\tv_add_f32 v134, v134, v135\n\ts_nop 1\n\t"
            "v_add_f32_dpp v134, v134, v134 row_ror:8 row_mask:0xf bank_mask:0xf\n\t"
            "v_cvt_pk_bf16_f32 v134, v134, v134\n\tv_lshlrev_b32 %[t], 16, v134\n\t"
            "v_mul_f32 v134, %[t], %[gt]\n\tv_cvt_pk_bf16_f32 v134, v134, v134\n\tv_lshlrev_b32 v134, 16, v134\n\t"
            "v_add_f32 v134, %[xv], v134\n\tv_cvt_pk_bf16_f32 v134, v134, v134\n\t"
            "s_mov_b64 exec, %[ms]\n\tglobal_store_short %[po], v134, off sc1\n\ts_mov_b64 exec, -1"
            : [t] "=&v"(t), [n] "=&s"(n)
            : [tag] "s"(tag), [max] "n"(SPIN_MAX), [p] "v"(py), [m3] "s"(m3), [ms] "s"(ms), [gt] "v"(gt), [xv] "v"(xv), [po] "v"(po)
            : CLOB_T, "vcc", "scc", "memory");
        if (pass) break;
        gt = bfr(silu(t));
        py -= GZ - GY;
        m3 = ~0ull;
        ms = 0x000000ff000000ffull;
      }
      if (b == 0 && tid == NT - 64)
        __hip_atomic_store(reinterpret_cast<unsigned*>(gw + GEP), tag, __ATOMIC_RELAXED, __HIP_MEMORY_SCOPE_AGENT);
    }
    asm volatile(".Lcode_lastb:");
    return;
  }
  pace<T_LAST + W2T, 0>(clk, ph);
#pragma unroll
  for (int i = 3; i < 6; ++i) WO[i] = __builtin_amdgcn_raw_buffer_load_b128(rwo, wooff, i * (4 * D * 2), 2);
  unsigned dm0 = __builtin_amdgcn_raw_buffer_load_b32(rwo, NOWHERE, 0, 2), dm1 = __builtin_amdgcn_raw_buffer_load_b32(rwo, NOWHERE, 4, 2);
  SCHED_FENCE();
  const int ly = isF ? lane & 31 : lane;
  u64* ydst = gw + GY + (size_t)(4 * sp + (ly >> 3) + 4 * (ly >> 5)) * YB + 32 * h + 16 * (h >> 4) + 2 * (ly & 7);
  static_assert(RS * 8 * 4 == 4608, "record r4 + 4 is 4608 bytes behind record r4");
  const u64* pr = gw + G2 + (size_t)(h * NSP + r4 + 2) * RS + 32 * w + 2 * c16;
  const u64 ownm = 0xffffull << (16 * (sp & 3));
  u64 own0 = sp < 4 ? ownm : 0ull, own1 = sp < 4 ? 0ull : ownm, my = isF ? 0xffffffffull : ~0ull;
  u64 for0 = m52 & ~own0, for1 = m52 & ~own1;
  own0 &= m48;
  own1 &= m48;
  unsigned osrc = lds_addr(lds + L_PART + 48 * w + 4 * c16), ysrc = lds_addr(lds + L_Y + 2 * ly), cnt1 = lds_addr(lds + L_SY + 1);
  unsigned ydl = lds_addr(lds + L_Y + 128 * w + (isF ? 8 * c + g : 8 * c16 + 2 * r4));
  asm volatile("" : "+s"(own0), "+s"(own1), "+s"(for0), "+s"(for1), "+s"(my));
  PIN(ydst);
  PIN(pr);
  PIN(osrc);
  PIN(ysrc);
  PIN(cnt1);
  PIN(ydl);
  u32x8 WP[3], XP[3];
#pragma unroll
  for (int p = 0; p < 3; ++p)
#pragma unroll
    for (int k = 0; k < 4; ++k) {
      XP[p][2 * k] = __builtin_amdgcn_perm(X[2 * p + 1][k], X[2 * p][k], 0x05040100u);
      XP[p][2 * k + 1] = __builtin_amdgcn_perm(X[2 * p + 1][k], X[2 * p][k], 0x07060302u);
    }
  asm volatile("" : "+{v[64:71]}"(XP[0]), "+{v[72:79]}"(XP[1]), "+{v[80:87]}"(XP[2]));
  SCHED_FENCE();
  auto perm_wo = [&]() {
#pragma unroll
    for (int p = 0; p < 3; ++p)
#pragma unroll
      for (int k = 0; k < 4; ++k) {
        WP[p][2 * k] = __builtin_amdgcn_perm(WO[2 * p + 1][k], WO[2 * p][k], 0x05040100u);
        WP[p][2 * k + 1] = __builtin_amdgcn_perm(WO[2 * p + 1][k], WO[2 * p][k], 0x07060302u);
      }
    asm volatile("" : "+{v[88:95]}"(WP[0]), "+{v[96:103]}"(WP[1]), "+{v[104:111]}"(WP[2]));
    SCHED_FENCE();
  };
  lds_wait(lds, L_SY + 6, 8u);
  pace<T_LAST + PT_, 0>(clk, 0);
  u32x4 ra0, ra1;
  asm volatile(
      "v_mov_b32 v148, %[cw]\n\tv_mov_b32 v149, %[tag]\n\tv_mov_b32 v151, %[tag]\n\t"
      "v_mov_b32 v152, %[cw]\n\tv_mov_b32 v153, %[tag]\n\tv_mov_b32 v155, %[tag]\n\t"
      "s_mov_b64 exec, %[for0]\n\tglobal_load_dwordx4 v[148:151], %[p], off offset:-2304 sc1\n\t"
      "s_mov_b64 exec, %[for1]\n\tglobal_load_dwordx4 v[152:155], %[p], off offset:2304 sc1\n\t"
      "s_mov_b64 exec, %[m48]\n\t" SUM8("%[src]") SUM8B SUM8C
      "s_mov_b64 exec, %[own0]\n\tv_cvt_pk_bf16_f32 v148, v116, v117\n\tv_cvt_pk_bf16_f32 v150, v118, v119\n\t"
      "s_mov_b64 exec, %[own1]\n\tv_cvt_pk_bf16_f32 v152, v116, v117\n\tv_cvt_pk_bf16_f32 v154, v118, v119\n\t"
      "s_mov_b64 exec, -1"
      : "=&{v[148:151]}"(ra0), "=&{v[152:155]}"(ra1)
      : [cw] "v"(cw), [tag] "s"(tag), [for0] "s"(for0), [for1] "s"(for1), [p] "v"(pr), [own0] "s"(own0), [own1] "s"(own1),
        [m48] "s"(m48), [src] "v"(osrc)
      : CLOB_T, "memory");
  SCHED_FENCE();
  perm_wo();
  unsigned np;
  asm volatile(
      "s_mov_b32 %[n], 0\n\t"
      "s_mov_b64 exec, -1\n\tv_mov_b64 v[160:161], 0\n\tv_mov_b64 v[162:163], 0\n\tv_mov_b64 v[164:165], 0\n\t"
      "v_mov_b64 v[166:167], 0\n\ts_branch .Lrc%=\n"
      ".Lrp%=:\n\t"
      "s_add_u32 %[n], %[n], 1\n\ts_cmp_gt_u32 %[n], %[max]\n\ts_cbranch_scc1 .Lrd%=\n\t"
      "v_cmp_ne_u32 vcc, 0, v112\n\tv_cmp_ne_u32 s[98:99], 0, v113\n\ts_sleep " "1" "\n\t"
      "s_mov_b64 exec, vcc\n\tglobal_load_dwordx4 v[148:151], %[p], off offset:-2304 sc1\n\t"
      "s_mov_b64 exec, s[98:99]\n\tglobal_load_dwordx4 v[152:155], %[p], off offset:2304 sc1\n\t"
      "s_mov_b64 exec, -1\n"
      ".Lrc%=:\n\t"
      "s_waitcnt vmcnt(0)\n\t"
      "v_bitop3_b32 v112, v149, v151, %[tag] bitop3:0x7e\n\tv_bitop3_b32 v113, v153, v155, %[tag] bitop3:0x7e\n\t"
      "v_max_f32 v115, v148, v152\n\tv_or_b32 v114, v112, v113\n\tv_max_f32 v116, v148, v152\n\t"
      "v_cmp_ne_u32 vcc, 0, v114\n\tv_lshlrev_b32 v122, 16, v148\n\ts_cbranch_vccnz .Lrp%=\n"
      ".Lrd%=:\n\t"
      "v_permlane16_swap_b32 v115, v116\n\tv_max_f32 v117, v115, v116\n\tv_max_f32 v118, v115, v116\n\t"
      "v_and_b32 v123, 0xffff0000, v148\n\tv_lshlrev_b32 v124, 16, v150\n\t"
      "v_permlane32_swap_b32 v117, v118\n\tv_max_f32 v117, v117, v118\n\t"
      "v_sub_f32 v115, v148, v117\n\tv_sub_f32 v116, v152, v117\n\tv_exp_f32 v115, v115\n\tv_exp_f32 v116, v116\n\t"
      "v_and_b32 v125, 0xffff0000, v150\n\tv_lshlrev_b32 v126, 16, v152\n\t"
      "v_add_f32 v117, v115, v116\n\tv_add_f32 v118, v115, v116\n\t"
      "v_mul_f32_dpp v122, v115, v122" BC12 "v_mul_f32_dpp v123, v115, v123" BC12
      "v_permlane16_swap_b32 v117, v118\n\tv_add_f32 v119, v117, v118\n\tv_add_f32 v120, v117, v118\n\t"
      "v_mul_f32_dpp v124, v115, v124" BC12 "v_mul_f32_dpp v125, v115, v125" BC12
      "v_permlane32_swap_b32 v119, v120\n\tv_add_f32 v119, v119, v120\n\tv_lshlrev_b32 v128, 16, v154\n\t"
      "v_rcp_f32 v119, v119\n\tv_and_b32 v127, 0xffff0000, v152\n\tv_and_b32 v129, 0xffff0000, v154\n\t"
      "v_fmac_f32_dpp v122, v116, v126" BC12 "v_fmac_f32_dpp v124, v116, v128" BC12
      "v_fmac_f32_dpp v123, v116, v127" BC12 "v_fmac_f32_dpp v125, v116, v129" BC12
      "v_permlane32_swap_b32 v122, v124\n\tv_add_f32 v122, v122, v124\n\t"
      "v_permlane32_swap_b32 v123, v125\n\tv_add_f32 v123, v123, v125\n\t"
      "v_mov_b32_dpp v120, v119" BC12 "s_nop 0\n\t"
      "v_permlane16_swap_b32 v122, v123\n\tv_add_f32 v122, v122, v123\n\t"
      "v_mul_f32 v123, v122, v120\n\ts_cmp_lt_u32 %[sp], 6\n\t"
      "v_mul_f32_dpp v124, v122, v120 quad_perm:[1,0,3,2] row_mask:0xf bank_mask:0xf\n\t"
      "v_cvt_pk_bf16_f32 v124, v123, v124\n\t"
      "s_cbranch_scc1 .Lyf%=\n\ts_nop 0\n\t"
      DOT8(64, 65, 66, 67, 68, 69, 70, 71, 0) DOT8(72, 73, 74, 75, 76, 77, 78, 79, 2) DOT8(80, 81, 82, 83, 84, 85, 86, 87, 4)
      DOT8(88, 89, 90, 91, 92, 93, 94, 95, 6) DOT8(96, 97, 98, 99, 100, 101, 102, 103, 8)
      DOT8(104, 105, 106, 107, 108, 109, 110, 111, 10) YRED
      "v_pk_add_f32 v[160:161], v[160:161], v[162:163]\n\tds_write_b64 %[ydl], v[160:161]\n"
      ".Lys%=:\n\t"
      "s_mov_b64 exec, 1\n\tds_add_rtn_u32 v112, %[cnt], %[one]\n\ts_mov_b64 exec, -1\n\t"
      "ds_read2st64_b64 v[116:119], %[ysrc] offset0:0 offset1:1\n\tds_read2st64_b64 v[120:123], %[ysrc] offset0:2 offset1:3\n\t"
      "s_waitcnt lgkmcnt(2)\n\tv_readfirstlane_b32 s98, v112\n\ts_waitcnt lgkmcnt(1)\n\t"
      "v_pk_add_f32 v[116:117], v[116:117], v[118:119]\n\ts_cmp_eq_u32 s98, 3\n\ts_cselect_b64 exec, %[my], 0\n\t"
      "s_waitcnt lgkmcnt(0)\n\tv_pk_add_f32 v[120:121], v[120:121], v[122:123]\n\ts_nop 0\n\t"
      "v_add_f32 v156, v116, v120\n\tv_add_f32 v158, v117, v121\n\t"
      "global_store_dwordx4 %[dst], v[156:159], off sc1\n\ts_mov_b64 exec, -1\n\ts_branch .Lye%=\n"
      ".Lyf%=:\n\ts_nop 0\n\t"
      "v_mov_b32_dpp v125, v124 row_newbcast:0 row_mask:0xf bank_mask:0x3\n\t"
      "v_mov_b32_dpp v126, v124 row_newbcast:2 row_mask:0xf bank_mask:0x3\n\t"
      "v_mov_b32_dpp v127, v124 row_newbcast:4 row_mask:0xf bank_mask:0x3\n\t"
      "v_mov_b32_dpp v125, v124 row_newbcast:6 row_mask:0xf bank_mask:0xc\n\t"
      "v_mov_b32_dpp v126, v124 row_newbcast:8 row_mask:0xf bank_mask:0xc\n\t"
      "v_mov_b32_dpp v127, v124 row_newbcast:10 row_mask:0xf bank_mask:0xc\n\t"
      DOT8F(88, 89, 90, 91, 92, 93, 94, 95, 125) DOT8F(96, 97, 98, 99, 100, 101, 102, 103, 126)
      DOT8F(104, 105, 106, 107, 108, 109, 110, 111, 127) YRED
      "v_add_f32 v160, v160, v162\n\tv_add_f32 v161, v161, v163\n\ts_nop 0\n\t"
      "v_add_f32_dpp v160, v160, v160 row_ror:8 row_mask:0xf bank_mask:0x3\n\t"
      "v_add_f32_dpp v160, v161, v161 row_ror:8 row_mask:0xf bank_mask:0xc\n\t"
      "ds_write_b32 %[ydl], v160\n\ts_branch .Lys%=\n"
      ".Lye%=:"
      : [n] "=&s"(np), "+{v[156:159]}"(rq), "+{v[148:151]}"(ra0), "+{v[152:155]}"(ra1)
      : [tag] "s"(tag), [p] "v"(pr), [max] "n"(SPIN_MAX), [sp] "s"(sp), [ydl] "v"(ydl), [cnt] "v"(cnt1), [one] "v"(one),
        [ysrc] "v"(ysrc), [my] "s"(my), [dst] "v"(ydst), "{v[64:71]}"(XP[0]), "{v[72:79]}"(XP[1]), "{v[80:87]}"(XP[2]),
        "{v[88:95]}"(WP[0]), "{v[96:103]}"(WP[1]), "{v[104:111]}"(WP[2])
      : CLOB_T, "v160", "v161", "v162", "v163", "v164", "v165", "v166", "v167", "s98", "s99", "vcc", "scc", "memory");
  asm volatile("" ::"v"(dm0), "v"(dm1));
  asm volatile(".Lcode_last:");
}

__global__ void __launch_bounds__(NT, 2)
fused_decode(const bf16* __restrict__ wq, const bf16* __restrict__ kc, const bf16* __restrict__ w1, const bf16* __restrict__ w3,
             u64* __restrict__ gw, const bf16* __restrict__ x, const bf16* __restrict__ vc,
             const bf16* __restrict__ wo, const bf16* __restrict__ w2, bf16* __restrict__ out) {
  __shared__ __attribute__((aligned(16))) float lds[L_END];
  const int b = blockIdx.x;
  const int xcd = b & 7, jj = b >> 3;
  const int h = xcd * 4 + (jj >> 3), sp = jj & 7;
  const int w = __builtin_amdgcn_readfirstlane(threadIdx.x >> 6);
  unsigned cpf = threadIdx.x;
  {
    u64 pc;
    unsigned len;
    asm volatile(
        "s_cmp_lt_u32 %[w], 4\n\ts_cbranch_scc1 .Lnocpf\n\t"
        "s_getpc_b64 %[pc]\n.Lcode_here:\n\tv_xor_b32 %[c], %[hi], %[c]\n\tv_lshlrev_b32 %[c], 6, %[c]\n\t"
        "s_mov_b32 %[len], .Lcode_last-.Lcode_here-4\n\ts_max_i32 %[len], %[len], .Lcode_lastb-.Lcode_here+1020\n\t"
        "v_min_u32 %[c], %[len], %[c]\n\tglobal_load_dword %[c], %[c], %[pc]\n.Lnocpf:"
        : [c] "+v"(cpf), [pc] "=&s"(pc), [len] "=&s"(len) : [w] "s"(w), [hi] "s"((b & 8) << 5) : "scc", "memory");
  }
  SCHED_FENCE();
  if (w >= 4)
    body<false>(lds, h, sp, w, x, kc, vc, wq, wo, w1, w3, w2, gw, out, cpf);
  else
    body<true>(lds, h, sp, w, x, kc, vc, wq, wo, w1, w3, w2, gw, out, cpf);
}

}

extern "C" void kernel_launch(void* const* d_in, const int* in_sizes, int n_in, void* d_out, int out_size, void* d_ws,
                              size_t ws_size, hipStream_t stream) {
  const bf16* x = (const bf16*)d_in[0];
  const bf16* kc = (const bf16*)d_in[1];
  const bf16* vc = (const bf16*)d_in[2];
  const bf16* wq = (const bf16*)d_in[3];
  const bf16* wo = (const bf16*)d_in[4];
  const bf16* w1 = (const bf16*)d_in[6];
  const bf16* w3 = (const bf16*)d_in[7];
  const bf16* w2 = (const bf16*)d_in[8];
  u64* gw = (u64*)d_ws;
  fused_decode<<<NWG, NT, 0, stream>>>(wq, kc, w1, w3, gw, x, vc, wo, w2, (bf16*)d_out);
}
```
